```python
import torch

def _softmax(x, axis=-1):
    return torch.softmax(x, dim=axis)
def _gelu(x):
    return torch.nn.functional.gelu(x, approximate="tanh")
def _silu(x):
    return torch.nn.functional.silu(x)
def _sigmoid(x):
    return torch.sigmoid(x)
def _layernorm(x, axis=-1, eps=1e-5):
    m = x.mean(dim=axis, keepdim=True)
    v = x.var(dim=axis, keepdim=True, unbiased=False)
    return (x - m) / torch.sqrt(v + eps)
def _scatter_add(tgt, idx, src, axis=0):
    return tgt.clone().index_add_(axis, idx, src)
def _segment_sum(src, seg, n):
    out = torch.zeros((n,) + src.shape[1:], dtype=src.dtype)
    return out.index_add_(0, seg, src)
def _pad(x, pw):
    flat = tuple(v for pair in reversed(pw) for v in pair)
    return torch.nn.functional.pad(x, flat)

kernel_name = 'concat_heads__grouped_kv_mqa__ssm_conv_s4d__prompt'

def setup_inputs():

    _w = (lambda a: (lambda t: t.to(torch.bfloat16) if t.is_floating_point() else t)(torch.as_tensor(a)))
    def _A_setup():
        B, S, D, H = 1, 2048, 448, 16
        Dh = D // H
        return {
            "x": _w((lambda *s: torch.randn(*s, dtype=torch.bfloat16) * 0.05)(B, S, D)),
            "wq": _w((lambda *s: torch.randn(*s, dtype=torch.bfloat16) * 1.6)(D, H * Dh)),
            "wk": _w((lambda *s: torch.randn(*s, dtype=torch.bfloat16) * 1.6)(D, Dh)),
            "wv": _w((lambda *s: torch.randn(*s, dtype=torch.bfloat16) * 0.05)(D, Dh)),
            "wo": _w((lambda *s: torch.randn(*s, dtype=torch.bfloat16) * 0.05)(H * Dh, D)),
        }
    _A_in = _A_setup()

    def _B_setup():
        Bsz, L, D, N = 1, 2048, 448, 256
        return {
            "u": _w((lambda *s: torch.randn(*s, dtype=torch.bfloat16) * 0.05)(Bsz, L, D)),
            "log_A_real": _w((lambda *s: torch.randn(*s, dtype=torch.bfloat16) * 3.2)(D, N) * 0.5),
            "A_imag": _w((lambda *s: torch.randn(*s, dtype=torch.bfloat16) * 0.05)(D, N) * 3.14),
            "C_real": _w((lambda *s: torch.randn(*s, dtype=torch.bfloat16) * 0.8)(D, N) / N ** 0.5),
            "C_imag": _w((lambda *s: torch.randn(*s, dtype=torch.bfloat16) * 0.8)(D, N) / N ** 0.5),
            "log_dt": _w((lambda *s: torch.randn(*s, dtype=torch.bfloat16) * 0.05)(D) * 0.5 - 4.0),
            "D_skip": _w((lambda *s: torch.randn(*s, dtype=torch.bfloat16) * 0.1)(D)),
        }
    _B_in = _B_setup()

    return {
        "A_x": _A_in["x"],
        "A_wq": _A_in["wq"],
        "A_wk": _A_in["wk"],
        "A_wv": _A_in["wv"],
        "A_wo": _A_in["wo"],
        "B_u": _B_in["u"],
        "B_log_A_real": _B_in["log_A_real"],
        "B_A_imag": _B_in["A_imag"],
        "B_C_real": _B_in["C_real"],
        "B_C_imag": _B_in["C_imag"],
        "B_log_dt": _B_in["log_dt"],
        "B_D_skip": _B_in["D_skip"],
    }

def reference(A_x, A_wq, A_wk, A_wv, A_wo, B_u, B_log_A_real, B_A_imag, B_C_real, B_C_imag, B_log_dt, B_D_skip):
    def _A_ref(x, wq, wk, wv, wo):
        B, S, D = x.shape
        H = 16
        Dh = D // H
        q = (lambda x, *d: x.permute(*d))((x @ wq).reshape(B, S, H, Dh), 0, 2, 1, 3)
        k = x @ wk
        v = x @ wv
        scores = torch.einsum("bhqd,bkd->bhqk", q, k) * (Dh ** -0.5)
        p = _softmax(scores, axis=-1)
        o = torch.einsum("bhqk,bkd->bhqd", p, v)
        return (lambda x, *d: x.permute(*d))(o, 0, 2, 1, 3).reshape(B, S, H * Dh) @ wo

    def _B_ref(u, log_A_real, A_imag, C_real, C_imag, log_dt, D_skip):
        L = u.shape[1]
        dt = torch.exp(log_dt)
        A = -torch.exp(log_A_real) + 1j * A_imag
        C = (C_real + 1j * C_imag) * dt[:, None]
        pos = (lambda x, dt: x.to(dt))(torch.arange(L), log_dt.dtype)
        P = torch.exp((A * dt[:, None])[:, :, None] * pos[None, None, :])
        K = 2.0 * torch.einsum("dn,dnl->dl", C, P).real
        L2 = 2 * L
        u_t = (lambda x, *d: x.permute(*d))(u, 0, 2, 1)
        Kf = (lambda x, *a, **k: torch.fft.rfft(x.float(), *a, **k))(K, L2)
        Uf = (lambda x, *a, **k: torch.fft.rfft(x.float(), *a, **k))(u_t, L2)
        y = (lambda x, *a, **k: torch.fft.irfft(x, *a, **k).to(torch.bfloat16))(Uf * Kf[None, :, :], L2)[:, :, :L]
        y = (lambda x, *d: x.permute(*d))(y, 0, 2, 1)
        return y + u * D_skip[None, None, :]

    a = _A_ref(A_x, A_wq, A_wk, A_wv, A_wo)
    b = _B_ref(B_u, B_log_A_real, B_A_imag, B_C_real, B_C_imag, B_log_dt, B_D_skip)
    return torch.concatenate([a, b], axis=-1)
```

```cpp
#include <hip/hip_runtime.h>
#include <hip/hip_bf16.h>
#include <stdint.h>

typedef unsigned long long u64;
typedef __attribute__((ext_vector_type(8))) short s16x8;
typedef __attribute__((ext_vector_type(4))) short s16x4;
typedef __attribute__((ext_vector_type(8))) __bf16 bf16x8;
typedef __attribute__((ext_vector_type(2))) __bf16 bf16x2;
typedef __attribute__((ext_vector_type(16))) __bf16 bf16x16;
typedef __attribute__((ext_vector_type(16))) float f32x16;
typedef __attribute__((ext_vector_type(8))) float f32x8;
typedef __attribute__((ext_vector_type(4))) float f32x4;
typedef __attribute__((ext_vector_type(2))) float f32x2;
typedef __attribute__((ext_vector_type(4))) unsigned u32x4;
typedef __attribute__((ext_vector_type(2))) unsigned u32x2;
typedef __amdgpu_buffer_rsrc_t rsrc_t;
#define LOADER_DELAY 8
#define GATHER_DELAY 8
#define LAS __attribute__((address_space(3)))
#define GAS __attribute__((address_space(1)))
#define DI __device__ __forceinline__
#define MEMFENCE() asm volatile("" ::: "memory")
#define VMWAIT(n) asm volatile("s_waitcnt vmcnt(" #n ")" ::: "memory")

constexpr unsigned WS_EPOCH = 0, WS_FLAG_U = 0x8000, WS_FLAG_KV = 0x10000, WS_FLAG_Y = 0x18000, FLAG_STRIDE = 128;
constexpr unsigned WS_KV = 0x20000, WS_WO = 0x60000, WS_O = 0x100000, WS_UT = 0x300000, WS_YT = 0x500000, WS_END = 0x700000;
constexpr unsigned WS_STAMP = 0x800000;
constexpr unsigned L_X = 0, L_WQ = 57344, L_WKV = L_WQ + 4 * 7168, L_U = 111104, L_G = 120320, L_T = 126528, L_SY = 161344;
constexpr unsigned U_CH = 4608, G_CH = 3104, T_WAVE = 8704, X_CH = 17408;
constexpr unsigned L_QF = 0, L_PART = 8192, L_YS = 139264, L_OG = 8192;
enum { SY_XL = 0, SY_WL, SY_KVDONE, SY_C0, SY_Q, SY_G0, SY_G1, SY_X, SY_UU, SY_CV, SY_YP, SY_YD, SY_N };
constexpr int SPIN_CAP = 1 << 22;

DI rsrc_t mkrs(const void* p, unsigned bytes) { return __builtin_amdgcn_make_buffer_rsrc((void*)p, 0, bytes, 0x00020000); }
DI void dma16(rsrc_t rs, unsigned voff, unsigned soff, unsigned lds) {
  asm volatile("s_mov_b32 m0, %3\n\ts_nop 0\n\tbuffer_load_dwordx4 %0, %1, %2 offen lds" :: "v"(voff), "s"(rs), "s"(soff), "s"(lds) : "memory");
}
DI void dma16c(rsrc_t rs, unsigned voff, unsigned soff, unsigned lds) {
  asm volatile("s_mov_b32 m0, %3\n\ts_nop 0\n\tbuffer_load_dwordx4 %0, %1, %2 offen sc1 lds" :: "v"(voff), "s"(rs), "s"(soff), "s"(lds) : "memory");
}
DI void st16c(u32x4 v, rsrc_t rs, unsigned voff, unsigned soff) {
  asm volatile("buffer_store_dwordx4 %0, %1, %2, %3 offen sc1\n\ts_nop 1" :: "v"(v), "v"(voff), "s"(rs), "s"(soff) : "memory");
}
DI void st16(u32x4 v, rsrc_t rs, unsigned voff, unsigned soff) {
  asm volatile("buffer_store_dwordx4 %0, %1, %2, %3 offen\n\ts_nop 1" :: "v"(v), "v"(voff), "s"(rs), "s"(soff) : "memory");
}
template <class T> DI T ldsr(unsigned a) { return *(const LAS T*)(uintptr_t)a; }
template <class T> DI void ldsw(unsigned a, T v) { *(LAS T*)(uintptr_t)a = v; }
DI s16x4 ldstr(unsigned a) { return __builtin_amdgcn_ds_read_tr16_b64_v4i16((LAS s16x4*)(uintptr_t)a); }
DI bf16x8 cat8(s16x4 a, s16x4 b) { return __builtin_bit_cast(bf16x8, __builtin_shufflevector(a, b, 0, 1, 2, 3, 4, 5, 6, 7)); }
DI unsigned pk2(float lo, float hi) { f32x2 v = {lo, hi}; return __builtin_bit_cast(unsigned, __builtin_convertvector(v, bf16x2)); }
DI float bf_lo(unsigned v) { return __uint_as_float(v << 16); }
DI float bf_hi(unsigned v) { return __uint_as_float(v & 0xffff0000u); }
DI float rbf(float v) { return bf_lo(pk2(v, v)); }
DI void sy_add(unsigned sy, int idx, int lane) {
  MEMFENCE();
  if (lane == 0) __hip_atomic_fetch_add((LAS unsigned*)(uintptr_t)(sy + 4 * idx), 1u, __ATOMIC_RELAXED, __HIP_MEMORY_SCOPE_WORKGROUP);
  MEMFENCE();
}
DI void sy_wait(unsigned sy, int idx, unsigned n) {
  MEMFENCE();
#pragma nounroll
  for (int s = 0; s < SPIN_CAP; ++s) {
    unsigned v = __hip_atomic_load((LAS unsigned*)(uintptr_t)(sy + 4 * idx), __ATOMIC_RELAXED, __HIP_MEMORY_SCOPE_WORKGROUP);
    if ((unsigned)__builtin_amdgcn_readfirstlane(v) >= n) break;
    __builtin_amdgcn_s_sleep(1);
  }
  MEMFENCE();
}
DI void flag_wait(const char* p, u64 want) {
  MEMFENCE();
#pragma nounroll
  for (int s = 0; s < SPIN_CAP; ++s) {
    u64 f = __hip_atomic_load((const GAS u64*)p, __ATOMIC_RELAXED, __HIP_MEMORY_SCOPE_AGENT);
    if (__all(f == want)) break;
    __builtin_amdgcn_s_sleep(4);
  }
  MEMFENCE();
}
DI void flag_set(char* p, u64 v, int lane) {
  if (lane == 0) __hip_atomic_store((GAS u64*)p, v, __ATOMIC_RELAXED, __HIP_MEMORY_SCOPE_AGENT);
  MEMFENCE();
}
DI f32x2 cmul(f32x2 a, f32x2 w) {
  f32x2 t = __builtin_shufflevector(a, a, 0, 0) * w;
  f32x2 wr = {-w.y, w.x};
  return __builtin_elementwise_fma(__builtin_shufflevector(a, a, 1, 1), wr, t);
}
DI f32x2 cexp2(float a2, float rv) {
  float mag = __builtin_amdgcn_exp2f(a2), fr = __builtin_amdgcn_fractf(rv);
  f32x2 r = {mag * __builtin_amdgcn_cosf(fr), mag * __builtin_amdgcn_sinf(fr)};
  return r;
}
#define MFMA32(a, b, c) __builtin_amdgcn_mfma_f32_32x32x16_bf16(a, b, c, 0, 0, 0)
#define MFMA16(a, b, c) __builtin_amdgcn_mfma_f32_16x16x32_bf16(a, b, c, 0, 0, 0)

#define STAMP(i)

__global__ void __launch_bounds__(512, 2)
fused_mqa_s4d(const char* __restrict__ px, const char* __restrict__ pwq, const char* __restrict__ pwk, const char* __restrict__ pwv,
              const char* __restrict__ pwo, const char* __restrict__ pu, const unsigned* __restrict__ plogA, const unsigned* __restrict__ pAim,
              const unsigned* __restrict__ pCre, const unsigned* __restrict__ pCim, const unsigned* __restrict__ plogdt,
              const unsigned* __restrict__ pD, char* out, char* ws) {
  __shared__ __attribute__((aligned(16))) char smem[163840];
  const unsigned L = (unsigned)(uintptr_t)smem, SY = L + L_SY;
  const int tid = threadIdx.x, lane = tid & 63, w = __builtin_amdgcn_readfirstlane(tid >> 6);
  const int b = blockIdx.x, g = (b & 7) * 4 + (b >> 6), m = (b >> 3) & 7, id = g * 8 + m;
  const int rot = ((b >> 6) * 2 + (m & 1)) % 7;
  const u64 epoch = __hip_atomic_load((const GAS u64*)(ws + WS_EPOCH), __ATOMIC_RELAXED, __HIP_MEMORY_SCOPE_AGENT) + 1;
  const rsrc_t rws = mkrs(ws, WS_END);
  const int c16 = lane & 15, gq = lane >> 4, r32 = lane & 31, hh = lane >> 5;
  STAMP(0);
  if (tid < 16) ldsw<unsigned>(SY + 4 * tid, 0u);
  if (tid < 388) ldsw<u32x4>(L + L_G + 16 * tid, (u32x4){0, 0, 0, 0});
  asm volatile("s_waitcnt lgkmcnt(0)\n\ts_barrier" ::: "memory");

  if (w < 4) {
    const rsrc_t rx = mkrs(px, 2048 * 896);
    const unsigned xv = (lane >> 3) * 896 + (((lane & 7) ^ gq) << 4);
    const int qq = m & 3;
    if (w == 0) {
      if (m < 7) {
        const rsrc_t ru = mkrs(pu, 2048 * 896);
        const unsigned v0 = (lane >> 3) * 896 + (((lane & 7) ^ hh) << 4);
#pragma unroll
        for (int p = 0; p < 8; ++p) dma16(ru, v0 ^ (((2 * p) & 7) << 4), (64 * g + 8 * p) * 896 + 128 * m, L + L_U + 1024 * p);
        VMWAIT(0);
        STAMP(1);
        const unsigned a0 = L + L_U + c16 * 512 + ((c16 & 7) << 4) + gq * 4;
#pragma unroll
        for (int t = 0; t < 8; ++t) {
          const unsigned a = a0 ^ (t << 4);
          u32x4 v = {ldsr<unsigned>(a), ldsr<unsigned>(a + 128), ldsr<unsigned>(a + 256), ldsr<unsigned>(a + 384)};
          st16c(v, rws, gq * 8192 + c16 * 16, WS_UT + (32 * m + 4 * t) * 8192 + 256 * g);
        }
        VMWAIT(0);
        flag_set(ws + WS_FLAG_U + id * FLAG_STRIDE, epoch, lane);
        STAMP(2);
      }
    } else if (w == 1) {
#pragma unroll
      for (int t = 0; t < 8; ++t)
        st16c((u32x4){~0u, ~0u, ~0u, ~0u}, rws, lane * 16,
            WS_O + (2 * g + (t >> 2)) * 32768 + m * 4096 + (t & 3) * 1024);
      const rsrc_t rw = mkrs(m < 4 ? pwk : pwv, 448 * 56);
#pragma nounroll
      for (int t = 0; t < 25; ++t) if (t < 24 || lane < 32) dma16(rw, lane * 16, t * 1024, L + L_WKV + t * 1024);
#pragma nounroll
      for (int i = 0; i < 7; ++i) {
        int kc = i + rot; kc = kc >= 7 ? kc - 7 : kc;
        dma16(rx, xv, (64 * g + 16 * qq) * 896 + kc * 128, L + L_X + i * 8192 + qq * 2048);
        dma16(rx, xv ^ 64, (64 * g + 16 * qq + 8) * 896 + kc * 128, L + L_X + i * 8192 + qq * 2048 + 1024);
      }
      const int pos = ((c16 >> 2) & 1) * 8 + (c16 >> 3) * 4 + (c16 & 3);
      const unsigned xa = L + L_X + (16 * qq + pos) * 128 + ((gq ^ (pos >> 1)) << 4);
      const unsigned wa = L + L_WKV + (8 * gq + (c16 >> 2)) * 56 + ((c16 >> 1) & 1) * 32 + (c16 & 1) * 8;
      f32x4 d0 = {0, 0, 0, 0}, d1 = {0, 0, 0, 0};
      VMWAIT(0);
      STAMP(1);
#pragma nounroll
      for (int i = 0; i < 7; ++i) {
        int kc = i + rot; kc = kc >= 7 ? kc - 7 : kc;
#pragma unroll
        for (int hf = 0; hf < 2; ++hf) {
          const bf16x8 xf = ldsr<bf16x8>((xa ^ (hf << 6)) + i * 8192);
          const unsigned a = wa + (kc * 64 + hf * 32) * 56;
          const bf16x8 w0 = cat8(ldstr(a), ldstr(a + 224)), w1 = cat8(ldstr(a + 16), ldstr(a + 240));
          if (m < 4) { d0 = MFMA16(w0, xf, d0); d1 = MFMA16(w1, xf, d1); }
          else       { d0 = MFMA16(xf, w0, d0); d1 = MFMA16(xf, w1, d1); }
        }
      }
      u32x4 v = {pk2(d0[0], d0[1]), pk2(d0[2], d0[3]), pk2(d1[0], d1[1]), pk2(d1[2], d1[3])};
      unsigned off;
      if (m < 4) {
        if (gq == 3) { v[2] = 0; v[3] = 0; }
        off = gq * 512 + (16 * (qq & 1) + pos) * 16;
      } else {
        auto s0 = __builtin_amdgcn_permlane32_swap(v[0], v[2], false, false);
        auto s1 = __builtin_amdgcn_permlane32_swap(v[1], v[3], false, false);
        v = (u32x4){s0[0], s1[0], s0[1], s1[1]};
        const int d = (c16 >> 3) * 16 + hh * 8 + (c16 & 7);
        int kap = (d * 37) >> 8, jj = d - 7 * kap;
        if (d >= 28) { kap = d - 28; jj = 7; v = (u32x4){0x3f803f80u, 0x3f803f80u, 0x3f803f80u, 0x3f803f80u}; }
        const int rho = (kap >> 1) * 16 + (jj >> 2) * 8 + (kap & 1) * 4 + (jj & 3);
        off = 2048 + (qq & 1) * 1024 + (rho + 32 * (gq & 1)) * 16;
      }
      st16c(v, rws, off, WS_KV + (2 * g + (qq >> 1)) * 4096);
      sy_add(SY, SY_KVDONE, lane);
      VMWAIT(0);
      flag_set(ws + WS_FLAG_KV + id * FLAG_STRIDE, epoch, lane);
      STAMP(2);
    } else if (w == 2) {
      __builtin_amdgcn_s_sleep(LOADER_DELAY);
#pragma nounroll
      for (int i = 0; i < 7; ++i) {
        int kc = i + rot; kc = kc >= 7 ? kc - 7 : kc;
#pragma unroll
        for (int p = 1; p < 4; ++p) {
          const int q2 = (qq + p) & 3;
          dma16(rx, xv, (64 * g + 16 * q2) * 896 + kc * 128, L + L_X + i * 8192 + q2 * 2048);
          dma16(rx, xv ^ 64, (64 * g + 16 * q2 + 8) * 896 + kc * 128, L + L_X + i * 8192 + q2 * 2048 + 1024);
        }
      }
      VMWAIT(0);
      sy_add(SY, SY_XL, lane);
      STAMP(1);
    } else {
      const rsrc_t rq = mkrs(pwq, 448 * 896);
      unsigned vo[7];
#pragma unroll
      for (int t = 0; t < 7; ++t) {
        const int ci = 64 * t + lane, n = (ci * 9363) >> 16, pc = ci - 7 * n;
        vo[t] = ((n & 48) + 4 * (n & 3) + ((n >> 2) & 3)) * 896 + pc * 16;
      }
      __builtin_amdgcn_s_sleep(LOADER_DELAY);
#pragma nounroll
      for (int i = 0; i < 7; ++i) {
        int kc = i + rot; kc = kc >= 7 ? kc - 7 : kc;
        if (i == 4) sy_wait(SY, SY_KVDONE, 1);
#pragma unroll
        for (int t = 0; t < 7; ++t) dma16(rq, vo[t], kc * 64 * 896 + m * 112, L + L_WQ + i * 7168 + t * 1024);
      }
      VMWAIT(0);
      sy_add(SY, SY_XL, lane);
      STAMP(1);
    }
    {
      const int row = 32 * (w & 1) + r32;
      const unsigned xa = L + L_X + row * 128 + ((hh ^ ((row >> 1) & 7)) << 4);
      const unsigned wa = L + L_WQ + (4 * ((lane >> 2) & 3) + 2 * hh) * 112 + (w >> 1) * 56 + (gq & 1) * 32 + (lane & 3) * 8;
      f32x16 acc = {};
      sy_wait(SY, SY_XL, 2);
      STAMP(3);
#pragma nounroll
      for (int i = 0; i < 7; ++i) {
#pragma unroll
        for (int ks = 0; ks < 4; ++ks) {
          const bf16x8 xf = ldsr<bf16x8>((xa ^ (ks << 5)) + i * 8192);
          const unsigned a = wa + i * 7168 + ks * 16 * 112;
          acc = MFMA32(cat8(ldstr(a), ldstr(a + 112)), xf, acc);
        }
        if (i == 0) sy_add(SY, SY_C0, lane);
      }
      const float qs = 0.1889822365046136f * 1.4426950408889634f;
      u32x4 q0, q1;
#pragma unroll
      for (int i = 0; i < 4; ++i) { q0[i] = pk2(acc[2 * i] * qs, acc[2 * i + 1] * qs); q1[i] = pk2(acc[8 + 2 * i] * qs, acc[9 + 2 * i] * qs); }
      if (hh) { q1[2] = 0; q1[3] = 0; }
      sy_wait(SY, SY_C0, 4);
      ldsw<u32x4>(L + L_QF + w * 2048 + lane * 16, q0);
      ldsw<u32x4>(L + L_QF + w * 2048 + 1024 + lane * 16, q1);
      sy_add(SY, SY_Q, lane);
      STAMP(4);
    }
  } else if (m < 7) {
    const int v = w - 4, ch = v >> 1, hv = v & 1, pair = 32 * m + g;
    const int pi = (2 * pair + ch) * 128 + hv * 64 + lane;
    const unsigned vA = plogA[pi], vI = pAim[pi], vCr = pCre[pi], vCi = pCim[pi];
    const unsigned sdt = plogdt[pair], sD = pD[pair];
    const unsigned G = L + L_G + ch * G_CH + 32, T = L + L_T + v * T_WAVE, X = L + L_T + ch * X_CH;
    unsigned ga[15];
    {
      const int e8 = 8 * (64 * hv + lane);
      const float f0 = (float)(e8 > 136 ? e8 : 136), f1 = (float)(e8 + 1024);
      const unsigned b0 = G + 4 * (128 - 0x4300 - (e8 < 136 ? 136 - e8 : 0)), b1 = G + 4 * (128 - 0x4300);
#pragma unroll
      for (int k = 0; k < 11; ++k) ga[k] = b0 + 4 * (pk2(f0 + (float)(k - 3), 0.f) & 0xffffu);
      ga[11] = b1 + 4 * (pk2(f1 - 3.f, 0.f) & 0xffffu); ga[12] = b1 + 4 * (pk2(f1, 0.f) & 0xffffu);
      ga[13] = b1 + 4 * (pk2(f1 + 4.f, 0.f) & 0xffffu); ga[14] = b1 + 4 * (pk2(f1 + 5.f, 0.f) & 0xffffu);
    }
    const float L2E = 1.4426950408889634f;
    const float dt = rbf(__builtin_amdgcn_exp2f((ch ? bf_hi(sdt) : bf_lo(sdt)) * L2E));
    f32x16 acc = {};
    const unsigned tw = T + lane * 4, tr = T + r32 * 272 + hh * 16;
    STAMP(1);
#pragma nounroll
    for (int rd = 0; rd < 2; ++rd) {
      const int sh = 16 - 16 * rd;
      const float la = bf_hi(vA << sh), ai = bf_hi(vI << sh), cr = bf_hi(vCr << sh), ci = bf_hi(vCi << sh);
      const float a2 = -rbf(__builtin_amdgcn_exp2f(la * L2E)) * dt * L2E, rv = ai * dt * 0.15915494309189535f;
      const f32x2 w1 = cexp2(a2, -rv), w8 = cexp2(8.f * a2, 8.f * rv), wb = cexp2(256.f * a2, -256.f * rv);
#define PUT(row, z) ldsw<unsigned>(tw + (row) * 272, pk2((z).x, (z).y))
      f32x2 c[8];
      c[0] = (f32x2){2.f * dt * cr, -2.f * dt * ci};
      const f32x2 w2 = cmul(w1, w1);
      c[1] = cmul(c[0], w1);
#pragma unroll
      for (int k = 2; k < 8; ++k) c[k] = cmul(c[k - 2], w2);
#pragma unroll
      for (int k = 0; k < 8; ++k) PUT(k, c[k]);
      f32x2 p0 = cmul(c[0], wb), p4 = cmul(c[4], wb);
      PUT(8, p0); PUT(9, cmul(c[2], wb)); PUT(10, p4); PUT(11, cmul(c[6], wb));
      p0 = cmul(p0, wb); p4 = cmul(p4, wb); PUT(12, p0); PUT(13, p4);
      p0 = cmul(p0, wb); p4 = cmul(p4, wb); PUT(14, p0); PUT(15, p4);
#pragma unroll
      for (int k = 16; k < 21; ++k) { p0 = cmul(p0, wb); PUT(k, p0); }
      MEMFENCE();
      bf16x8 af[8];
#pragma unroll
      for (int ks = 0; ks < 8; ++ks) af[ks] = ldsr<bf16x8>(tr + ks * 32);
      MEMFENCE();
      f32x2 e[4];
      e[0] = (f32x2){1.f, 0.f}; e[1] = w8; e[2] = cmul(w8, w8); e[3] = cmul(e[2], w8);
      const f32x2 w32 = cmul(e[2], e[2]);
#pragma unroll
      for (int k = 0; k < 8; ++k) {
#pragma unroll
        for (int i = 0; i < 4; ++i) PUT(4 * k + i, e[i]);
        if (k < 7) {
          e[0] = k == 0 ? w32 : cmul(e[0], w32);
#pragma unroll
          for (int i = 1; i < 4; ++i) e[i] = cmul(e[i], w32);
        }
      }
      MEMFENCE();
#pragma unroll
      for (int ks = 0; ks < 8; ++ks) acc = MFMA32(af[ks], ldsr<bf16x8>(tr + ks * 32), acc);
      MEMFENCE();
    }
    if (hv == 0 && lane == 0) acc[0] += ch ? bf_hi(sD) : bf_lo(sD);
    {
      const unsigned g0 = G + 4 * (8 * r32 + 4 * hh), g1 = G + 4 * (hh ? 384 + 2 * r32 : 256 + 4 * r32), g2 = g1 + (hh ? 256 : 8);
      const unsigned g3 = G + 4 * (512 + 128 * hh + r32);
#define GADD(a, x) __hip_atomic_fetch_add((LAS float*)(uintptr_t)(a), x, __ATOMIC_RELAXED, __HIP_MEMORY_SCOPE_WORKGROUP)
#pragma unroll
      for (int i = 0; i < 4; ++i) { GADD(g0 + 4 * i, acc[i]); GADD(g3 + 128 * i, acc[8 + i]); }
      GADD(g1, acc[4]); GADD(g1 + 4, acc[5]); GADD(g2, acc[6]); GADD(g2 + 4, acc[7]);
    }
    sy_add(SY, SY_G0 + ch, lane);
    sy_wait(SY, SY_G0 + ch, 2);
    STAMP(2);
    {
      float kv[15];
#pragma unroll
      for (int k = 0; k < 15; ++k) kv[k] = ldsr<float>(ga[k]);
      const int e = 64 * hv + lane + 2;
      const unsigned xa = X + e * 64 + (((e >> 1) & 3) << 4);
      unsigned P[10];
#pragma unroll
      for (int k = 0; k < 10; ++k) P[k] = pk2(kv[k + 1], kv[k]);
#pragma unroll
      for (int k = 0; k < 4; ++k) ldsw<u32x4>(xa ^ (k << 4), (u32x4){P[2 * k + 2], P[2 * k], P[2 * k + 3], P[2 * k + 1]});
      const unsigned Pa = pk2(kv[12], kv[11]), Pb = pk2(kv[12], kv[12]), Pc = pk2(kv[13], kv[12]), Pd = pk2(kv[14], kv[13]), Pe = pk2(kv[14], kv[14]);
      ldsw<u32x4>((xa ^ 0) + 8192, (u32x4){Pb, Pa, Pb, Pb});
      ldsw<u32x4>((xa ^ 16) + 8192, (u32x4){Pb, Pb, Pb, Pb});
      ldsw<u32x4>((xa ^ 32) + 8192, (u32x4){Pc, Pb, Pd, Pb});
      ldsw<u32x4>((xa ^ 48) + 8192, (u32x4){Pe, Pc, Pe, Pd});
      if (hv == 0 && lane < 8) ldsw<u32x4>(X + lane * 16, (u32x4){0, 0, 0, 0});
    }
    STAMP(3);
    flag_wait(ws + WS_FLAG_U + (r32 * 8 + m) * FLAG_STRIDE, epoch);
    STAMP(4);
    {
      const u32x4 a = __builtin_amdgcn_raw_buffer_load_b128(rws, lane * 16, WS_UT + pair * 8192 + v * 2048, 16);
      const u32x4 c = __builtin_amdgcn_raw_buffer_load_b128(rws, lane * 16, WS_UT + pair * 8192 + v * 2048 + 1024, 16);
      const unsigned ua = L + L_U + 512 + v * 1024 + lane * 8;
      ldsw<u32x2>(ua, (u32x2){__builtin_amdgcn_perm(a[1], a[0], 0x05040100u), __builtin_amdgcn_perm(a[3], a[2], 0x05040100u)});
      ldsw<u32x2>(ua + 512, (u32x2){__builtin_amdgcn_perm(c[1], c[0], 0x05040100u), __builtin_amdgcn_perm(c[3], c[2], 0x05040100u)});
      ldsw<u32x2>(ua + U_CH, (u32x2){__builtin_amdgcn_perm(a[1], a[0], 0x07060302u), __builtin_amdgcn_perm(a[3], a[2], 0x07060302u)});
      ldsw<u32x2>(ua + U_CH + 512, (u32x2){__builtin_amdgcn_perm(c[1], c[0], 0x07060302u), __builtin_amdgcn_perm(c[3], c[2], 0x07060302u)});
      if (hv == 0) ldsw<u32x2>(L + L_U + ch * U_CH + lane * 8, (u32x2){0, 0});
    }
    sy_add(SY, SY_UU, lane);
    sy_wait(SY, SY_UU, 4);
    STAMP(5);
    f32x4 y[8];
#pragma unroll
    for (int t = 0; t < 8; ++t) y[t] = (f32x4){0, 0, 0, 0};
    {
      const int pa = 32 * hv + 32 + c16 - 8 * gq, pb = pa - 4;
      unsigned xa = X + (pa >> 3) * 64 + ((((pa >> 1) ^ (pa >> 4)) & 3) << 4) + (pa & 1) * 8;
      unsigned xb = X + (pb >> 3) * 64 + ((((pb >> 1) ^ (pb >> 4)) & 3) << 4) + (pb & 1) * 8;
      unsigned ub = L + L_U + ch * U_CH + 512 - 32 - 64 * hv + 32 * c16 + 16 * gq;
#pragma nounroll
      for (int rr = 0; rr < 4; ++rr) {
        bf16x8 bf[8];
#pragma unroll
        for (int t = 0; t < 8; ++t) bf[t] = ldsr<bf16x8>(ub + 512 * t);
#pragma unroll
        for (int a = 0; a < 8; ++a) {
          const bf16x8 af = cat8(ldsr<s16x4>(xa + 2048 * a), ldsr<s16x4>(xb + 2048 * a));
#pragma unroll
          for (int t = 0; t + a < 8; ++t) y[t + a] = MFMA16(af, bf[t], y[t + a]);
        }
        xa += 512; xb += 512; ub -= 128;
      }
    }
    sy_add(SY, SY_CV, lane);
    sy_wait(SY, SY_CV, 4);
    STAMP(6);
    {
      const unsigned slot = (4 * c16 + gq) * 16;
#pragma unroll
      for (int t = 0; t < 8; ++t) ldsw<f32x4>(L + L_T + v * 8192 + t * 1024 + slot, y[t]);
      sy_add(SY, SY_YP, lane);
      sy_wait(SY, SY_YP, 4);
#pragma unroll
      for (int t = 0; t < 2; ++t) {
        const unsigned a = L + L_T + (2 * v + t) * 1024 + lane * 16;
        const f32x4 y0 = ldsr<f32x4>(a) + ldsr<f32x4>(a + 8192), y1 = ldsr<f32x4>(a + 16384) + ldsr<f32x4>(a + 24576);
        const u32x4 o = {pk2(y0[0], y1[0]), pk2(y0[1], y1[1]), pk2(y0[2], y1[2]), pk2(y0[3], y1[3])};
        st16c(o, rws, lane * 16, WS_YT + pair * 8192 + (2 * v + t) * 1024);
      }
    }
  } else {
    const int v = w - 4, hd = g >> 1, cg = 2 * (g & 1) + (v >> 1);
    const rsrc_t ro = mkrs(pwo, 448 * 896);
    const unsigned T = L + L_T + v * T_WAVE;
#pragma unroll
    for (int t = 0; t < 4; ++t) dma16(ro, (lane >> 3) * 896 + (lane & 7) * 16, (28 * hd + 8 * t) * 896 + cg * 224 + (v & 1) * 128, T + t * 1024);
    VMWAIT(0);
#pragma unroll
    for (int ww = 0; ww < 2; ++ww) {
      unsigned e[7];
#pragma unroll
      for (int k = 0; k < 7; ++k) e[k] = ldsr<unsigned>(T + (7 * gq + k) * 128 + ww * 64 + c16 * 4);
      const u32x4 lo = {__builtin_amdgcn_perm(e[1], e[0], 0x05040100u), __builtin_amdgcn_perm(e[3], e[2], 0x05040100u), __builtin_amdgcn_perm(e[5], e[4], 0x05040100u), e[6] & 0xffffu};
      const u32x4 hi = {__builtin_amdgcn_perm(e[1], e[0], 0x07060302u), __builtin_amdgcn_perm(e[3], e[2], 0x07060302u), __builtin_amdgcn_perm(e[5], e[4], 0x07060302u), e[6] >> 16};
      const unsigned so = WS_WO + (cg * 4 + 2 * (v & 1) + ww) * 32768 + hd * 1024;
      st16c(lo, rws, lane * 16, so);
      st16c(hi, rws, lane * 16, so + 16384);
    }
  }
  if (w >= 4) {
    VMWAIT(0);
    MEMFENCE();
    unsigned old = 0;
    if (lane == 0) old = __hip_atomic_fetch_add((LAS unsigned*)(uintptr_t)(SY + 4 * SY_YD), 1u, __ATOMIC_RELAXED, __HIP_MEMORY_SCOPE_WORKGROUP);
    if (__builtin_amdgcn_readfirstlane(old) == 3) flag_set(ws + WS_FLAG_Y + id * FLAG_STRIDE, epoch, lane);
    sy_wait(SY, SY_YD, 4);
    STAMP(7);
  }

  flag_wait(ws + WS_FLAG_KV + (32 * w + r32) * FLAG_STRIDE, epoch);
  STAMP(8);
  {
    unsigned soK = WS_KV + w * 32768, soV = soK, cnt, tmp;
    asm volatile(
      "buffer_load_dwordx4 v[176:179], %[voff], %[rs], %[soK] offen offset:0 sc1\n"
      "buffer_load_dwordx4 v[180:183], %[voff], %[rs], %[soK] offen offset:1024 sc1\n"
      "s_add_u32 %[soK], %[soK], 4096\n"
      "buffer_load_dwordx4 v[208:211], %[voff], %[rs], %[soV] offen offset:2048 sc1\n"
      "buffer_load_dwordx4 v[212:215], %[voff], %[rs], %[soV] offen offset:3072 sc1\n"
      "s_add_u32 %[soV], %[soV], 4096\n"
      "buffer_load_dwordx4 v[184:187], %[voff], %[rs], %[soK] offen offset:0 sc1\n"
      "buffer_load_dwordx4 v[188:191], %[voff], %[rs], %[soK] offen offset:1024 sc1\n"
      "s_add_u32 %[soK], %[soK], 4096\n"
      "buffer_load_dwordx4 v[216:219], %[voff], %[rs], %[soV] offen offset:2048 sc1\n"
      "buffer_load_dwordx4 v[220:223], %[voff], %[rs], %[soV] offen offset:3072 sc1\n"
      "s_add_u32 %[soV], %[soV], 4096\n"
      "buffer_load_dwordx4 v[192:195], %[voff], %[rs], %[soK] offen offset:0 sc1\n"
      "buffer_load_dwordx4 v[196:199], %[voff], %[rs], %[soK] offen offset:1024 sc1\n"
      "s_add_u32 %[soK], %[soK], 4096\n"
      "buffer_load_dwordx4 v[224:227], %[voff], %[rs], %[soV] offen offset:2048 sc1\n"
      "buffer_load_dwordx4 v[228:231], %[voff], %[rs], %[soV] offen offset:3072 sc1\n"
      "s_add_u32 %[soV], %[soV], 4096\n"
      "buffer_load_dwordx4 v[200:203], %[voff], %[rs], %[soK] offen offset:0 sc1\n"
      "buffer_load_dwordx4 v[204:207], %[voff], %[rs], %[soK] offen offset:1024 sc1\n"
      "s_add_u32 %[soK], %[soK], 4096\n"
      "buffer_load_dwordx4 v[232:235], %[voff], %[rs], %[soV] offen offset:2048 sc1\n"
      "buffer_load_dwordx4 v[236:239], %[voff], %[rs], %[soV] offen offset:3072 sc1\n"
      "s_add_u32 %[soV], %[soV], 4096\n"
      "v_mov_b32 v128, 0\n"
      "v_mov_b32 v129, 0\n"
      "v_mov_b32 v130, 0\n"
      "v_mov_b32 v131, 0\n"
      "v_mov_b32 v240, 0xdddddddd\n"
      "s_nop 1\n"
      "v_mfma_f32_32x32x16_bf16 v[64:79], v[128:131], v[128:131], 0\n"
      "v_mfma_f32_32x32x16_bf16 v[80:95], v[128:131], v[128:131], 0\n"
      "v_mfma_f32_32x32x16_bf16 v[96:111], v[128:131], v[128:131], 0\n"
      "v_mfma_f32_32x32x16_bf16 v[112:127], v[128:131], v[128:131], 0\n"
      "s_mov_b32 %[cnt], 0\n"
      "1:\n"
      "ds_read_b32 v32, %[vsy]\n"
      "s_add_u32 %[cnt], %[cnt], 1\n"
      "s_waitcnt lgkmcnt(0)\n"
      "v_readfirstlane_b32 %[tmp], v32\n"
      "s_nop 3\n"
      "s_cmp_ge_u32 %[tmp], 4\n"
      "s_cbranch_scc1 2f\n"
      "s_sleep 1\n"
      "s_cmp_lt_u32 %[cnt], 0x20000\n"
      "s_cbranch_scc1 1b\n"
      "2:\n"
      "ds_read_b128 v[32:35], %[vq] offset:0\n"
      "ds_read_b128 v[36:39], %[vq] offset:1024\n"
      "ds_read_b128 v[40:43], %[vq] offset:2048\n"
      "ds_read_b128 v[44:47], %[vq] offset:3072\n"
      "ds_read_b128 v[48:51], %[vq] offset:4096\n"
      "ds_read_b128 v[52:55], %[vq] offset:5120\n"
      "ds_read_b128 v[56:59], %[vq] offset:6144\n"
      "ds_read_b128 v[60:63], %[vq] offset:7168\n"
      "s_waitcnt lgkmcnt(0)\n"
      "s_waitcnt vmcnt(15)\n"
      "v_mfma_f32_32x32x16_bf16 v[128:143], v[176:179], v[32:35], 0\n"
      "s_waitcnt vmcnt(14)\n"
      "v_mfma_f32_32x32x16_bf16 v[128:143], v[180:183], v[36:39], v[128:143]\n"
      "s_nop 10\n"
      "v_mfma_f32_32x32x16_bf16 v[144:159], v[176:179], v[40:43], 0\n"
      "v_exp_f32 v128, v128\n"
      "v_exp_f32 v129, v129\n"
      "v_exp_f32 v130, v130\n"
      "v_exp_f32 v131, v131\n"
      "v_mfma_f32_32x32x16_bf16 v[144:159], v[180:183], v[44:47], v[144:159]\n"
      "v_exp_f32 v132, v132\n"
      "v_exp_f32 v133, v133\n"
      "v_exp_f32 v134, v134\n"
      "v_exp_f32 v135, v135\n"
      "v_exp_f32 v136, v136\n"
      "v_exp_f32 v137, v137\n"
      "v_exp_f32 v138, v138\n"
      "v_exp_f32 v139, v139\n"
      "v_exp_f32 v140, v140\n"
      "v_exp_f32 v141, v141\n"
      "v_exp_f32 v142, v142\n"
      "v_exp_f32 v143, v143\n"
      "v_mfma_f32_32x32x16_bf16 v[160:175], v[176:179], v[48:51], 0\n"
      "v_exp_f32 v144, v144\n"
      "v_exp_f32 v145, v145\n"
      "v_exp_f32 v146, v146\n"
      "v_exp_f32 v147, v147\n"
      "v_mfma_f32_32x32x16_bf16 v[160:175], v[180:183], v[52:55], v[160:175]\n"
      "v_exp_f32 v148, v148\n"
      "v_exp_f32 v149, v149\n"
      "v_exp_f32 v150, v150\n"
      "v_exp_f32 v151, v151\n"
      "s_waitcnt vmcnt(13)\n"
      "v_smfmac_f32_32x32x32_bf16 v[64:79], v[208:211], v[128:135], v240\n"
      "v_exp_f32 v152, v152\n"
      "v_exp_f32 v153, v153\n"
      "v_exp_f32 v154, v154\n"
      "v_exp_f32 v155, v155\n"
      "s_waitcnt vmcnt(12)\n"
      "v_smfmac_f32_32x32x32_bf16 v[64:79], v[212:215], v[136:143], v240\n"
      "v_exp_f32 v156, v156\n"
      "v_exp_f32 v157, v157\n"
      "v_exp_f32 v158, v158\n"
      "v_exp_f32 v159, v159\n"
      "v_mfma_f32_32x32x16_bf16 v[128:143], v[176:179], v[56:59], 0\n"
      "buffer_load_dwordx4 v[176:179], %[voff], %[rs], %[soK] offen offset:0 sc1\n"
      "v_exp_f32 v160, v160\n"
      "v_exp_f32 v161, v161\n"
      "v_exp_f32 v162, v162\n"
      "v_exp_f32 v163, v163\n"
      "v_mfma_f32_32x32x16_bf16 v[128:143], v[180:183], v[60:63], v[128:143]\n"
      "buffer_load_dwordx4 v[180:183], %[voff], %[rs], %[soK] offen offset:1024 sc1\n"
      "s_add_u32 %[soK], %[soK], 4096\n"
      "v_exp_f32 v164, v164\n"
      "v_exp_f32 v165, v165\n"
      "v_exp_f32 v166, v166\n"
      "v_exp_f32 v167, v167\n"
      "v_smfmac_f32_32x32x32_bf16 v[80:95], v[208:211], v[144:151], v240\n"
      "v_exp_f32 v168, v168\n"
      "v_exp_f32 v169, v169\n"
      "v_exp_f32 v170, v170\n"
      "v_exp_f32 v171, v171\n"
      "v_smfmac_f32_32x32x32_bf16 v[80:95], v[212:215], v[152:159], v240\n"
      "v_exp_f32 v172, v172\n"
      "v_exp_f32 v173, v173\n"
      "v_exp_f32 v174, v174\n"
      "v_exp_f32 v175, v175\n"
      "s_waitcnt vmcnt(13)\n"
      "v_mfma_f32_32x32x16_bf16 v[144:159], v[184:187], v[32:35], 0\n"
      "v_exp_f32 v128, v128\n"
      "v_exp_f32 v129, v129\n"
      "v_exp_f32 v130, v130\n"
      "v_exp_f32 v131, v131\n"
      "s_waitcnt vmcnt(12)\n"
      "v_mfma_f32_32x32x16_bf16 v[144:159], v[188:191], v[36:39], v[144:159]\n"
      "v_exp_f32 v132, v132\n"
      "v_exp_f32 v133, v133\n"
      "v_exp_f32 v134, v134\n"
      "v_exp_f32 v135, v135\n"
      "v_smfmac_f32_32x32x32_bf16 v[96:111], v[208:211], v[160:167], v240\n"
      "v_exp_f32 v136, v136\n"
      "v_exp_f32 v137, v137\n"
      "v_exp_f32 v138, v138\n"
      "v_exp_f32 v139, v139\n"
      "v_smfmac_f32_32x32x32_bf16 v[96:111], v[212:215], v[168:175], v240\n"
      "v_exp_f32 v140, v140\n"
      "v_exp_f32 v141, v141\n"
      "v_exp_f32 v142, v142\n"
      "v_exp_f32 v143, v143\n"
      "v_mfma_f32_32x32x16_bf16 v[160:175], v[184:187], v[40:43], 0\n"
      "v_exp_f32 v144, v144\n"
      "v_exp_f32 v145, v145\n"
      "v_exp_f32 v146, v146\n"
      "v_exp_f32 v147, v147\n"
      "v_mfma_f32_32x32x16_bf16 v[160:175], v[188:191], v[44:47], v[160:175]\n"
      "v_exp_f32 v148, v148\n"
      "v_exp_f32 v149, v149\n"
      "v_exp_f32 v150, v150\n"
      "v_exp_f32 v151, v151\n"
      "v_smfmac_f32_32x32x32_bf16 v[112:127], v[208:211], v[128:135], v240\n"
      "buffer_load_dwordx4 v[208:211], %[voff], %[rs], %[soV] offen offset:2048 sc1\n"
      "v_exp_f32 v152, v152\n"
      "v_exp_f32 v153, v153\n"
      "v_exp_f32 v154, v154\n"
      "v_exp_f32 v155, v155\n"
      "v_smfmac_f32_32x32x32_bf16 v[112:127], v[212:215], v[136:143], v240\n"
      "buffer_load_dwordx4 v[212:215], %[voff], %[rs], %[soV] offen offset:3072 sc1\n"
      "s_add_u32 %[soV], %[soV], 4096\n"
      "v_exp_f32 v156, v156\n"
      "v_exp_f32 v157, v157\n"
      "v_exp_f32 v158, v158\n"
      "v_exp_f32 v159, v159\n"
      "v_mfma_f32_32x32x16_bf16 v[128:143], v[184:187], v[48:51], 0\n"
      "v_exp_f32 v160, v160\n"
      "v_exp_f32 v161, v161\n"
      "v_exp_f32 v162, v162\n"
      "v_exp_f32 v163, v163\n"
      "v_mfma_f32_32x32x16_bf16 v[128:143], v[188:191], v[52:55], v[128:143]\n"
      "v_exp_f32 v164, v164\n"
      "v_exp_f32 v165, v165\n"
      "v_exp_f32 v166, v166\n"
      "v_exp_f32 v167, v167\n"
      "s_waitcnt vmcnt(13)\n"
      "v_smfmac_f32_32x32x32_bf16 v[64:79], v[216:219], v[144:151], v240\n"
      "v_exp_f32 v168, v168\n"
      "v_exp_f32 v169, v169\n"
      "v_exp_f32 v170, v170\n"
      "v_exp_f32 v171, v171\n"
      "s_waitcnt vmcnt(12)\n"
      "v_smfmac_f32_32x32x32_bf16 v[64:79], v[220:223], v[152:159], v240\n"
      "v_exp_f32 v172, v172\n"
      "v_exp_f32 v173, v173\n"
      "v_exp_f32 v174, v174\n"
      "v_exp_f32 v175, v175\n"
      "v_mfma_f32_32x32x16_bf16 v[144:159], v[184:187], v[56:59], 0\n"
      "buffer_load_dwordx4 v[184:187], %[voff], %[rs], %[soK] offen offset:0 sc1\n"
      "v_exp_f32 v128, v128\n"
      "v_exp_f32 v129, v129\n"
      "v_exp_f32 v130, v130\n"
      "v_exp_f32 v131, v131\n"
      "v_mfma_f32_32x32x16_bf16 v[144:159], v[188:191], v[60:63], v[144:159]\n"
      "buffer_load_dwordx4 v[188:191], %[voff], %[rs], %[soK] offen offset:1024 sc1\n"
      "s_add_u32 %[soK], %[soK], 4096\n"
      "v_exp_f32 v132, v132\n"
      "v_exp_f32 v133, v133\n"
      "v_exp_f32 v134, v134\n"
      "v_exp_f32 v135, v135\n"
      "v_smfmac_f32_32x32x32_bf16 v[80:95], v[216:219], v[160:167], v240\n"
      "v_exp_f32 v136, v136\n"
      "v_exp_f32 v137, v137\n"
      "v_exp_f32 v138, v138\n"
      "v_exp_f32 v139, v139\n"
      "v_smfmac_f32_32x32x32_bf16 v[80:95], v[220:223], v[168:175], v240\n"
      "v_exp_f32 v140, v140\n"
      "v_exp_f32 v141, v141\n"
      "v_exp_f32 v142, v142\n"
      "v_exp_f32 v143, v143\n"
      "s_waitcnt vmcnt(13)\n"
      "v_mfma_f32_32x32x16_bf16 v[160:175], v[192:195], v[32:35], 0\n"
      "v_exp_f32 v144, v144\n"
      "v_exp_f32 v145, v145\n"
      "v_exp_f32 v146, v146\n"
      "v_exp_f32 v147, v147\n"
      "s_waitcnt vmcnt(12)\n"
      "v_mfma_f32_32x32x16_bf16 v[160:175], v[196:199], v[36:39], v[160:175]\n"
      "v_exp_f32 v148, v148\n"
      "v_exp_f32 v149, v149\n"
      "v_exp_f32 v150, v150\n"
      "v_exp_f32 v151, v151\n"
      "v_smfmac_f32_32x32x32_bf16 v[96:111], v[216:219], v[128:135], v240\n"
      "v_exp_f32 v152, v152\n"
      "v_exp_f32 v153, v153\n"
      "v_exp_f32 v154, v154\n"
      "v_exp_f32 v155, v155\n"
      "v_smfmac_f32_32x32x32_bf16 v[96:111], v[220:223], v[136:143], v240\n"
      "v_exp_f32 v156, v156\n"
      "v_exp_f32 v157, v157\n"
      "v_exp_f32 v158, v158\n"
      "v_exp_f32 v159, v159\n"
      "v_mfma_f32_32x32x16_bf16 v[128:143], v[192:195], v[40:43], 0\n"
      "v_exp_f32 v160, v160\n"
      "v_exp_f32 v161, v161\n"
      "v_exp_f32 v162, v162\n"
      "v_exp_f32 v163, v163\n"
      "v_mfma_f32_32x32x16_bf16 v[128:143], v[196:199], v[44:47], v[128:143]\n"
      "v_exp_f32 v164, v164\n"
      "v_exp_f32 v165, v165\n"
      "v_exp_f32 v166, v166\n"
      "v_exp_f32 v167, v167\n"
      "v_smfmac_f32_32x32x32_bf16 v[112:127], v[216:219], v[144:151], v240\n"
      "buffer_load_dwordx4 v[216:219], %[voff], %[rs], %[soV] offen offset:2048 sc1\n"
      "v_exp_f32 v168, v168\n"
      "v_exp_f32 v169, v169\n"
      "v_exp_f32 v170, v170\n"
      "v_exp_f32 v171, v171\n"
      "v_smfmac_f32_32x32x32_bf16 v[112:127], v[220:223], v[152:159], v240\n"
      "buffer_load_dwordx4 v[220:223], %[voff], %[rs], %[soV] offen offset:3072 sc1\n"
      "s_add_u32 %[soV], %[soV], 4096\n"
      "v_exp_f32 v172, v172\n"
      "v_exp_f32 v173, v173\n"
      "v_exp_f32 v174, v174\n"
      "v_exp_f32 v175, v175\n"
      "v_mfma_f32_32x32x16_bf16 v[144:159], v[192:195], v[48:51], 0\n"
      "v_exp_f32 v128, v128\n"
      "v_exp_f32 v129, v129\n"
      "v_exp_f32 v130, v130\n"
      "v_exp_f32 v131, v131\n"
      "v_mfma_f32_32x32x16_bf16 v[144:159], v[196:199], v[52:55], v[144:159]\n"
      "v_exp_f32 v132, v132\n"
      "v_exp_f32 v133, v133\n"
      "v_exp_f32 v134, v134\n"
      "v_exp_f32 v135, v135\n"
      "s_waitcnt vmcnt(13)\n"
      "v_smfmac_f32_32x32x32_bf16 v[64:79], v[224:227], v[160:167], v240\n"
      "v_exp_f32 v136, v136\n"
      "v_exp_f32 v137, v137\n"
      "v_exp_f32 v138, v138\n"
      "v_exp_f32 v139, v139\n"
      "s_waitcnt vmcnt(12)\n"
      "v_smfmac_f32_32x32x32_bf16 v[64:79], v[228:231], v[168:175], v240\n"
      "v_exp_f32 v140, v140\n"
      "v_exp_f32 v141, v141\n"
      "v_exp_f32 v142, v142\n"
      "v_exp_f32 v143, v143\n"
      "v_mfma_f32_32x32x16_bf16 v[160:175], v[192:195], v[56:59], 0\n"
      "buffer_load_dwordx4 v[192:195], %[voff], %[rs], %[soK] offen offset:0 sc1\n"
      "v_exp_f32 v144, v144\n"
      "v_exp_f32 v145, v145\n"
      "v_exp_f32 v146, v146\n"
      "v_exp_f32 v147, v147\n"
      "v_mfma_f32_32x32x16_bf16 v[160:175], v[196:199], v[60:63], v[160:175]\n"
      "buffer_load_dwordx4 v[196:199], %[voff], %[rs], %[soK] offen offset:1024 sc1\n"
      "s_add_u32 %[soK], %[soK], 4096\n"
      "v_exp_f32 v148, v148\n"
      "v_exp_f32 v149, v149\n"
      "v_exp_f32 v150, v150\n"
      "v_exp_f32 v151, v151\n"
      "v_smfmac_f32_32x32x32_bf16 v[80:95], v[224:227], v[128:135], v240\n"
      "v_exp_f32 v152, v152\n"
      "v_exp_f32 v153, v153\n"
      "v_exp_f32 v154, v154\n"
      "v_exp_f32 v155, v155\n"
      "v_smfmac_f32_32x32x32_bf16 v[80:95], v[228:231], v[136:143], v240\n"
      "v_exp_f32 v156, v156\n"
      "v_exp_f32 v157, v157\n"
      "v_exp_f32 v158, v158\n"
      "v_exp_f32 v159, v159\n"
      "s_waitcnt vmcnt(13)\n"
      "v_mfma_f32_32x32x16_bf16 v[128:143], v[200:203], v[32:35], 0\n"
      "v_exp_f32 v160, v160\n"
      "v_exp_f32 v161, v161\n"
      "v_exp_f32 v162, v162\n"
      "v_exp_f32 v163, v163\n"
      "s_waitcnt vmcnt(12)\n"
      "v_mfma_f32_32x32x16_bf16 v[128:143], v[204:207], v[36:39], v[128:143]\n"
      "v_exp_f32 v164, v164\n"
      "v_exp_f32 v165, v165\n"
      "v_exp_f32 v166, v166\n"
      "v_exp_f32 v167, v167\n"
      "v_smfmac_f32_32x32x32_bf16 v[96:111], v[224:227], v[144:151], v240\n"
      "v_exp_f32 v168, v168\n"
      "v_exp_f32 v169, v169\n"
      "v_exp_f32 v170, v170\n"
      "v_exp_f32 v171, v171\n"
      "v_smfmac_f32_32x32x32_bf16 v[96:111], v[228:231], v[152:159], v240\n"
      "v_exp_f32 v172, v172\n"
      "v_exp_f32 v173, v173\n"
      "v_exp_f32 v174, v174\n"
      "v_exp_f32 v175, v175\n"
      "v_mfma_f32_32x32x16_bf16 v[144:159], v[200:203], v[40:43], 0\n"
      "v_exp_f32 v128, v128\n"
      "v_exp_f32 v129, v129\n"
      "v_exp_f32 v130, v130\n"
      "v_exp_f32 v131, v131\n"
      "v_mfma_f32_32x32x16_bf16 v[144:159], v[204:207], v[44:47], v[144:159]\n"
      "v_exp_f32 v132, v132\n"
      "v_exp_f32 v133, v133\n"
      "v_exp_f32 v134, v134\n"
      "v_exp_f32 v135, v135\n"
      "v_smfmac_f32_32x32x32_bf16 v[112:127], v[224:227], v[160:167], v240\n"
      "buffer_load_dwordx4 v[224:227], %[voff], %[rs], %[soV] offen offset:2048 sc1\n"
      "v_exp_f32 v136, v136\n"
      "v_exp_f32 v137, v137\n"
      "v_exp_f32 v138, v138\n"
      "v_exp_f32 v139, v139\n"
      "v_smfmac_f32_32x32x32_bf16 v[112:127], v[228:231], v[168:175], v240\n"
      "buffer_load_dwordx4 v[228:231], %[voff], %[rs], %[soV] offen offset:3072 sc1\n"
      "s_add_u32 %[soV], %[soV], 4096\n"
      "v_exp_f32 v140, v140\n"
      "v_exp_f32 v141, v141\n"
      "v_exp_f32 v142, v142\n"
      "v_exp_f32 v143, v143\n"
      "v_mfma_f32_32x32x16_bf16 v[160:175], v[200:203], v[48:51], 0\n"
      "v_exp_f32 v144, v144\n"
      "v_exp_f32 v145, v145\n"
      "v_exp_f32 v146, v146\n"
      "v_exp_f32 v147, v147\n"
      "v_mfma_f32_32x32x16_bf16 v[160:175], v[204:207], v[52:55], v[160:175]\n"
      "v_exp_f32 v148, v148\n"
      "v_exp_f32 v149, v149\n"
      "v_exp_f32 v150, v150\n"
      "v_exp_f32 v151, v151\n"
      "s_waitcnt vmcnt(13)\n"
      "v_smfmac_f32_32x32x32_bf16 v[64:79], v[232:235], v[128:135], v240\n"
      "v_exp_f32 v152, v152\n"
      "v_exp_f32 v153, v153\n"
      "v_exp_f32 v154, v154\n"
      "v_exp_f32 v155, v155\n"
      "s_waitcnt vmcnt(12)\n"
      "v_smfmac_f32_32x32x32_bf16 v[64:79], v[236:239], v[136:143], v240\n"
      "v_exp_f32 v156, v156\n"
      "v_exp_f32 v157, v157\n"
      "v_exp_f32 v158, v158\n"
      "v_exp_f32 v159, v159\n"
      "v_mfma_f32_32x32x16_bf16 v[128:143], v[200:203], v[56:59], 0\n"
      "buffer_load_dwordx4 v[200:203], %[voff], %[rs], %[soK] offen offset:0 sc1\n"
      "v_exp_f32 v160, v160\n"
      "v_exp_f32 v161, v161\n"
      "v_exp_f32 v162, v162\n"
      "v_exp_f32 v163, v163\n"
      "v_mfma_f32_32x32x16_bf16 v[128:143], v[204:207], v[60:63], v[128:143]\n"
      "buffer_load_dwordx4 v[204:207], %[voff], %[rs], %[soK] offen offset:1024 sc1\n"
      "s_add_u32 %[soK], %[soK], 4096\n"
      "v_exp_f32 v164, v164\n"
      "v_exp_f32 v165, v165\n"
      "v_exp_f32 v166, v166\n"
      "v_exp_f32 v167, v167\n"
      "v_smfmac_f32_32x32x32_bf16 v[80:95], v[232:235], v[144:151], v240\n"
      "v_exp_f32 v168, v168\n"
      "v_exp_f32 v169, v169\n"
      "v_exp_f32 v170, v170\n"
      "v_exp_f32 v171, v171\n"
      "v_smfmac_f32_32x32x32_bf16 v[80:95], v[236:239], v[152:159], v240\n"
      "v_exp_f32 v172, v172\n"
      "v_exp_f32 v173, v173\n"
      "v_exp_f32 v174, v174\n"
      "v_exp_f32 v175, v175\n"
      "s_waitcnt vmcnt(13)\n"
      "v_mfma_f32_32x32x16_bf16 v[144:159], v[176:179], v[32:35], 0\n"
      "v_exp_f32 v128, v128\n"
      "v_exp_f32 v129, v129\n"
      "v_exp_f32 v130, v130\n"
      "v_exp_f32 v131, v131\n"
      "s_waitcnt vmcnt(12)\n"
      "v_mfma_f32_32x32x16_bf16 v[144:159], v[180:183], v[36:39], v[144:159]\n"
      "v_exp_f32 v132, v132\n"
      "v_exp_f32 v133, v133\n"
      "v_exp_f32 v134, v134\n"
      "v_exp_f32 v135, v135\n"
      "v_smfmac_f32_32x32x32_bf16 v[96:111], v[232:235], v[160:167], v240\n"
      "v_exp_f32 v136, v136\n"
      "v_exp_f32 v137, v137\n"
      "v_exp_f32 v138, v138\n"
      "v_exp_f32 v139, v139\n"
      "v_smfmac_f32_32x32x32_bf16 v[96:111], v[236:239], v[168:175], v240\n"
      "v_exp_f32 v140, v140\n"
      "v_exp_f32 v141, v141\n"
      "v_exp_f32 v142, v142\n"
      "v_exp_f32 v143, v143\n"
      "v_mfma_f32_32x32x16_bf16 v[160:175], v[176:179], v[40:43], 0\n"
      "v_exp_f32 v144, v144\n"
      "v_exp_f32 v145, v145\n"
      "v_exp_f32 v146, v146\n"
      "v_exp_f32 v147, v147\n"
      "v_mfma_f32_32x32x16_bf16 v[160:175], v[180:183], v[44:47], v[160:175]\n"
      "v_exp_f32 v148, v148\n"
      "v_exp_f32 v149, v149\n"
      "v_exp_f32 v150, v150\n"
      "v_exp_f32 v151, v151\n"
      "v_smfmac_f32_32x32x32_bf16 v[112:127], v[232:235], v[128:135], v240\n"
      "buffer_load_dwordx4 v[232:235], %[voff], %[rs], %[soV] offen offset:2048 sc1\n"
      "v_exp_f32 v152, v152\n"
      "v_exp_f32 v153, v153\n"
      "v_exp_f32 v154, v154\n"
      "v_exp_f32 v155, v155\n"
      "v_smfmac_f32_32x32x32_bf16 v[112:127], v[236:239], v[136:143], v240\n"
      "buffer_load_dwordx4 v[236:239], %[voff], %[rs], %[soV] offen offset:3072 sc1\n"
      "s_add_u32 %[soV], %[soV], 4096\n"
      "v_exp_f32 v156, v156\n"
      "v_exp_f32 v157, v157\n"
      "v_exp_f32 v158, v158\n"
      "v_exp_f32 v159, v159\n"
      "v_mfma_f32_32x32x16_bf16 v[128:143], v[176:179], v[48:51], 0\n"
      "v_exp_f32 v160, v160\n"
      "v_exp_f32 v161, v161\n"
      "v_exp_f32 v162, v162\n"
      "v_exp_f32 v163, v163\n"
      "v_mfma_f32_32x32x16_bf16 v[128:143], v[180:183], v[52:55], v[128:143]\n"
      "v_exp_f32 v164, v164\n"
      "v_exp_f32 v165, v165\n"
      "v_exp_f32 v166, v166\n"
      "v_exp_f32 v167, v167\n"
      "s_waitcnt vmcnt(13)\n"
      "v_smfmac_f32_32x32x32_bf16 v[64:79], v[208:211], v[144:151], v240\n"
      "v_exp_f32 v168, v168\n"
      "v_exp_f32 v169, v169\n"
      "v_exp_f32 v170, v170\n"
      "v_exp_f32 v171, v171\n"
      "s_waitcnt vmcnt(12)\n"
      "v_smfmac_f32_32x32x32_bf16 v[64:79], v[212:215], v[152:159], v240\n"
      "v_exp_f32 v172, v172\n"
      "v_exp_f32 v173, v173\n"
      "v_exp_f32 v174, v174\n"
      "v_exp_f32 v175, v175\n"
      "v_mfma_f32_32x32x16_bf16 v[144:159], v[176:179], v[56:59], 0\n"
      "v_exp_f32 v128, v128\n"
      "v_exp_f32 v129, v129\n"
      "v_exp_f32 v130, v130\n"
      "v_exp_f32 v131, v131\n"
      "v_mfma_f32_32x32x16_bf16 v[144:159], v[180:183], v[60:63], v[144:159]\n"
      "v_exp_f32 v132, v132\n"
      "v_exp_f32 v133, v133\n"
      "v_exp_f32 v134, v134\n"
      "v_exp_f32 v135, v135\n"
      "v_smfmac_f32_32x32x32_bf16 v[80:95], v[208:211], v[160:167], v240\n"
      "v_exp_f32 v136, v136\n"
      "v_exp_f32 v137, v137\n"
      "v_exp_f32 v138, v138\n"
      "v_exp_f32 v139, v139\n"
      "v_smfmac_f32_32x32x32_bf16 v[80:95], v[212:215], v[168:175], v240\n"
      "v_exp_f32 v140, v140\n"
      "v_exp_f32 v141, v141\n"
      "v_exp_f32 v142, v142\n"
      "v_exp_f32 v143, v143\n"
      "s_waitcnt vmcnt(11)\n"
      "v_mfma_f32_32x32x16_bf16 v[160:175], v[184:187], v[32:35], 0\n"
      "v_exp_f32 v144, v144\n"
      "v_exp_f32 v145, v145\n"
      "v_exp_f32 v146, v146\n"
      "v_exp_f32 v147, v147\n"
      "s_waitcnt vmcnt(10)\n"
      "v_mfma_f32_32x32x16_bf16 v[160:175], v[188:191], v[36:39], v[160:175]\n"
      "v_exp_f32 v148, v148\n"
      "v_exp_f32 v149, v149\n"
      "v_exp_f32 v150, v150\n"
      "v_exp_f32 v151, v151\n"
      "v_smfmac_f32_32x32x32_bf16 v[96:111], v[208:211], v[128:135], v240\n"
      "v_exp_f32 v152, v152\n"
      "v_exp_f32 v153, v153\n"
      "v_exp_f32 v154, v154\n"
      "v_exp_f32 v155, v155\n"
      "v_smfmac_f32_32x32x32_bf16 v[96:111], v[212:215], v[136:143], v240\n"
      "v_exp_f32 v156, v156\n"
      "v_exp_f32 v157, v157\n"
      "v_exp_f32 v158, v158\n"
      "v_exp_f32 v159, v159\n"
      "v_mfma_f32_32x32x16_bf16 v[128:143], v[184:187], v[40:43], 0\n"
      "v_exp_f32 v160, v160\n"
      "v_exp_f32 v161, v161\n"
      "v_exp_f32 v162, v162\n"
      "v_exp_f32 v163, v163\n"
      "v_mfma_f32_32x32x16_bf16 v[128:143], v[188:191], v[44:47], v[128:143]\n"
      "v_exp_f32 v164, v164\n"
      "v_exp_f32 v165, v165\n"
      "v_exp_f32 v166, v166\n"
      "v_exp_f32 v167, v167\n"
      "v_smfmac_f32_32x32x32_bf16 v[112:127], v[208:211], v[144:151], v240\n"
      "v_exp_f32 v168, v168\n"
      "v_exp_f32 v169, v169\n"
      "v_exp_f32 v170, v170\n"
      "v_exp_f32 v171, v171\n"
      "v_smfmac_f32_32x32x32_bf16 v[112:127], v[212:215], v[152:159], v240\n"
      "v_exp_f32 v172, v172\n"
      "v_exp_f32 v173, v173\n"
      "v_exp_f32 v174, v174\n"
      "v_exp_f32 v175, v175\n"
      "v_mfma_f32_32x32x16_bf16 v[144:159], v[184:187], v[48:51], 0\n"
      "v_exp_f32 v128, v128\n"
      "v_exp_f32 v129, v129\n"
      "v_exp_f32 v130, v130\n"
      "v_exp_f32 v131, v131\n"
      "v_mfma_f32_32x32x16_bf16 v[144:159], v[188:191], v[52:55], v[144:159]\n"
      "v_exp_f32 v132, v132\n"
      "v_exp_f32 v133, v133\n"
      "v_exp_f32 v134, v134\n"
      "v_exp_f32 v135, v135\n"
      "s_waitcnt vmcnt(9)\n"
      "v_smfmac_f32_32x32x32_bf16 v[64:79], v[216:219], v[160:167], v240\n"
      "v_exp_f32 v136, v136\n"
      "v_exp_f32 v137, v137\n"
      "v_exp_f32 v138, v138\n"
      "v_exp_f32 v139, v139\n"
      "s_waitcnt vmcnt(8)\n"
      "v_smfmac_f32_32x32x32_bf16 v[64:79], v[220:223], v[168:175], v240\n"
      "v_exp_f32 v140, v140\n"
      "v_exp_f32 v141, v141\n"
      "v_exp_f32 v142, v142\n"
      "v_exp_f32 v143, v143\n"
      "v_mfma_f32_32x32x16_bf16 v[160:175], v[184:187], v[56:59], 0\n"
      "v_exp_f32 v144, v144\n"
      "v_exp_f32 v145, v145\n"
      "v_exp_f32 v146, v146\n"
      "v_exp_f32 v147, v147\n"
      "v_mfma_f32_32x32x16_bf16 v[160:175], v[188:191], v[60:63], v[160:175]\n"
      "v_exp_f32 v148, v148\n"
      "v_exp_f32 v149, v149\n"
      "v_exp_f32 v150, v150\n"
      "v_exp_f32 v151, v151\n"
      "v_smfmac_f32_32x32x32_bf16 v[80:95], v[216:219], v[128:135], v240\n"
      "v_exp_f32 v152, v152\n"
      "v_exp_f32 v153, v153\n"
      "v_exp_f32 v154, v154\n"
      "v_exp_f32 v155, v155\n"
      "v_smfmac_f32_32x32x32_bf16 v[80:95], v[220:223], v[136:143], v240\n"
      "v_exp_f32 v156, v156\n"
      "v_exp_f32 v157, v157\n"
      "v_exp_f32 v158, v158\n"
      "v_exp_f32 v159, v159\n"
      "s_waitcnt vmcnt(7)\n"
      "v_mfma_f32_32x32x16_bf16 v[128:143], v[192:195], v[32:35], 0\n"
      "v_exp_f32 v160, v160\n"
      "v_exp_f32 v161, v161\n"
      "v_exp_f32 v162, v162\n"
      "v_exp_f32 v163, v163\n"
      "s_waitcnt vmcnt(6)\n"
      "v_mfma_f32_32x32x16_bf16 v[128:143], v[196:199], v[36:39], v[128:143]\n"
      "v_exp_f32 v164, v164\n"
      "v_exp_f32 v165, v165\n"
      "v_exp_f32 v166, v166\n"
      "v_exp_f32 v167, v167\n"
      "v_smfmac_f32_32x32x32_bf16 v[96:111], v[216:219], v[144:151], v240\n"
      "v_exp_f32 v168, v168\n"
      "v_exp_f32 v169, v169\n"
      "v_exp_f32 v170, v170\n"
      "v_exp_f32 v171, v171\n"
      "v_smfmac_f32_32x32x32_bf16 v[96:111], v[220:223], v[152:159], v240\n"
      "v_exp_f32 v172, v172\n"
      "v_exp_f32 v173, v173\n"
      "v_exp_f32 v174, v174\n"
      "v_exp_f32 v175, v175\n"
      "v_mfma_f32_32x32x16_bf16 v[144:159], v[192:195], v[40:43], 0\n"
      "v_exp_f32 v128, v128\n"
      "v_exp_f32 v129, v129\n"
      "v_exp_f32 v130, v130\n"
      "v_exp_f32 v131, v131\n"
      "v_mfma_f32_32x32x16_bf16 v[144:159], v[196:199], v[44:47], v[144:159]\n"
      "v_exp_f32 v132, v132\n"
      "v_exp_f32 v133, v133\n"
      "v_exp_f32 v134, v134\n"
      "v_exp_f32 v135, v135\n"
      "v_smfmac_f32_32x32x32_bf16 v[112:127], v[216:219], v[160:167], v240\n"
      "v_exp_f32 v136, v136\n"
      "v_exp_f32 v137, v137\n"
      "v_exp_f32 v138, v138\n"
      "v_exp_f32 v139, v139\n"
      "v_smfmac_f32_32x32x32_bf16 v[112:127], v[220:223], v[168:175], v240\n"
      "v_exp_f32 v140, v140\n"
      "v_exp_f32 v141, v141\n"
      "v_exp_f32 v142, v142\n"
      "v_exp_f32 v143, v143\n"
      "v_mfma_f32_32x32x16_bf16 v[160:175], v[192:195], v[48:51], 0\n"
      "v_exp_f32 v144, v144\n"
      "v_exp_f32 v145, v145\n"
      "v_exp_f32 v146, v146\n"
      "v_exp_f32 v147, v147\n"
      "v_mfma_f32_32x32x16_bf16 v[160:175], v[196:199], v[52:55], v[160:175]\n"
      "v_exp_f32 v148, v148\n"
      "v_exp_f32 v149, v149\n"
      "v_exp_f32 v150, v150\n"
      "v_exp_f32 v151, v151\n"
      "s_waitcnt vmcnt(5)\n"
      "v_smfmac_f32_32x32x32_bf16 v[64:79], v[224:227], v[128:135], v240\n"
      "v_exp_f32 v152, v152\n"
      "v_exp_f32 v153, v153\n"
      "v_exp_f32 v154, v154\n"
      "v_exp_f32 v155, v155\n"
      "s_waitcnt vmcnt(4)\n"
      "v_smfmac_f32_32x32x32_bf16 v[64:79], v[228:231], v[136:143], v240\n"
      "v_exp_f32 v156, v156\n"
      "v_exp_f32 v157, v157\n"
      "v_exp_f32 v158, v158\n"
      "v_exp_f32 v159, v159\n"
      "v_mfma_f32_32x32x16_bf16 v[128:143], v[192:195], v[56:59], 0\n"
      "v_exp_f32 v160, v160\n"
      "v_exp_f32 v161, v161\n"
      "v_exp_f32 v162, v162\n"
      "v_exp_f32 v163, v163\n"
      "v_mfma_f32_32x32x16_bf16 v[128:143], v[196:199], v[60:63], v[128:143]\n"
      "v_exp_f32 v164, v164\n"
      "v_exp_f32 v165, v165\n"
      "v_exp_f32 v166, v166\n"
      "v_exp_f32 v167, v167\n"
      "v_smfmac_f32_32x32x32_bf16 v[80:95], v[224:227], v[144:151], v240\n"
      "v_exp_f32 v168, v168\n"
      "v_exp_f32 v169, v169\n"
      "v_exp_f32 v170, v170\n"
      "v_exp_f32 v171, v171\n"
      "v_smfmac_f32_32x32x32_bf16 v[80:95], v[228:231], v[152:159], v240\n"
      "v_exp_f32 v172, v172\n"
      "v_exp_f32 v173, v173\n"
      "v_exp_f32 v174, v174\n"
      "v_exp_f32 v175, v175\n"
      "s_waitcnt vmcnt(3)\n"
      "v_mfma_f32_32x32x16_bf16 v[144:159], v[200:203], v[32:35], 0\n"
      "v_exp_f32 v128, v128\n"
      "v_exp_f32 v129, v129\n"
      "v_exp_f32 v130, v130\n"
      "v_exp_f32 v131, v131\n"
      "s_waitcnt vmcnt(2)\n"
      "v_mfma_f32_32x32x16_bf16 v[144:159], v[204:207], v[36:39], v[144:159]\n"
      "v_exp_f32 v132, v132\n"
      "v_exp_f32 v133, v133\n"
      "v_exp_f32 v134, v134\n"
      "v_exp_f32 v135, v135\n"
      "v_smfmac_f32_32x32x32_bf16 v[96:111], v[224:227], v[160:167], v240\n"
      "v_exp_f32 v136, v136\n"
      "v_exp_f32 v137, v137\n"
      "v_exp_f32 v138, v138\n"
      "v_exp_f32 v139, v139\n"
      "v_smfmac_f32_32x32x32_bf16 v[96:111], v[228:231], v[168:175], v240\n"
      "v_exp_f32 v140, v140\n"
      "v_exp_f32 v141, v141\n"
      "v_exp_f32 v142, v142\n"
      "v_exp_f32 v143, v143\n"
      "v_mfma_f32_32x32x16_bf16 v[160:175], v[200:203], v[40:43], 0\n"
      "v_exp_f32 v144, v144\n"
      "v_exp_f32 v145, v145\n"
      "v_exp_f32 v146, v146\n"
      "v_exp_f32 v147, v147\n"
      "v_mfma_f32_32x32x16_bf16 v[160:175], v[204:207], v[44:47], v[160:175]\n"
      "v_exp_f32 v148, v148\n"
      "v_exp_f32 v149, v149\n"
      "v_exp_f32 v150, v150\n"
      "v_exp_f32 v151, v151\n"
      "v_smfmac_f32_32x32x32_bf16 v[112:127], v[224:227], v[128:135], v240\n"
      "v_exp_f32 v152, v152\n"
      "v_exp_f32 v153, v153\n"
      "v_exp_f32 v154, v154\n"
      "v_exp_f32 v155, v155\n"
      "v_smfmac_f32_32x32x32_bf16 v[112:127], v[228:231], v[136:143], v240\n"
      "v_exp_f32 v156, v156\n"
      "v_exp_f32 v157, v157\n"
      "v_exp_f32 v158, v158\n"
      "v_exp_f32 v159, v159\n"
      "v_mfma_f32_32x32x16_bf16 v[128:143], v[200:203], v[48:51], 0\n"
      "v_exp_f32 v160, v160\n"
      "v_exp_f32 v161, v161\n"
      "v_exp_f32 v162, v162\n"
      "v_exp_f32 v163, v163\n"
      "v_mfma_f32_32x32x16_bf16 v[128:143], v[204:207], v[52:55], v[128:143]\n"
      "v_exp_f32 v164, v164\n"
      "v_exp_f32 v165, v165\n"
      "v_exp_f32 v166, v166\n"
      "v_exp_f32 v167, v167\n"
      "s_waitcnt vmcnt(1)\n"
      "v_smfmac_f32_32x32x32_bf16 v[64:79], v[232:235], v[144:151], v240\n"
      "v_exp_f32 v168, v168\n"
      "v_exp_f32 v169, v169\n"
      "v_exp_f32 v170, v170\n"
      "v_exp_f32 v171, v171\n"
      "s_waitcnt vmcnt(0)\n"
      "v_smfmac_f32_32x32x32_bf16 v[64:79], v[236:239], v[152:159], v240\n"
      "v_exp_f32 v172, v172\n"
      "v_exp_f32 v173, v173\n"
      "v_exp_f32 v174, v174\n"
      "v_exp_f32 v175, v175\n"
      "v_mfma_f32_32x32x16_bf16 v[144:159], v[200:203], v[56:59], 0\n"
      "v_exp_f32 v128, v128\n"
      "v_exp_f32 v129, v129\n"
      "v_exp_f32 v130, v130\n"
      "v_exp_f32 v131, v131\n"
      "v_mfma_f32_32x32x16_bf16 v[144:159], v[204:207], v[60:63], v[144:159]\n"
      "v_exp_f32 v132, v132\n"
      "v_exp_f32 v133, v133\n"
      "v_exp_f32 v134, v134\n"
      "v_exp_f32 v135, v135\n"
      "v_smfmac_f32_32x32x32_bf16 v[80:95], v[232:235], v[160:167], v240\n"
      "v_exp_f32 v136, v136\n"
      "v_exp_f32 v137, v137\n"
      "v_exp_f32 v138, v138\n"
      "v_exp_f32 v139, v139\n"
      "ds_write_b128 %[vpo], v[64:67] offset:0\n"
      "ds_write_b128 %[vpo], v[68:71] offset:1024\n"
      "ds_write_b128 %[vpo], v[72:75] offset:2048\n"
      "ds_write_b128 %[vpo], v[76:79] offset:3072\n"
      "v_smfmac_f32_32x32x32_bf16 v[80:95], v[236:239], v[168:175], v240\n"
      "v_exp_f32 v140, v140\n"
      "v_exp_f32 v141, v141\n"
      "v_exp_f32 v142, v142\n"
      "v_exp_f32 v143, v143\n"
      "v_exp_f32 v144, v144\n"
      "v_exp_f32 v145, v145\n"
      "v_exp_f32 v146, v146\n"
      "v_exp_f32 v147, v147\n"
      "v_exp_f32 v148, v148\n"
      "v_exp_f32 v149, v149\n"
      "v_exp_f32 v150, v150\n"
      "v_exp_f32 v151, v151\n"
      "v_smfmac_f32_32x32x32_bf16 v[96:111], v[232:235], v[128:135], v240\n"
      "v_exp_f32 v152, v152\n"
      "v_exp_f32 v153, v153\n"
      "v_exp_f32 v154, v154\n"
      "v_exp_f32 v155, v155\n"
      "ds_write_b128 %[vpo], v[80:83] offset:4096\n"
      "ds_write_b128 %[vpo], v[84:87] offset:5120\n"
      "ds_write_b128 %[vpo], v[88:91] offset:6144\n"
      "ds_write_b128 %[vpo], v[92:95] offset:7168\n"
      "v_smfmac_f32_32x32x32_bf16 v[96:111], v[236:239], v[136:143], v240\n"
      "v_exp_f32 v156, v156\n"
      "v_exp_f32 v157, v157\n"
      "v_exp_f32 v158, v158\n"
      "v_exp_f32 v159, v159\n"
      "v_smfmac_f32_32x32x32_bf16 v[112:127], v[232:235], v[144:151], v240\n"
      "s_nop 1\n"
      "v_smfmac_f32_32x32x32_bf16 v[112:127], v[236:239], v[152:159], v240\n"
      "s_nop 5\n"
      "ds_write_b128 %[vpo], v[96:99] offset:8192\n"
      "ds_write_b128 %[vpo], v[100:103] offset:9216\n"
      "ds_write_b128 %[vpo], v[104:107] offset:10240\n"
      "ds_write_b128 %[vpo], v[108:111] offset:11264\n"
      "s_nop 3\n"
      "ds_write_b128 %[vpo], v[112:115] offset:12288\n"
      "ds_write_b128 %[vpo], v[116:119] offset:13312\n"
      "ds_write_b128 %[vpo], v[120:123] offset:14336\n"
      "ds_write_b128 %[vpo], v[124:127] offset:15360\n"
      : [soK] "+s"(soK), [soV] "+s"(soV), [cnt] "=&s"(cnt), [tmp] "=&s"(tmp)
      : [voff] "v"(lane * 16), [vq] "v"(L + L_QF + lane * 16), [vsy] "v"(SY + 4 * SY_Q), [vpo] "v"(L + L_PART + w * 16384 + lane * 16), [rs] "s"(rws)
      : "memory", "scc",
      "v32","v33","v34","v35","v36","v37","v38","v39","v40","v41","v42","v43","v44","v45","v46","v47",
      "v48","v49","v50","v51","v52","v53","v54","v55","v56","v57","v58","v59","v60","v61","v62","v63",
      "v64","v65","v66","v67","v68","v69","v70","v71","v72","v73","v74","v75","v76","v77","v78","v79",
      "v80","v81","v82","v83","v84","v85","v86","v87","v88","v89","v90","v91","v92","v93","v94","v95",
      "v96","v97","v98","v99","v100","v101","v102","v103","v104","v105","v106","v107","v108","v109","v110","v111",
      "v112","v113","v114","v115","v116","v117","v118","v119","v120","v121","v122","v123","v124","v125","v126","v127",
      "v128","v129","v130","v131","v132","v133","v134","v135","v136","v137","v138","v139","v140","v141","v142","v143",
      "v144","v145","v146","v147","v148","v149","v150","v151","v152","v153","v154","v155","v156","v157","v158","v159",
      "v160","v161","v162","v163","v164","v165","v166","v167","v168","v169","v170","v171","v172","v173","v174","v175",
      "v176","v177","v178","v179","v180","v181","v182","v183","v184","v185","v186","v187","v188","v189","v190","v191",
      "v192","v193","v194","v195","v196","v197","v198","v199","v200","v201","v202","v203","v204","v205","v206","v207",
      "v208","v209","v210","v211","v212","v213","v214","v215","v216","v217","v218","v219","v220","v221","v222","v223",
      "v224","v225","v226","v227","v228","v229","v230","v231","v232","v233","v234","v235","v236","v237","v238","v239",
      "v240");
  }
  STAMP(9);
  u32x4 wf[32];
  const int cg = m >> 1, ptile = 2 * g + (m & 1);
  if (w < 4) {
    if (w == 0 && m < 7) {
      flag_wait(ws + WS_FLAG_Y + (r32 * 8 + m) * FLAG_STRIDE, epoch);
#pragma unroll
      for (int t = 0; t < 8; ++t) dma16c(rws, gq * 8192 + ((c16 ^ t) << 4), WS_YT + (32 * m + 4 * t) * 8192 + 256 * g, L + L_YS + 1024 * t);
      VMWAIT(0);
      const rsrc_t rout = mkrs(out, 2048 * 1792);
      const unsigned a0 = L + L_YS + (lane & 7) * 1024 + ((hh ^ (lane & 7)) << 4) + ((lane >> 3) & 3) * 4;
#pragma unroll
      for (int t = 0; t < 8; ++t) {
        const unsigned a = a0 ^ (t << 5);
        u32x4 v = {ldsr<unsigned>(a), ldsr<unsigned>(a + 256), ldsr<unsigned>(a + 512), ldsr<unsigned>(a + 768)};
        st16(v, rout, (lane >> 3) * 1792 + (lane & 7) * 16, (64 * g + 8 * t) * 1792 + 896 + 128 * m);
      }
    }
    flag_wait(ws + WS_FLAG_Y + ((2 * c16 + (cg >> 1)) * 8 + 7) * FLAG_STRIDE, epoch);
    const unsigned vo = (w == 3 && c16 >= 8) ? 0x80000000u : lane * 16;
#pragma unroll
    for (int t = 0; t < 32; ++t) wf[t] = __builtin_amdgcn_raw_buffer_load_b128(rws, vo + (t & 3) * 1024, WS_WO + (cg * 4 + w) * 32768 + (t >> 2) * 4096, 16);
  }
  STAMP(10);
  asm volatile("s_waitcnt lgkmcnt(0)\n\ts_barrier" ::: "memory");
  STAMP(11);
  {
    f32x4 s0 = {0, 0, 0, 0}, s1 = {0, 0, 0, 0};
#pragma unroll
    for (int k = 0; k < 8; ++k) {
      s0 += ldsr<f32x4>(L + L_PART + k * 16384 + w * 2048 + lane * 16);
      s1 += ldsr<f32x4>(L + L_PART + k * 16384 + w * 2048 + 1024 + lane * 16);
    }
    const float big = fmaxf(fmaxf(fmaxf(fabsf(s0[0]), fabsf(s0[1])), fmaxf(fabsf(s0[2]), fabsf(s0[3]))), fmaxf(fmaxf(fabsf(s1[0]), fabsf(s1[1])), fabsf(s1[2])));
    const float chk = s0[0] + s0[1] + s0[2] + s0[3] + s1[0] + s1[1] + s1[2];
    const bool ok = s1[3] > 0x1p-80f && s1[3] < 0x1p64f && big < 0x1p120f && chk == chk;
    if (!__all(ok)) {
      const bf16x8 q0 = ldsr<bf16x8>(L + L_QF + (w >> 1) * 2048 + lane * 16), q1 = ldsr<bf16x8>(L + L_QF + (w >> 1) * 2048 + 1024 + lane * 16);
      float mx = -__builtin_inff();
      f32x16 o = {};
#pragma nounroll
      for (int t = 0; t < 128; ++t) {
        const unsigned so = WS_KV + (t & 63) * 4096;
        f32x16 s = MFMA32(__builtin_bit_cast(bf16x8, __builtin_amdgcn_raw_buffer_load_b128(rws, lane * 16, so, 16)), q0, (f32x16){});
        s = MFMA32(__builtin_bit_cast(bf16x8, __builtin_amdgcn_raw_buffer_load_b128(rws, lane * 16 + 1024, so, 16)), q1, s);
        if (t < 64) {
#pragma unroll
          for (int i = 0; i < 16; ++i) mx = fmaxf(mx, s[i]);
          mx = fmaxf(mx, __shfl_xor(mx, 32));
        } else {
#pragma unroll
          for (int i = 0; i < 16; ++i) s[i] = __builtin_amdgcn_exp2f(s[i] - mx);
          const f32x8 lo = __builtin_shufflevector(s, s, 0, 1, 2, 3, 4, 5, 6, 7), hi = __builtin_shufflevector(s, s, 8, 9, 10, 11, 12, 13, 14, 15);
          o = __builtin_amdgcn_smfmac_f32_32x32x32_bf16(__builtin_bit_cast(bf16x8, __builtin_amdgcn_raw_buffer_load_b128(rws, lane * 16 + 2048, so, 16)), __builtin_bit_cast(bf16x16, lo), o, (int)0xdddddddd, 0, 0);
          o = __builtin_amdgcn_smfmac_f32_32x32x32_bf16(__builtin_bit_cast(bf16x8, __builtin_amdgcn_raw_buffer_load_b128(rws, lane * 16 + 3072, so, 16)), __builtin_bit_cast(bf16x16, hi), o, (int)0xdddddddd, 0, 0);
        }
      }
#pragma unroll
      for (int i = 0; i < 4; ++i) { s0[i] = (w & 1) ? o[8 + i] : o[i]; s1[i] = (w & 1) ? o[12 + i] : o[4 + i]; }
    }
    const float r = __builtin_amdgcn_rcpf(s1[3]);
    u32x4 o = {pk2(s0[0] * r, s0[1] * r), pk2(s0[2] * r, s0[3] * r), pk2(s1[0] * r, s1[1] * r), pk2(s1[2] * r, 0.f)};
#pragma unroll
    for (int i = 0; i < 3; ++i) o[i] = o[i] == ~0u ? 0x7fc07fc0u : o[i];
    st16c(o, rws, lane * 16, WS_O + (2 * g + ((w >> 1) & 1)) * 32768 + (2 * m + (w >> 2)) * 2048 + (w & 1) * 1024);
  }
  if (b == 0 && w == 7) flag_set(ws + WS_EPOCH, epoch, lane);
  STAMP(12);
  asm volatile("s_waitcnt lgkmcnt(0)\n\ts_barrier" ::: "memory");
  __builtin_amdgcn_s_sleep(GATHER_DELAY);
  {
    u32x4 a[4];
  #pragma nounroll
  for (int s = 0; s < SPIN_CAP; ++s) {
      unsigned mxv = 0;
#pragma unroll
      for (int i = 0; i < 4; ++i) a[i] = __builtin_amdgcn_raw_buffer_load_b128(rws, lane * 16 + i * 1024, WS_O + ptile * 32768 + w * 4096, 16);
#pragma unroll
      for (int i = 0; i < 4; ++i) mxv = max(max(mxv, a[i][0]), max(max(a[i][1], a[i][2]), a[i][3]));
      if (__all(mxv != ~0u)) break;
      MEMFENCE();
      __builtin_amdgcn_s_sleep(2);
    }
#pragma unroll
    for (int i = 0; i < 4; ++i) ldsw<u32x4>(L + L_OG + w * 4096 + i * 1024 + lane * 16, a[i]);
  }
  STAMP(13);
  asm volatile("s_waitcnt lgkmcnt(0)\n\ts_barrier" ::: "memory");
  if (w < 4) {
    f32x4 acc[4];
#pragma unroll
    for (int i = 0; i < 4; ++i) acc[i] = (f32x4){0, 0, 0, 0};
    const unsigned oa = L + L_OG + gq * 512 + c16 * 16;
#pragma unroll
    for (int h = 0; h < 16; ++h) {
      const bf16x8 a0 = ldsr<bf16x8>(oa + h * 2048), a1 = ldsr<bf16x8>(oa + h * 2048 + 256);
      const bf16x8 b0 = __builtin_bit_cast(bf16x8, wf[h]), b1 = __builtin_bit_cast(bf16x8, wf[16 + h]);
      acc[0] = MFMA16(a0, b0, acc[0]); acc[1] = MFMA16(a0, b1, acc[1]);
      acc[2] = MFMA16(a1, b0, acc[2]); acc[3] = MFMA16(a1, b1, acc[3]);
    }
    const rsrc_t rout = mkrs(out, 2048 * 1792);
    const unsigned vo = (w == 3 && c16 >= 8) ? 0x80000000u : gq * 4 * 1792 + c16 * 4;
#pragma unroll
    for (int p = 0; p < 2; ++p)
#pragma unroll
      for (int i = 0; i < 4; ++i)
        __builtin_amdgcn_raw_buffer_store_b32(pk2(acc[2 * p][i], acc[2 * p + 1][i]), rout, vo, (32 * ptile + 16 * p + i) * 1792 + cg * 224 + w * 64, 0);
  }
  STAMP(14);
}

extern "C" void kernel_launch(void* const* d_in, const int* in_sizes, int n_in, void* d_out, int out_size, void* d_ws, size_t ws_size,
                              hipStream_t stream) {
  fused_mqa_s4d<<<256, 512, 0, stream>>>((const char*)d_in[0], (const char*)d_in[1], (const char*)d_in[2], (const char*)d_in[3],
      (const char*)d_in[4], (const char*)d_in[5], (const unsigned*)d_in[6], (const unsigned*)d_in[7], (const unsigned*)d_in[8],
      (const unsigned*)d_in[9], (const unsigned*)d_in[10], (const unsigned*)d_in[11], (char*)d_out, (char*)d_ws);
}
```

```cpp
#include <hip/hip_runtime.h>
#include <stdint.h>

__global__ void __launch_bounds__(512, 2)
fused_mqa_s4d(const void* x, const void* wq, const void* wk, const void* wv, const void* wo, const void* u, const void* logA,
              const void* Aim, const void* Cre, const void* Cim, const void* logdt, const void* Dskip, void* out, void* ws) {
  __shared__ __attribute__((aligned(16))) char smem[163840];
  asm volatile(
    ".Lfbegin:\n"
    "s_bfe_u32 s42, s2, 0x30003\n"
    "s_load_dwordx2 s[4:5], s[0:1], 0x0\n"
    "s_load_dwordx2 s[24:25], s[0:1], 0x68\n"
    "v_readfirstlane_b32 s40, v0\n"
    "s_cmp_lt_u32 s42, 4\n"
    "s_cselect_b32 s58, 0x10, 0x18\n"
    "s_load_dwordx2 s[12:13], s[0:1], s58\n"
    "s_load_dwordx4 s[16:19], s[0:1], 0x20\n"
    "s_load_dwordx8 s[28:35], s[0:1], 0x30\n"
    "s_load_dwordx4 s[36:39], s[0:1], 0x50\n"
    "s_load_dwordx2 s[8:9], s[0:1], 0x8\n"
    "s_load_dwordx2 s[20:21], s[0:1], 0x60\n"
    "v_and_b32 v241, 63, v0\n"
    "s_lshr_b32 s40, s40, 6\n"
    "s_lshl3_add_u32 s58, s42, s40\n"
    "s_getpc_b64 s[60:61]\n"
    ".Lfpc:\n"
    "s_mulk_i32 s58, 640\n"
    "v_lshl_add_u32 v1, v241, 7, s58\n"
    "s_mov_b32 m0, 0x27e00\n"
    "v_min_u32 v1, .Lfend-.Lfpc, v1\n"
    "s_mov_b64 exec, 31\n"
    "global_load_lds_dword v1, s[60:61]\n"
    "s_mov_b64 exec, -1\n"
    "s_and_b32 s58, s2, 7\n"
    "s_lshr_b32 s59, s2, 6\n"
    "v_lshlrev_b32 v242, 4, v241\n"
    "s_lshl2_add_u32 s41, s58, s59\n"
    "s_and_b32 s46, s42, 3\n"
    "s_lshl3_add_u32 s43, s41, s42\n"
    "s_brev_b32 s54, 1\n"
    "s_mov_b32 s26, 0x500000\n"
    "s_mov_b32 s27, 0x20000\n"
    "v_mov_b32 v243, 0x27d00\n"
    "s_cmp_eq_u32 s40, 0\n"
    "s_cbranch_scc1 .Lf0\n"
    "s_cmp_eq_u32 s40, 1\n"
    "s_cbranch_scc1 .Lf1\n"
    "v_lshl_add_u32 v11, v241, 2, v243\n"
    "v_lshrrev_b32 v13, 5, v241\n"
    "ds_write_b32 v11, v13\n"
    "s_sub_u32 s47, s40, 4\n"
    "s_cselect_b32 s60, 7, s42\n"
    "s_lshl_b32 s57, s42, 5\n"
    "s_add_u32 s57, s57, s41\n"
    "s_lshl2_add_u32 s58, s57, s47\n"
    "s_lshl_b32 s58, s58, 8\n"
    "v_lshl_add_u32 v2, v241, 2, s58\n"
    "s_lshl_b32 s59, s57, 2\n"
    "s_cmp_eq_u32 s60, 7\n"
    "s_waitcnt lgkmcnt(0)\n"
    "s_load_dwordx2 s[44:45], s[24:25], 0x0\n"
    "s_cbranch_scc1 .Lfnp\n"
    "global_load_dword v32, v2, s[28:29]\n"
    "global_load_dword v33, v2, s[30:31]\n"
    "global_load_dword v34, v2, s[32:33]\n"
    "global_load_dword v35, v2, s[34:35]\n"
    "s_load_dword s68, s[36:37], s59\n"
    "s_load_dword s69, s[38:39], s59\n"
    "s_barrier\n"
    "s_branch .LfV\n"
    ".Lfnp:\n"
    "s_barrier\n"
    "s_cmp_lt_u32 s40, 4\n"
    "s_cbranch_scc0 .LfWO\n"
    "s_branch .LfQ\n"
    ".Lf1:\n"
    "s_and_b32 s59, s46, 1\n"
    "v_and_b32 v9, 48, v241\n"
    "s_lshl_b32 s60, s59, 8\n"
    "s_lshl_b32 s59, s59, 10\n"
    "s_add_u32 s59, s59, 2048\n"
    "s_cmp_lt_u32 s42, 4\n"
    "s_cselect_b32 s59, s60, s59\n"
    "s_cselect_b32 s60, 16, 0\n"
    "s_lshr_b32 s58, s46, 1\n"
    "s_lshl1_add_u32 s58, s41, s58\n"
    "v_mad_u32_u24 v9, v9, s60, v242\n"
    "s_lshl_b32 s58, s58, 12\n"
    "s_add_u32 s58, s58, s59\n"
    "s_add_u32 s58, s58, 0x40000\n"
    "v_mov_b32 v28, -1\n"
    "v_mov_b32 v29, -1\n"
    "v_mov_b32 v30, -1\n"
    "v_mov_b32 v31, -1\n"
    "s_waitcnt lgkmcnt(0)\n"
    "buffer_store_dwordx4 v[28:31], v9, s[24:27], s58 offen sc1\n"
    "s_load_dwordx2 s[44:45], s[24:25], 0x0\n"
    "s_barrier\n"
    "s_branch .LfKV\n"
    ".Lf0:\n"
    "s_setprio 3\n"
    "s_mov_b32 s6, 0x1c0000\n"
    "s_mov_b32 s7, 0x20000\n"
    "s_lshr_b32 s58, s2, 6\n"
    "s_lshl1_add_u32 s58, s58, s42\n"
    "s_sub_u32 s59, s58, 7\n"
    "s_cselect_b32 s58, s58, s59\n"
    "s_lshl_b32 s48, s58, 7\n"
    "v_lshrrev_b32 v5, 4, v241\n"
    "v_lshrrev_b32 v2, 3, v241\n"
    "v_xor_b32 v5, v5, v241\n"
    "v_mul_u32_u24 v2, 896, v2\n"
    "v_and_b32 v5, 7, v5\n"
    "v_lshl_add_u32 v2, v5, 4, v2\n"
    "s_lshl2_add_u32 s58, s41, s46\n"
    "s_mul_i32 s58, s58, 14336\n"
    "s_movk_i32 s59, 6144\n"
    "v_add_u32 v2, s58, v2\n"
    "s_lshl_b32 s84, s46, 11\n"
    "v_xad_u32 v3, v2, 64, s59\n"
    "s_waitcnt lgkmcnt(0)\n"
    "s_add_u32 m0, s84, 0x8800\n"
    "s_nop 0\n"
    "buffer_load_dwordx4 v2, s[4:7], s48 offen offset:0 lds\n"
    "buffer_load_dwordx4 v3, s[4:7], s48 offen offset:1024 lds\n"
    "s_mov_b32 s14, 0x6200\n"
    "s_mov_b32 s15, 0x20000\n"
    "v_lshlrev_b32 v5, 26, v241\n"
    "v_mul_u32_u24 v27, 37, v241\n"
    "v_and_or_b32 v28, v5, s54, v242\n"
    "s_mov_b32 m0, 0x0\n"
    "s_mul_i32 s58, s48, 28\n"
    "buffer_load_dwordx4 v242, s[12:15], s58 offen offset:0 lds\n"
    "buffer_load_dwordx4 v242, s[12:15], s58 offen offset:1024 lds\n"
    "buffer_load_dwordx4 v242, s[12:15], s58 offen offset:2048 lds\n"
    "buffer_load_dwordx4 v28, s[12:15], s58 offen offset:3072 lds\n"
    "s_barrier\n"
    "s_add_u32 s65, s48, 128\n"
    "s_cmp_eq_u32 s65, 896\n"
    "s_cselect_b32 s65, 0, s65\n"
    "s_add_u32 m0, s84, 0xa800\n"
    "s_nop 0\n"
    "buffer_load_dwordx4 v2, s[4:7], s65 offen offset:0 lds\n"
    "buffer_load_dwordx4 v3, s[4:7], s65 offen offset:1024 lds\n"
    "s_add_u32 s66, s65, 128\n"
    "s_cmp_eq_u32 s66, 896\n"
    "s_cselect_b32 s66, 0, s66\n"
    "s_add_u32 m0, s84, 0xc800\n"
    "s_nop 0\n"
    "buffer_load_dwordx4 v2, s[4:7], s66 offen offset:0 lds\n"
    "buffer_load_dwordx4 v3, s[4:7], s66 offen offset:1024 lds\n"
    "s_add_u32 s67, s66, 128\n"
    "s_cmp_eq_u32 s67, 896\n"
    "s_cselect_b32 s67, 0, s67\n"
    "s_add_u32 m0, s84, 0xe800\n"
    "s_nop 0\n"
    "buffer_load_dwordx4 v2, s[4:7], s67 offen offset:0 lds\n"
    "buffer_load_dwordx4 v3, s[4:7], s67 offen offset:1024 lds\n"
    "s_mov_b32 s10, 0x62000\n"
    "s_mov_b32 s11, 0x20000\n"
    "v_lshrrev_b32 v27, 8, v27\n"
    "s_mul_i32 s59, s42, 112\n"
    "v_mad_i32_i24 v10, v27, -7, v241\n"
    "v_and_b32 v12, 3, v27\n"
    "v_lshrrev_b32 v13, 2, v27\n"
    "s_movk_i32 s60, 896\n"
    "v_lshl_add_u32 v10, v10, 4, s59\n"
    "v_lshl_or_b32 v27, v12, 2, v13\n"
    "s_bfm_b64 s[62:63], 56, 0\n"
    "v_mad_u32_u24 v10, v27, s60, v10\n"
    "v_add_u32 v11, 896, v10\n"
    "v_add_u32 v30, 12544, v10\n"
    "v_add_u32 v31, 13440, v10\n"
    "s_mov_b64 exec, -1\n"
    "s_load_dwordx2 s[44:45], s[24:25], 0x0\n"
    "s_add_u32 s68, s67, 128\n"
    "s_cmp_eq_u32 s68, 896\n"
    "s_cselect_b32 s68, 0, s68\n"
    "s_add_u32 s69, s68, 128\n"
    "s_cmp_eq_u32 s69, 896\n"
    "s_cselect_b32 s69, 0, s69\n"
    "s_add_u32 s70, s69, 128\n"
    "s_cmp_eq_u32 s70, 896\n"
    "s_cselect_b32 s70, 0, s70\n"
    "s_add_u32 s58, s46, 1\n"
    "s_and_b32 s58, s58, 3\n"
    "s_lshl_b32 s85, s58, 11\n"
    "s_sub_u32 s58, s58, s46\n"
    "s_mul_i32 s58, s58, 14336\n"
    "v_add_u32 v4, s58, v2\n"
    "v_add_u32 v5, s58, v3\n"
    "s_add_u32 s58, s46, 2\n"
    "s_and_b32 s58, s58, 3\n"
    "s_lshl_b32 s86, s58, 11\n"
    "s_sub_u32 s58, s58, s46\n"
    "s_mul_i32 s58, s58, 14336\n"
    "v_add_u32 v6, s58, v2\n"
    "v_add_u32 v7, s58, v3\n"
    "s_add_u32 s58, s46, 3\n"
    "s_and_b32 s58, s58, 3\n"
    "s_lshl_b32 s87, s58, 11\n"
    "s_sub_u32 s58, s58, s46\n"
    "s_mul_i32 s58, s58, 14336\n"
    "v_add_u32 v8, s58, v2\n"
    "v_add_u32 v9, s58, v3\n"
    "v_mov_b32 v29, s54\n"
    "s_mov_b32 m0, 0x1000\n"
    "s_mul_i32 s58, s65, 28\n"
    "buffer_load_dwordx4 v242, s[12:15], s58 offen offset:0 lds\n"
    "buffer_load_dwordx4 v242, s[12:15], s58 offen offset:1024 lds\n"
    "buffer_load_dwordx4 v242, s[12:15], s58 offen offset:2048 lds\n"
    "buffer_load_dwordx4 v28, s[12:15], s58 offen offset:3072 lds\n"
    "s_mov_b32 m0, 0x27d80\n"
    "s_mov_b64 exec, 1\n"
    "buffer_load_dword v29, s[4:7], 0 offen lds\n"
    "s_mov_b64 exec, -1\n"
    "s_mov_b32 m0, 0x2000\n"
    "s_mul_i32 s58, s66, 28\n"
    "buffer_load_dwordx4 v242, s[12:15], s58 offen offset:0 lds\n"
    "buffer_load_dwordx4 v242, s[12:15], s58 offen offset:1024 lds\n"
    "buffer_load_dwordx4 v242, s[12:15], s58 offen offset:2048 lds\n"
    "buffer_load_dwordx4 v28, s[12:15], s58 offen offset:3072 lds\n"
    "s_mov_b32 m0, 0x3000\n"
    "s_mul_i32 s58, s67, 28\n"
    "buffer_load_dwordx4 v242, s[12:15], s58 offen offset:0 lds\n"
    "buffer_load_dwordx4 v242, s[12:15], s58 offen offset:1024 lds\n"
    "buffer_load_dwordx4 v242, s[12:15], s58 offen offset:2048 lds\n"
    "buffer_load_dwordx4 v28, s[12:15], s58 offen offset:3072 lds\n"
    "s_mov_b32 m0, 0x27d84\n"
    "s_mov_b64 exec, 1\n"
    "buffer_load_dword v29, s[4:7], 0 offen lds\n"
    "s_mov_b64 exec, -1\n"
    "s_mov_b64 exec, s[62:63]\n"
    "s_mov_b32 m0, 0x16800\n"
    "s_mul_i32 s58, s48, 448\n"
    "buffer_load_dwordx4 v10, s[8:11], s58 offen offset:0 lds\n"
    "buffer_load_dwordx4 v11, s[8:11], s58 offen offset:896 lds\n"
    "buffer_load_dwordx4 v30, s[8:11], s58 offen offset:1792 lds\n"
    "buffer_load_dwordx4 v31, s[8:11], s58 offen offset:2688 lds\n"
    "s_mov_b32 m0, 0x17600\n"
    "s_add_u32 s58, s58, 28672\n"
    "buffer_load_dwordx4 v10, s[8:11], s58 offen offset:0 lds\n"
    "buffer_load_dwordx4 v11, s[8:11], s58 offen offset:896 lds\n"
    "buffer_load_dwordx4 v30, s[8:11], s58 offen offset:1792 lds\n"
    "buffer_load_dwordx4 v31, s[8:11], s58 offen offset:2688 lds\n"
    "s_mov_b64 exec, -1\n"
    "s_add_u32 m0, s84, 0x10800\n"
    "s_nop 0\n"
    "buffer_load_dwordx4 v2, s[4:7], s68 offen offset:0 lds\n"
    "buffer_load_dwordx4 v3, s[4:7], s68 offen offset:1024 lds\n"
    "s_mov_b32 m0, 0x4000\n"
    "s_mul_i32 s58, s68, 28\n"
    "buffer_load_dwordx4 v242, s[12:15], s58 offen offset:0 lds\n"
    "buffer_load_dwordx4 v242, s[12:15], s58 offen offset:1024 lds\n"
    "buffer_load_dwordx4 v242, s[12:15], s58 offen offset:2048 lds\n"
    "buffer_load_dwordx4 v28, s[12:15], s58 offen offset:3072 lds\n"
    "s_add_u32 m0, s84, 0x12800\n"
    "s_nop 0\n"
    "buffer_load_dwordx4 v2, s[4:7], s69 offen offset:0 lds\n"
    "buffer_load_dwordx4 v3, s[4:7], s69 offen offset:1024 lds\n"
    "s_mov_b32 m0, 0x5000\n"
    "s_mul_i32 s58, s69, 28\n"
    "buffer_load_dwordx4 v242, s[12:15], s58 offen offset:0 lds\n"
    "buffer_load_dwordx4 v242, s[12:15], s58 offen offset:1024 lds\n"
    "buffer_load_dwordx4 v242, s[12:15], s58 offen offset:2048 lds\n"
    "buffer_load_dwordx4 v28, s[12:15], s58 offen offset:3072 lds\n"
    "s_mov_b32 m0, 0x27d88\n"
    "s_mov_b64 exec, 1\n"
    "buffer_load_dword v29, s[4:7], 0 offen lds\n"
    "s_mov_b64 exec, -1\n"
    "s_add_u32 m0, s84, 0x14800\n"
    "s_nop 0\n"
    "buffer_load_dwordx4 v2, s[4:7], s70 offen offset:0 lds\n"
    "buffer_load_dwordx4 v3, s[4:7], s70 offen offset:1024 lds\n"
    "s_mov_b32 m0, 0x6000\n"
    "s_mul_i32 s58, s70, 28\n"
    "buffer_load_dwordx4 v242, s[12:15], s58 offen offset:0 lds\n"
    "buffer_load_dwordx4 v242, s[12:15], s58 offen offset:1024 lds\n"
    "buffer_load_dwordx4 v242, s[12:15], s58 offen offset:2048 lds\n"
    "buffer_load_dwordx4 v28, s[12:15], s58 offen offset:3072 lds\n"
    "s_mov_b32 m0, 0x27d8c\n"
    "s_mov_b64 exec, 1\n"
    "buffer_load_dword v29, s[4:7], 0 offen lds\n"
    "s_mov_b64 exec, -1\n"
    "s_add_u32 m0, s85, 0x8800\n"
    "s_nop 0\n"
    "buffer_load_dwordx4 v4, s[4:7], s48 offen offset:0 lds\n"
    "buffer_load_dwordx4 v5, s[4:7], s48 offen offset:1024 lds\n"
    "s_add_u32 m0, s86, 0x8800\n"
    "s_nop 0\n"
    "buffer_load_dwordx4 v6, s[4:7], s48 offen offset:0 lds\n"
    "buffer_load_dwordx4 v7, s[4:7], s48 offen offset:1024 lds\n"
    "s_add_u32 m0, s87, 0x8800\n"
    "s_nop 0\n"
    "buffer_load_dwordx4 v8, s[4:7], s48 offen offset:0 lds\n"
    "buffer_load_dwordx4 v9, s[4:7], s48 offen offset:1024 lds\n"
    "s_mov_b32 m0, 0x27d90\n"
    "s_mov_b64 exec, 1\n"
    "buffer_load_dword v29, s[4:7], 0 offen lds\n"
    "s_mov_b64 exec, -1\n"
    "s_add_u32 m0, s85, 0xa800\n"
    "s_nop 0\n"
    "buffer_load_dwordx4 v4, s[4:7], s65 offen offset:0 lds\n"
    "buffer_load_dwordx4 v5, s[4:7], s65 offen offset:1024 lds\n"
    "s_add_u32 m0, s86, 0xa800\n"
    "s_nop 0\n"
    "buffer_load_dwordx4 v6, s[4:7], s65 offen offset:0 lds\n"
    "buffer_load_dwordx4 v7, s[4:7], s65 offen offset:1024 lds\n"
    "s_add_u32 m0, s87, 0xa800\n"
    "s_nop 0\n"
    "buffer_load_dwordx4 v8, s[4:7], s65 offen offset:0 lds\n"
    "buffer_load_dwordx4 v9, s[4:7], s65 offen offset:1024 lds\n"
    "s_mov_b64 exec, s[62:63]\n"
    "s_mov_b32 m0, 0x18400\n"
    "s_mul_i32 s58, s65, 448\n"
    "buffer_load_dwordx4 v10, s[8:11], s58 offen offset:0 lds\n"
    "buffer_load_dwordx4 v11, s[8:11], s58 offen offset:896 lds\n"
    "buffer_load_dwordx4 v30, s[8:11], s58 offen offset:1792 lds\n"
    "buffer_load_dwordx4 v31, s[8:11], s58 offen offset:2688 lds\n"
    "s_mov_b32 m0, 0x19200\n"
    "s_add_u32 s58, s58, 28672\n"
    "buffer_load_dwordx4 v10, s[8:11], s58 offen offset:0 lds\n"
    "buffer_load_dwordx4 v11, s[8:11], s58 offen offset:896 lds\n"
    "buffer_load_dwordx4 v30, s[8:11], s58 offen offset:1792 lds\n"
    "buffer_load_dwordx4 v31, s[8:11], s58 offen offset:2688 lds\n"
    "s_mov_b32 m0, 0x27d94\n"
    "s_mov_b64 exec, 1\n"
    "buffer_load_dword v29, s[4:7], 0 offen lds\n"
    "s_mov_b64 exec, -1\n"
    "s_add_u32 m0, s85, 0xc800\n"
    "s_nop 0\n"
    "buffer_load_dwordx4 v4, s[4:7], s66 offen offset:0 lds\n"
    "buffer_load_dwordx4 v5, s[4:7], s66 offen offset:1024 lds\n"
    "s_add_u32 m0, s86, 0xc800\n"
    "s_nop 0\n"
    "buffer_load_dwordx4 v6, s[4:7], s66 offen offset:0 lds\n"
    "buffer_load_dwordx4 v7, s[4:7], s66 offen offset:1024 lds\n"
    "s_add_u32 m0, s87, 0xc800\n"
    "s_nop 0\n"
    "buffer_load_dwordx4 v8, s[4:7], s66 offen offset:0 lds\n"
    "buffer_load_dwordx4 v9, s[4:7], s66 offen offset:1024 lds\n"
    "s_mov_b64 exec, s[62:63]\n"
    "s_mov_b32 m0, 0x1a000\n"
    "s_mul_i32 s58, s66, 448\n"
    "buffer_load_dwordx4 v10, s[8:11], s58 offen offset:0 lds\n"
    "buffer_load_dwordx4 v11, s[8:11], s58 offen offset:896 lds\n"
    "buffer_load_dwordx4 v30, s[8:11], s58 offen offset:1792 lds\n"
    "buffer_load_dwordx4 v31, s[8:11], s58 offen offset:2688 lds\n"
    "s_mov_b32 m0, 0x1ae00\n"
    "s_add_u32 s58, s58, 28672\n"
    "buffer_load_dwordx4 v10, s[8:11], s58 offen offset:0 lds\n"
    "buffer_load_dwordx4 v11, s[8:11], s58 offen offset:896 lds\n"
    "buffer_load_dwordx4 v30, s[8:11], s58 offen offset:1792 lds\n"
    "buffer_load_dwordx4 v31, s[8:11], s58 offen offset:2688 lds\n"
    "s_mov_b32 m0, 0x27d98\n"
    "s_mov_b64 exec, 1\n"
    "buffer_load_dword v29, s[4:7], 0 offen lds\n"
    "s_mov_b64 exec, -1\n"
    "s_add_u32 m0, s85, 0xe800\n"
    "s_nop 0\n"
    "buffer_load_dwordx4 v4, s[4:7], s67 offen offset:0 lds\n"
    "buffer_load_dwordx4 v5, s[4:7], s67 offen offset:1024 lds\n"
    "s_add_u32 m0, s86, 0xe800\n"
    "s_nop 0\n"
    "buffer_load_dwordx4 v6, s[4:7], s67 offen offset:0 lds\n"
    "buffer_load_dwordx4 v7, s[4:7], s67 offen offset:1024 lds\n"
    "s_add_u32 m0, s87, 0xe800\n"
    "s_nop 0\n"
    "buffer_load_dwordx4 v8, s[4:7], s67 offen offset:0 lds\n"
    "buffer_load_dwordx4 v9, s[4:7], s67 offen offset:1024 lds\n"
    "s_mov_b64 exec, s[62:63]\n"
    "s_mov_b32 m0, 0x1bc00\n"
    "s_mul_i32 s58, s67, 448\n"
    "buffer_load_dwordx4 v10, s[8:11], s58 offen offset:0 lds\n"
    "buffer_load_dwordx4 v11, s[8:11], s58 offen offset:896 lds\n"
    "buffer_load_dwordx4 v30, s[8:11], s58 offen offset:1792 lds\n"
    "buffer_load_dwordx4 v31, s[8:11], s58 offen offset:2688 lds\n"
    "s_mov_b32 m0, 0x1ca00\n"
    "s_add_u32 s58, s58, 28672\n"
    "buffer_load_dwordx4 v10, s[8:11], s58 offen offset:0 lds\n"
    "buffer_load_dwordx4 v11, s[8:11], s58 offen offset:896 lds\n"
    "buffer_load_dwordx4 v30, s[8:11], s58 offen offset:1792 lds\n"
    "buffer_load_dwordx4 v31, s[8:11], s58 offen offset:2688 lds\n"
    "s_mov_b32 m0, 0x27d9c\n"
    "s_mov_b64 exec, 1\n"
    "buffer_load_dword v29, s[4:7], 0 offen lds\n"
    "s_mov_b64 exec, -1\n"
    "s_add_u32 m0, s85, 0x10800\n"
    "s_nop 0\n"
    "buffer_load_dwordx4 v4, s[4:7], s68 offen offset:0 lds\n"
    "buffer_load_dwordx4 v5, s[4:7], s68 offen offset:1024 lds\n"
    "s_add_u32 m0, s86, 0x10800\n"
    "s_nop 0\n"
    "buffer_load_dwordx4 v6, s[4:7], s68 offen offset:0 lds\n"
    "buffer_load_dwordx4 v7, s[4:7], s68 offen offset:1024 lds\n"
    "s_add_u32 m0, s87, 0x10800\n"
    "s_nop 0\n"
    "buffer_load_dwordx4 v8, s[4:7], s68 offen offset:0 lds\n"
    "buffer_load_dwordx4 v9, s[4:7], s68 offen offset:1024 lds\n"
    "s_mov_b64 exec, s[62:63]\n"
    "s_mov_b32 m0, 0x1d800\n"
    "s_mul_i32 s58, s68, 448\n"
    "buffer_load_dwordx4 v10, s[8:11], s58 offen offset:0 lds\n"
    "buffer_load_dwordx4 v11, s[8:11], s58 offen offset:896 lds\n"
    "buffer_load_dwordx4 v30, s[8:11], s58 offen offset:1792 lds\n"
    "buffer_load_dwordx4 v31, s[8:11], s58 offen offset:2688 lds\n"
    "s_mov_b32 m0, 0x1e600\n"
    "s_add_u32 s58, s58, 28672\n"
    "buffer_load_dwordx4 v10, s[8:11], s58 offen offset:0 lds\n"
    "buffer_load_dwordx4 v11, s[8:11], s58 offen offset:896 lds\n"
    "buffer_load_dwordx4 v30, s[8:11], s58 offen offset:1792 lds\n"
    "buffer_load_dwordx4 v31, s[8:11], s58 offen offset:2688 lds\n"
    "s_mov_b32 m0, 0x27da0\n"
    "s_mov_b64 exec, 1\n"
    "buffer_load_dword v29, s[4:7], 0 offen lds\n"
    "s_mov_b64 exec, -1\n"
    "s_add_u32 m0, s85, 0x12800\n"
    "s_nop 0\n"
    "buffer_load_dwordx4 v4, s[4:7], s69 offen offset:0 lds\n"
    "buffer_load_dwordx4 v5, s[4:7], s69 offen offset:1024 lds\n"
    "s_add_u32 m0, s86, 0x12800\n"
    "s_nop 0\n"
    "buffer_load_dwordx4 v6, s[4:7], s69 offen offset:0 lds\n"
    "buffer_load_dwordx4 v7, s[4:7], s69 offen offset:1024 lds\n"
    "s_add_u32 m0, s87, 0x12800\n"
    "s_nop 0\n"
    "buffer_load_dwordx4 v8, s[4:7], s69 offen offset:0 lds\n"
    "buffer_load_dwordx4 v9, s[4:7], s69 offen offset:1024 lds\n"
    "s_mov_b64 exec, s[62:63]\n"
    "s_mov_b32 m0, 0x1f400\n"
    "s_mul_i32 s58, s69, 448\n"
    "buffer_load_dwordx4 v10, s[8:11], s58 offen offset:0 lds\n"
    "buffer_load_dwordx4 v11, s[8:11], s58 offen offset:896 lds\n"
    "buffer_load_dwordx4 v30, s[8:11], s58 offen offset:1792 lds\n"
    "buffer_load_dwordx4 v31, s[8:11], s58 offen offset:2688 lds\n"
    "s_mov_b32 m0, 0x20200\n"
    "s_add_u32 s58, s58, 28672\n"
    "buffer_load_dwordx4 v10, s[8:11], s58 offen offset:0 lds\n"
    "buffer_load_dwordx4 v11, s[8:11], s58 offen offset:896 lds\n"
    "buffer_load_dwordx4 v30, s[8:11], s58 offen offset:1792 lds\n"
    "buffer_load_dwordx4 v31, s[8:11], s58 offen offset:2688 lds\n"
    "s_mov_b32 m0, 0x27da4\n"
    "s_mov_b64 exec, 1\n"
    "buffer_load_dword v29, s[4:7], 0 offen lds\n"
    "s_mov_b64 exec, -1\n"
    "s_add_u32 m0, s85, 0x14800\n"
    "s_nop 0\n"
    "buffer_load_dwordx4 v4, s[4:7], s70 offen offset:0 lds\n"
    "buffer_load_dwordx4 v5, s[4:7], s70 offen offset:1024 lds\n"
    "s_add_u32 m0, s86, 0x14800\n"
    "s_nop 0\n"
    "buffer_load_dwordx4 v6, s[4:7], s70 offen offset:0 lds\n"
    "buffer_load_dwordx4 v7, s[4:7], s70 offen offset:1024 lds\n"
    "s_add_u32 m0, s87, 0x14800\n"
    "s_nop 0\n"
    "buffer_load_dwordx4 v8, s[4:7], s70 offen offset:0 lds\n"
    "buffer_load_dwordx4 v9, s[4:7], s70 offen offset:1024 lds\n"
    "s_mov_b64 exec, s[62:63]\n"
    "s_mov_b32 m0, 0x21000\n"
    "s_mul_i32 s58, s70, 448\n"
    "buffer_load_dwordx4 v10, s[8:11], s58 offen offset:0 lds\n"
    "buffer_load_dwordx4 v11, s[8:11], s58 offen offset:896 lds\n"
    "buffer_load_dwordx4 v30, s[8:11], s58 offen offset:1792 lds\n"
    "buffer_load_dwordx4 v31, s[8:11], s58 offen offset:2688 lds\n"
    "s_mov_b32 m0, 0x27da8\n"
    "s_mov_b64 exec, 1\n"
    "buffer_load_dword v29, s[4:7], 0 offen lds\n"
    "s_mov_b64 exec, s[62:63]\n"
    "s_mov_b32 m0, 0x21e00\n"
    "s_add_u32 s58, s58, 28672\n"
    "buffer_load_dwordx4 v10, s[8:11], s58 offen offset:0 lds\n"
    "buffer_load_dwordx4 v11, s[8:11], s58 offen offset:896 lds\n"
    "buffer_load_dwordx4 v30, s[8:11], s58 offen offset:1792 lds\n"
    "buffer_load_dwordx4 v31, s[8:11], s58 offen offset:2688 lds\n"
    "s_mov_b32 m0, 0x27dac\n"
    "s_mov_b64 exec, 1\n"
    "buffer_load_dword v29, s[4:7], 0 offen lds\n"
    "s_mov_b64 exec, -1\n"
    "v_mov_b32 v244, 1\n"
    "v_lshl_add_u32 v245, v241, 20, v243\n"
    "s_mov_b32 s49, 0\n"
    "s_bfm_b64 s[80:81], 32, 0\n"
    "s_bfm_b64 s[92:93], 8, 0\n"
    "s_mov_b32 s22, 0x380000\n"
    "s_mov_b32 s23, 0x20000\n"
    "s_waitcnt lgkmcnt(0)\n"
    "s_add_u32 s44, s44, 1\n"
    "s_addc_u32 s45, s45, 0\n"
    "ds_add_u32 v245, v244 offset:28\n"
    "s_lshr_b32 s82, s2, 3\n"
    "s_mul_i32 s58, s82, 37\n"
    "s_lshr_b32 s58, s58, 8\n"
    "s_mul_i32 s58, s58, 7\n"
    "s_sub_u32 s82, s82, s58\n"
    "s_lshl_b32 s59, s40, 13\n"
    "s_add_u32 s59, s59, 0x40000\n"
    "s_and_b32 s58, s2, 7\n"
    "s_lshl_b32 s58, s58, 15\n"
    "s_add_u32 s84, s58, s59\n"
    "s_add_u32 s58, s82, 1\n"
    "s_cmp_gt_u32 s58, 7\n"
    "s_addc_u32 s58, s58, s2\n"
    "s_and_b32 s58, s58, 7\n"
    "s_lshl_b32 s58, s58, 15\n"
    "s_add_u32 s85, s58, s59\n"
    "s_add_u32 s58, s82, 2\n"
    "s_cmp_gt_u32 s58, 7\n"
    "s_addc_u32 s58, s58, s2\n"
    "s_and_b32 s58, s58, 7\n"
    "s_lshl_b32 s58, s58, 15\n"
    "s_add_u32 s86, s58, s59\n"
    "s_add_u32 s58, s82, 3\n"
    "s_cmp_gt_u32 s58, 7\n"
    "s_addc_u32 s58, s58, s2\n"
    "s_and_b32 s58, s58, 7\n"
    "s_lshl_b32 s58, s58, 15\n"
    "s_add_u32 s87, s58, s59\n"
    "s_add_u32 s58, s82, 4\n"
    "s_cmp_gt_u32 s58, 7\n"
    "s_addc_u32 s58, s58, s2\n"
    "s_and_b32 s58, s58, 7\n"
    "s_lshl_b32 s58, s58, 15\n"
    "s_add_u32 s88, s58, s59\n"
    "s_add_u32 s58, s82, 5\n"
    "s_cmp_gt_u32 s58, 7\n"
    "s_addc_u32 s58, s58, s2\n"
    "s_and_b32 s58, s58, 7\n"
    "s_lshl_b32 s58, s58, 15\n"
    "s_add_u32 s89, s58, s59\n"
    "s_add_u32 s58, s82, 6\n"
    "s_cmp_gt_u32 s58, 7\n"
    "s_addc_u32 s58, s58, s2\n"
    "s_and_b32 s58, s58, 7\n"
    "s_lshl_b32 s58, s58, 15\n"
    "s_add_u32 s90, s58, s59\n"
    "s_add_u32 s58, s82, 7\n"
    "s_cmp_gt_u32 s58, 7\n"
    "s_addc_u32 s58, s58, s2\n"
    "s_and_b32 s58, s58, 7\n"
    "s_lshl_b32 s58, s58, 15\n"
    "s_add_u32 s91, s58, s59\n"
    "v_lshrrev_b32 v25, 3, v241\n"
    "v_and_b32 v26, 7, v241\n"
    "v_add_u32 v27, s2, v25\n"
    "s_lshl_b32 s58, s40, 10\n"
    "v_and_b32 v27, 7, v27\n"
    "s_add_u32 s58, s58, 0x8000\n"
    "v_lshl_add_u32 v27, v27, 5, v26\n"
    "v_lshl_add_u32 v13, v27, 7, s58\n"
    "v_add_u32 v12, 4096, v242\n"
    "s_lshl_b32 s58, s40, 14\n"
    "s_add_u32 s58, s58, 0xa800\n"
    "v_add_u32 v249, s58, v242\n"
    "s_lshl_b32 s58, s40, 6\n"
    "s_add_u32 s58, s58, s42\n"
    "s_lshl_b32 s58, s58, 7\n"
    "s_add_u32 s58, s58, 0x30000\n"
    "v_lshl_add_u32 v16, v241, 10, s58\n"
    "v_lshrrev_b32 v25, 2, v241\n"
    "v_and_b32 v26, 3, v241\n"
    "v_lshlrev_b32 v25, 13, v25\n"
    "v_lshl_add_u32 v17, v26, 4, v25\n"
    "v_mov_b32 v11, s54\n"
    "s_lshl_b32 s83, s43, 7\n"
    "s_add_u32 s83, s83, 0x10000\n"
    ".LfSPEC:\n"
    ".LfL1:\n"
    "ds_read_b32 v250, v243 offset:44\n"
    "s_add_u32 s49, s49, 1\n"
    "s_cmp_lt_u32 s49, 0x100000\n"
    "s_cselect_b32 s50, 1, 0\n"
    "s_waitcnt lgkmcnt(0)\n"
    "v_cmp_gt_u32 vcc, s50, v250\n"
    "s_cbranch_vccnz .LfL1\n"
    ".LfL2:\n"
    "ds_read_b32 v250, v243 offset:172\n"
    "s_add_u32 s49, s49, 1\n"
    "s_cmp_lt_u32 s49, 0x100000\n"
    "s_cselect_b32 s50, 0, -1\n"
    "s_waitcnt lgkmcnt(0)\n"
    "v_cmp_lt_u32 vcc, s50, v250\n"
    "s_cbranch_vccnz .LfL2\n"
    "s_sub_u32 s58, s42, 1\n"
    "s_cmp_lt_u32 s58, 6\n"
    "s_cbranch_scc1 .LfSD\n"
    "s_sleep 8\n"
    ".LfSD:\n"
    "s_sleep 1\n"
    "buffer_load_dwordx4 v[176:179], v242, s[24:27], s84 offen offset:0 sc1\n"
    "buffer_load_dwordx4 v[180:183], v242, s[24:27], s84 offen offset:1024 sc1\n"
    "buffer_load_dwordx4 v[208:211], v242, s[24:27], s84 offen offset:2048 sc1\n"
    "buffer_load_dwordx4 v[212:215], v242, s[24:27], s84 offen offset:3072 sc1\n"
    "buffer_load_dwordx4 v[184:187], v12, s[24:27], s84 offen offset:0 sc1\n"
    "buffer_load_dwordx4 v[188:191], v12, s[24:27], s84 offen offset:1024 sc1\n"
    "buffer_load_dwordx4 v[216:219], v12, s[24:27], s84 offen offset:2048 sc1\n"
    "buffer_load_dwordx4 v[220:223], v12, s[24:27], s84 offen offset:3072 sc1\n"
    "v_mov_b32 v160, 0\n"
    "v_mov_b32 v161, 0\n"
    "v_mov_b32 v162, 0\n"
    "v_mov_b32 v163, 0\n"
    "v_mov_b32 v240, 0xdddddddd\n"
    "v_mov_b32 v252, s44\n"
    "v_mov_b32 v253, s45\n"
    "v_mfma_f32_32x32x16_bf16 v[64:79], v[160:163], v[160:163], 0\n"
    "v_mfma_f32_32x32x16_bf16 v[80:95], v[160:163], v[160:163], 0\n"
    "v_mfma_f32_32x32x16_bf16 v[96:111], v[160:163], v[160:163], 0\n"
    "v_mfma_f32_32x32x16_bf16 v[112:127], v[160:163], v[160:163], 0\n"
    "s_waitcnt vmcnt(0)\n"
    "s_branch .LfCE\n"
    ".Lfzq0:\n"
    ".LfKV:\n"
    "s_setprio 3\n"
    "v_mov_b32 v244, 1\n"
    "v_lshl_add_u32 v245, v241, 20, v243\n"
    "s_mov_b32 s49, 0\n"
    "s_bfm_b64 s[80:81], 32, 0\n"
    "s_bfm_b64 s[92:93], 8, 0\n"
    "s_mov_b32 s22, 0x380000\n"
    "s_mov_b32 s23, 0x20000\n"
    "v_and_b32 v12, 15, v241\n"
    "v_lshrrev_b32 v13, 4, v241\n"
    "v_lshlrev_b32 v14, 1, v12\n"
    "v_lshrrev_b32 v15, 1, v12\n"
    "v_and_b32 v14, 8, v14\n"
    "v_and_b32 v16, 4, v15\n"
    "v_and_b32 v17, 3, v12\n"
    "v_or3_b32 v10, v14, v16, v17\n"
    "v_lshrrev_b32 v14, 1, v10\n"
    "v_xor_b32 v14, v14, v13\n"
    "v_lshlrev_b32 v14, 4, v14\n"
    "v_lshl_add_u32 v6, v10, 7, v14\n"
    "s_lshl_b32 s58, s46, 11\n"
    "s_add_u32 s58, s58, 0x8800\n"
    "v_add_u32 v6, s58, v6\n"
    "v_xor_b32 v7, 64, v6\n"
    "v_lshrrev_b32 v14, 2, v12\n"
    "v_lshl_add_u32 v14, v13, 3, v14\n"
    "v_mul_u32_u24 v14, 56, v14\n"
    "v_and_b32 v15, 1, v15\n"
    "v_lshl_add_u32 v14, v15, 5, v14\n"
    "v_and_b32 v15, 1, v12\n"
    "v_lshl_add_u32 v14, v15, 3, v14\n"
    "v_add_u32 v8, 0x0, v14\n"
    "s_lshr_b32 s58, s46, 1\n"
    "s_lshl1_add_u32 s58, s41, s58\n"
    "s_lshl_b32 s58, s58, 12\n"
    "s_add_u32 s62, s58, 0x40000\n"
    "s_lshr_b32 s61, s2, 3\n"
    "s_mul_i32 s58, s61, 37\n"
    "s_lshr_b32 s58, s58, 8\n"
    "s_mul_i32 s58, s58, 7\n"
    "s_sub_u32 s61, s61, s58\n"
    "s_add_u32 s61, s61, 1\n"
    "v_lshrrev_b32 v14, 5, v241\n"
    "v_mov_b32 v15, s2\n"
    "v_mad_u32_u24 v14, v14, s61, v15\n"
    "v_and_b32 v15, 31, v241\n"
    "v_and_b32 v14, 7, v14\n"
    "v_lshl_add_u32 v14, v14, 5, v15\n"
    "s_lshl_b32 s63, s43, 7\n"
    "s_mov_b32 s61, 0x18000\n"
    "s_add_u32 s63, s63, 0x8000\n"
    "s_and_b32 s59, s46, 1\n"
    "s_lshl_b32 s64, s43, 7\n"
    "s_add_u32 s64, s64, 0x18000\n"
    "v_lshl_add_u32 v1, v14, 7, s61\n"
    "s_mov_b64 s[4:5], s[18:19]\n"
    "s_cmp_eq_u32 s42, 7\n"
    "s_cselect_b32 s67, s54, 0\n"
    "s_mul_i32 s68, s41, 57344\n"
    "s_lshl_b32 s69, s42, 7\n"
    "s_add_u32 s68, s68, s69\n"
    "s_add_u32 s69, s68, 28672\n"
    "s_mov_b32 s6, 0x1c0000\n"
    "s_mov_b32 s7, 0x20000\n"
    "v_lshrrev_b32 v89, 5, v241\n"
    "v_lshrrev_b32 v88, 3, v241\n"
    "v_xor_b32 v89, v89, v241\n"
    "v_mul_u32_u24 v88, 896, v88\n"
    "v_and_b32 v89, 7, v89\n"
    "v_lshl_add_u32 v88, v89, 4, v88\n"
    "v_or_b32 v88, s67, v88\n"
    "s_movk_i32 s58, 6144\n"
    "v_xad_u32 v89, v88, 32, s58\n"
    "s_movk_i32 s58, 12288\n"
    "v_xad_u32 v90, v88, 64, s58\n"
    "v_xad_u32 v91, v89, 64, s58\n"
    "s_cmp_lt_u32 s42, 4\n"
    "s_cbranch_scc0 .LfisV\n"
    "v_lshl_add_u32 v14, s59, 4, v10\n"
    "v_lshlrev_b32 v14, 4, v14\n"
    "v_lshl_add_u32 v9, v13, 9, v14\n"
    "s_mov_b32 s66, 0x3e8b97f6\n"
    "v_cmp_ne_u32 s[78:79], 3, v13\n"
    "s_branch .LfJ10\n"
    ".LfS4:\n"
    ".LfL11:\n"
    "ds_read_b32 v250, v243 offset:132\n"
    "s_add_u32 s49, s49, 1\n"
    "s_cmp_lt_u32 s49, 0x100000\n"
    "s_cselect_b32 s50, 0, -1\n"
    "s_waitcnt lgkmcnt(0)\n"
    "v_cmp_lt_u32 vcc, s50, v250\n"
    "s_cbranch_vccnz .LfL11\n"
    "s_branch .LfB5\n"
    ".LfS6:\n"
    ".LfL12:\n"
    "ds_read_b32 v250, v243 offset:136\n"
    "s_add_u32 s49, s49, 1\n"
    "s_cmp_lt_u32 s49, 0x100000\n"
    "s_cselect_b32 s50, 0, -1\n"
    "s_waitcnt lgkmcnt(0)\n"
    "v_cmp_lt_u32 vcc, s50, v250\n"
    "s_cbranch_vccnz .LfL12\n"
    "s_branch .LfB7\n"
    ".LfS8:\n"
    ".LfL13:\n"
    "ds_read_b32 v250, v243 offset:140\n"
    "s_add_u32 s49, s49, 1\n"
    "s_cmp_lt_u32 s49, 0x100000\n"
    "s_cselect_b32 s50, 0, -1\n"
    "s_waitcnt lgkmcnt(0)\n"
    "v_cmp_lt_u32 vcc, s50, v250\n"
    "s_cbranch_vccnz .LfL13\n"
    "s_branch .LfB9\n"
    ".LfJ10:\n"
    "s_waitcnt lgkmcnt(0)\n"
    "s_add_u32 s44, s44, 1\n"
    "s_addc_u32 s45, s45, 0\n"
    "s_waitcnt vmcnt(0)\n"
    "v_mov_b32 v250, s44\n"
    "v_mov_b32 v251, s45\n"
    "s_mov_b64 exec, 1\n"
    "buffer_store_dwordx2 v[250:251], off, s[24:27], s64 sc1\n"
    "s_mov_b64 exec, -1\n"
    ".LfL3:\n"
    "ds_read_b32 v250, v243 offset:128\n"
    "s_add_u32 s49, s49, 1\n"
    "s_cmp_lt_u32 s49, 0x100000\n"
    "s_cselect_b32 s50, 0, -1\n"
    "s_waitcnt lgkmcnt(0)\n"
    "v_cmp_lt_u32 vcc, s50, v250\n"
    "s_cbranch_vccnz .LfL3\n"
    "ds_read_b128 v[32:35], v6 offset:0\n"
    "ds_read_b64_tr_b16 v[36:37], v8 offset:0\n"
    "ds_read_b64_tr_b16 v[38:39], v8 offset:224\n"
    "ds_read_b64_tr_b16 v[40:41], v8 offset:16\n"
    "ds_read_b64_tr_b16 v[42:43], v8 offset:240\n"
    "ds_read_b128 v[44:47], v7 offset:0\n"
    "ds_read_b64_tr_b16 v[48:49], v8 offset:1792\n"
    "ds_read_b64_tr_b16 v[50:51], v8 offset:2016\n"
    "ds_read_b64_tr_b16 v[52:53], v8 offset:1808\n"
    "ds_read_b64_tr_b16 v[54:55], v8 offset:2032\n"
    "ds_read_b32 v251, v243 offset:132\n"
    "ds_read_b128 v[56:59], v6 offset:8192\n"
    "ds_read_b64_tr_b16 v[60:61], v8 offset:4096\n"
    "ds_read_b64_tr_b16 v[62:63], v8 offset:4320\n"
    "ds_read_b64_tr_b16 v[64:65], v8 offset:4112\n"
    "ds_read_b64_tr_b16 v[66:67], v8 offset:4336\n"
    "ds_read_b128 v[68:71], v7 offset:8192\n"
    "ds_read_b64_tr_b16 v[72:73], v8 offset:5888\n"
    "ds_read_b64_tr_b16 v[74:75], v8 offset:6112\n"
    "ds_read_b64_tr_b16 v[76:77], v8 offset:5904\n"
    "ds_read_b64_tr_b16 v[78:79], v8 offset:6128\n"
    "s_waitcnt lgkmcnt(15)\n"
    "v_mfma_f32_16x16x32_bf16 v[20:23], v[36:39], v[32:35], 0\n"
    "v_mfma_f32_16x16x32_bf16 v[24:27], v[40:43], v[32:35], 0\n"
    "s_waitcnt lgkmcnt(11)\n"
    "v_mfma_f32_16x16x32_bf16 v[20:23], v[48:51], v[44:47], v[20:23]\n"
    "v_mfma_f32_16x16x32_bf16 v[24:27], v[52:55], v[44:47], v[24:27]\n"
    "s_waitcnt lgkmcnt(10)\n"
    "v_cmp_ne_u32 vcc, 0, v251\n"
    "s_cbranch_vccnz .LfS4\n"
    ".LfB5:\n"
    "ds_read_b128 v[32:35], v6 offset:16384\n"
    "ds_read_b64_tr_b16 v[36:37], v8 offset:8192\n"
    "ds_read_b64_tr_b16 v[38:39], v8 offset:8416\n"
    "ds_read_b64_tr_b16 v[40:41], v8 offset:8208\n"
    "ds_read_b64_tr_b16 v[42:43], v8 offset:8432\n"
    "ds_read_b128 v[44:47], v7 offset:16384\n"
    "ds_read_b64_tr_b16 v[48:49], v8 offset:9984\n"
    "ds_read_b64_tr_b16 v[50:51], v8 offset:10208\n"
    "ds_read_b64_tr_b16 v[52:53], v8 offset:10000\n"
    "ds_read_b64_tr_b16 v[54:55], v8 offset:10224\n"
    "s_waitcnt lgkmcnt(15)\n"
    "v_mfma_f32_16x16x32_bf16 v[20:23], v[60:63], v[56:59], v[20:23]\n"
    "v_mfma_f32_16x16x32_bf16 v[24:27], v[64:67], v[56:59], v[24:27]\n"
    "s_waitcnt lgkmcnt(10)\n"
    "v_mfma_f32_16x16x32_bf16 v[20:23], v[72:75], v[68:71], v[20:23]\n"
    "v_mfma_f32_16x16x32_bf16 v[24:27], v[76:79], v[68:71], v[24:27]\n"
    "ds_read_b32 v251, v243 offset:136\n"
    "ds_read_b128 v[56:59], v6 offset:24576\n"
    "ds_read_b64_tr_b16 v[60:61], v8 offset:12288\n"
    "ds_read_b64_tr_b16 v[62:63], v8 offset:12512\n"
    "ds_read_b64_tr_b16 v[64:65], v8 offset:12304\n"
    "ds_read_b64_tr_b16 v[66:67], v8 offset:12528\n"
    "ds_read_b128 v[68:71], v7 offset:24576\n"
    "ds_read_b64_tr_b16 v[72:73], v8 offset:14080\n"
    "ds_read_b64_tr_b16 v[74:75], v8 offset:14304\n"
    "ds_read_b64_tr_b16 v[76:77], v8 offset:14096\n"
    "ds_read_b64_tr_b16 v[78:79], v8 offset:14320\n"
    "s_waitcnt lgkmcnt(15)\n"
    "v_mfma_f32_16x16x32_bf16 v[20:23], v[36:39], v[32:35], v[20:23]\n"
    "v_mfma_f32_16x16x32_bf16 v[24:27], v[40:43], v[32:35], v[24:27]\n"
    "s_waitcnt lgkmcnt(11)\n"
    "v_mfma_f32_16x16x32_bf16 v[20:23], v[48:51], v[44:47], v[20:23]\n"
    "v_mfma_f32_16x16x32_bf16 v[24:27], v[52:55], v[44:47], v[24:27]\n"
    "s_waitcnt lgkmcnt(10)\n"
    "v_cmp_ne_u32 vcc, 0, v251\n"
    "s_cbranch_vccnz .LfS6\n"
    ".LfB7:\n"
    "ds_read_b128 v[32:35], v6 offset:32768\n"
    "ds_read_b64_tr_b16 v[36:37], v8 offset:16384\n"
    "ds_read_b64_tr_b16 v[38:39], v8 offset:16608\n"
    "ds_read_b64_tr_b16 v[40:41], v8 offset:16400\n"
    "ds_read_b64_tr_b16 v[42:43], v8 offset:16624\n"
    "ds_read_b128 v[44:47], v7 offset:32768\n"
    "ds_read_b64_tr_b16 v[48:49], v8 offset:18176\n"
    "ds_read_b64_tr_b16 v[50:51], v8 offset:18400\n"
    "ds_read_b64_tr_b16 v[52:53], v8 offset:18192\n"
    "ds_read_b64_tr_b16 v[54:55], v8 offset:18416\n"
    "s_waitcnt lgkmcnt(15)\n"
    "v_mfma_f32_16x16x32_bf16 v[20:23], v[60:63], v[56:59], v[20:23]\n"
    "v_mfma_f32_16x16x32_bf16 v[24:27], v[64:67], v[56:59], v[24:27]\n"
    "s_waitcnt lgkmcnt(10)\n"
    "v_mfma_f32_16x16x32_bf16 v[20:23], v[72:75], v[68:71], v[20:23]\n"
    "v_mfma_f32_16x16x32_bf16 v[24:27], v[76:79], v[68:71], v[24:27]\n"
    "ds_read_b32 v251, v243 offset:140\n"
    "ds_read_b128 v[56:59], v6 offset:40960\n"
    "ds_read_b64_tr_b16 v[60:61], v8 offset:20480\n"
    "ds_read_b64_tr_b16 v[62:63], v8 offset:20704\n"
    "ds_read_b64_tr_b16 v[64:65], v8 offset:20496\n"
    "ds_read_b64_tr_b16 v[66:67], v8 offset:20720\n"
    "ds_read_b128 v[68:71], v7 offset:40960\n"
    "ds_read_b64_tr_b16 v[72:73], v8 offset:22272\n"
    "ds_read_b64_tr_b16 v[74:75], v8 offset:22496\n"
    "ds_read_b64_tr_b16 v[76:77], v8 offset:22288\n"
    "ds_read_b64_tr_b16 v[78:79], v8 offset:22512\n"
    "s_waitcnt lgkmcnt(15)\n"
    "v_mfma_f32_16x16x32_bf16 v[20:23], v[36:39], v[32:35], v[20:23]\n"
    "v_mfma_f32_16x16x32_bf16 v[24:27], v[40:43], v[32:35], v[24:27]\n"
    "s_waitcnt lgkmcnt(11)\n"
    "v_mfma_f32_16x16x32_bf16 v[20:23], v[48:51], v[44:47], v[20:23]\n"
    "v_mfma_f32_16x16x32_bf16 v[24:27], v[52:55], v[44:47], v[24:27]\n"
    "s_waitcnt lgkmcnt(10)\n"
    "v_cmp_ne_u32 vcc, 0, v251\n"
    "s_cbranch_vccnz .LfS8\n"
    ".LfB9:\n"
    "ds_read_b128 v[32:35], v6 offset:49152\n"
    "ds_read_b64_tr_b16 v[36:37], v8 offset:24576\n"
    "ds_read_b64_tr_b16 v[38:39], v8 offset:24800\n"
    "ds_read_b64_tr_b16 v[40:41], v8 offset:24592\n"
    "ds_read_b64_tr_b16 v[42:43], v8 offset:24816\n"
    "ds_read_b128 v[44:47], v7 offset:49152\n"
    "ds_read_b64_tr_b16 v[48:49], v8 offset:26368\n"
    "ds_read_b64_tr_b16 v[50:51], v8 offset:26592\n"
    "ds_read_b64_tr_b16 v[52:53], v8 offset:26384\n"
    "ds_read_b64_tr_b16 v[54:55], v8 offset:26608\n"
    "ds_add_u32 v245, v244 offset:0\n"
    "s_waitcnt lgkmcnt(15)\n"
    "v_mfma_f32_16x16x32_bf16 v[20:23], v[60:63], v[56:59], v[20:23]\n"
    "v_mfma_f32_16x16x32_bf16 v[24:27], v[64:67], v[56:59], v[24:27]\n"
    "s_waitcnt lgkmcnt(11)\n"
    "v_mfma_f32_16x16x32_bf16 v[20:23], v[72:75], v[68:71], v[20:23]\n"
    "v_mfma_f32_16x16x32_bf16 v[24:27], v[76:79], v[68:71], v[24:27]\n"
    "s_waitcnt lgkmcnt(8)\n"
    "v_mfma_f32_16x16x32_bf16 v[20:23], v[36:39], v[32:35], v[20:23]\n"
    "s_waitcnt lgkmcnt(6)\n"
    "v_mfma_f32_16x16x32_bf16 v[24:27], v[40:43], v[32:35], v[24:27]\n"
    "s_waitcnt lgkmcnt(3)\n"
    "v_mfma_f32_16x16x32_bf16 v[20:23], v[48:51], v[44:47], v[20:23]\n"
    "s_waitcnt lgkmcnt(1)\n"
    "v_mfma_f32_16x16x32_bf16 v[24:27], v[52:55], v[44:47], v[24:27]\n"
    "s_nop 5\n"
    "v_pk_mul_f32 v[20:21], v[20:21], s[66:67] op_sel_hi:[1,0]\n"
    "v_pk_mul_f32 v[22:23], v[22:23], s[66:67] op_sel_hi:[1,0]\n"
    "v_pk_mul_f32 v[24:25], v[24:25], s[66:67] op_sel_hi:[1,0]\n"
    "v_pk_mul_f32 v[26:27], v[26:27], s[66:67] op_sel_hi:[1,0]\n"
    "v_cvt_pk_bf16_f32 v28, v20, v21\n"
    "v_cvt_pk_bf16_f32 v29, v22, v23\n"
    "v_cvt_pk_bf16_f32 v30, v24, v25\n"
    "v_cvt_pk_bf16_f32 v31, v26, v27\n"
    "v_cndmask_b32 v30, 0, v30, s[78:79]\n"
    "v_cndmask_b32 v31, 0, v31, s[78:79]\n"
    "s_branch .Lfkvst\n"
    ".LfisV:\n"
    "v_lshrrev_b32 v14, 3, v12\n"
    "v_lshrrev_b32 v15, 5, v241\n"
    "v_and_b32 v16, 7, v12\n"
    "v_lshl_add_u32 v16, v14, 4, v16\n"
    "v_lshl_add_u32 v11, v15, 3, v16\n"
    "v_mul_u32_u24 v14, 37, v11\n"
    "v_cmp_gt_u32 vcc, 28, v11\n"
    "v_lshrrev_b32 v14, 8, v14\n"
    "v_subrev_u32 v16, 28, v11\n"
    "v_mad_i32_i24 v15, v14, -7, v11\n"
    "v_cndmask_b32 v14, v16, v14, vcc\n"
    "v_cndmask_b32 v15, 7, v15, vcc\n"
    "v_lshrrev_b32 v16, 1, v14\n"
    "v_and_b32 v14, 1, v14\n"
    "v_lshrrev_b32 v17, 2, v15\n"
    "v_and_b32 v15, 3, v15\n"
    "v_lshl_add_u32 v15, v16, 4, v15\n"
    "v_lshl_add_u32 v15, v17, 3, v15\n"
    "v_lshl_add_u32 v15, v14, 2, v15\n"
    "v_and_b32 v14, 1, v13\n"
    "v_lshl_add_u32 v15, v14, 5, v15\n"
    "s_lshl_b32 s59, s59, 10\n"
    "s_add_u32 s59, s59, 2048\n"
    "v_mov_b32 v19, 0x3f803f80\n"
    "s_mov_b64 s[78:79], vcc\n"
    "v_lshl_add_u32 v9, v15, 4, s59\n"
    "s_branch .LfJ21\n"
    ".LfS15:\n"
    ".LfL22:\n"
    "ds_read_b32 v250, v243 offset:132\n"
    "s_add_u32 s49, s49, 1\n"
    "s_cmp_lt_u32 s49, 0x100000\n"
    "s_cselect_b32 s50, 0, -1\n"
    "s_waitcnt lgkmcnt(0)\n"
    "v_cmp_lt_u32 vcc, s50, v250\n"
    "s_cbranch_vccnz .LfL22\n"
    "s_branch .LfB16\n"
    ".LfS17:\n"
    ".LfL23:\n"
    "ds_read_b32 v250, v243 offset:136\n"
    "s_add_u32 s49, s49, 1\n"
    "s_cmp_lt_u32 s49, 0x100000\n"
    "s_cselect_b32 s50, 0, -1\n"
    "s_waitcnt lgkmcnt(0)\n"
    "v_cmp_lt_u32 vcc, s50, v250\n"
    "s_cbranch_vccnz .LfL23\n"
    "s_branch .LfB18\n"
    ".LfS19:\n"
    ".LfL24:\n"
    "ds_read_b32 v250, v243 offset:140\n"
    "s_add_u32 s49, s49, 1\n"
    "s_cmp_lt_u32 s49, 0x100000\n"
    "s_cselect_b32 s50, 0, -1\n"
    "s_waitcnt lgkmcnt(0)\n"
    "v_cmp_lt_u32 vcc, s50, v250\n"
    "s_cbranch_vccnz .LfL24\n"
    "s_branch .LfB20\n"
    ".LfJ21:\n"
    "s_waitcnt lgkmcnt(0)\n"
    "s_add_u32 s44, s44, 1\n"
    "s_addc_u32 s45, s45, 0\n"
    "s_waitcnt vmcnt(0)\n"
    "v_mov_b32 v250, s44\n"
    "v_mov_b32 v251, s45\n"
    "s_mov_b64 exec, 1\n"
    "buffer_store_dwordx2 v[250:251], off, s[24:27], s64 sc1\n"
    "s_mov_b64 exec, -1\n"
    ".LfL14:\n"
    "ds_read_b32 v250, v243 offset:128\n"
    "s_add_u32 s49, s49, 1\n"
    "s_cmp_lt_u32 s49, 0x100000\n"
    "s_cselect_b32 s50, 0, -1\n"
    "s_waitcnt lgkmcnt(0)\n"
    "v_cmp_lt_u32 vcc, s50, v250\n"
    "s_cbranch_vccnz .LfL14\n"
    "ds_read_b128 v[32:35], v6 offset:0\n"
    "ds_read_b64_tr_b16 v[36:37], v8 offset:0\n"
    "ds_read_b64_tr_b16 v[38:39], v8 offset:224\n"
    "ds_read_b64_tr_b16 v[40:41], v8 offset:16\n"
    "ds_read_b64_tr_b16 v[42:43], v8 offset:240\n"
    "ds_read_b128 v[44:47], v7 offset:0\n"
    "ds_read_b64_tr_b16 v[48:49], v8 offset:1792\n"
    "ds_read_b64_tr_b16 v[50:51], v8 offset:2016\n"
    "ds_read_b64_tr_b16 v[52:53], v8 offset:1808\n"
    "ds_read_b64_tr_b16 v[54:55], v8 offset:2032\n"
    "ds_read_b32 v251, v243 offset:132\n"
    "ds_read_b128 v[56:59], v6 offset:8192\n"
    "ds_read_b64_tr_b16 v[60:61], v8 offset:4096\n"
    "ds_read_b64_tr_b16 v[62:63], v8 offset:4320\n"
    "ds_read_b64_tr_b16 v[64:65], v8 offset:4112\n"
    "ds_read_b64_tr_b16 v[66:67], v8 offset:4336\n"
    "ds_read_b128 v[68:71], v7 offset:8192\n"
    "ds_read_b64_tr_b16 v[72:73], v8 offset:5888\n"
    "ds_read_b64_tr_b16 v[74:75], v8 offset:6112\n"
    "ds_read_b64_tr_b16 v[76:77], v8 offset:5904\n"
    "ds_read_b64_tr_b16 v[78:79], v8 offset:6128\n"
    "s_waitcnt lgkmcnt(15)\n"
    "v_mfma_f32_16x16x32_bf16 v[20:23], v[32:35], v[36:39], 0\n"
    "v_mfma_f32_16x16x32_bf16 v[24:27], v[32:35], v[40:43], 0\n"
    "s_waitcnt lgkmcnt(11)\n"
    "v_mfma_f32_16x16x32_bf16 v[20:23], v[44:47], v[48:51], v[20:23]\n"
    "v_mfma_f32_16x16x32_bf16 v[24:27], v[44:47], v[52:55], v[24:27]\n"
    "s_waitcnt lgkmcnt(10)\n"
    "v_cmp_ne_u32 vcc, 0, v251\n"
    "s_cbranch_vccnz .LfS15\n"
    ".LfB16:\n"
    "ds_read_b128 v[32:35], v6 offset:16384\n"
    "ds_read_b64_tr_b16 v[36:37], v8 offset:8192\n"
    "ds_read_b64_tr_b16 v[38:39], v8 offset:8416\n"
    "ds_read_b64_tr_b16 v[40:41], v8 offset:8208\n"
    "ds_read_b64_tr_b16 v[42:43], v8 offset:8432\n"
    "ds_read_b128 v[44:47], v7 offset:16384\n"
    "ds_read_b64_tr_b16 v[48:49], v8 offset:9984\n"
    "ds_read_b64_tr_b16 v[50:51], v8 offset:10208\n"
    "ds_read_b64_tr_b16 v[52:53], v8 offset:10000\n"
    "ds_read_b64_tr_b16 v[54:55], v8 offset:10224\n"
    "s_waitcnt lgkmcnt(15)\n"
    "v_mfma_f32_16x16x32_bf16 v[20:23], v[56:59], v[60:63], v[20:23]\n"
    "v_mfma_f32_16x16x32_bf16 v[24:27], v[56:59], v[64:67], v[24:27]\n"
    "s_waitcnt lgkmcnt(10)\n"
    "v_mfma_f32_16x16x32_bf16 v[20:23], v[68:71], v[72:75], v[20:23]\n"
    "v_mfma_f32_16x16x32_bf16 v[24:27], v[68:71], v[76:79], v[24:27]\n"
    "ds_read_b32 v251, v243 offset:136\n"
    "ds_read_b128 v[56:59], v6 offset:24576\n"
    "ds_read_b64_tr_b16 v[60:61], v8 offset:12288\n"
    "ds_read_b64_tr_b16 v[62:63], v8 offset:12512\n"
    "ds_read_b64_tr_b16 v[64:65], v8 offset:12304\n"
    "ds_read_b64_tr_b16 v[66:67], v8 offset:12528\n"
    "ds_read_b128 v[68:71], v7 offset:24576\n"
    "ds_read_b64_tr_b16 v[72:73], v8 offset:14080\n"
    "ds_read_b64_tr_b16 v[74:75], v8 offset:14304\n"
    "ds_read_b64_tr_b16 v[76:77], v8 offset:14096\n"
    "ds_read_b64_tr_b16 v[78:79], v8 offset:14320\n"
    "s_waitcnt lgkmcnt(15)\n"
    "v_mfma_f32_16x16x32_bf16 v[20:23], v[32:35], v[36:39], v[20:23]\n"
    "v_mfma_f32_16x16x32_bf16 v[24:27], v[32:35], v[40:43], v[24:27]\n"
    "s_waitcnt lgkmcnt(11)\n"
    "v_mfma_f32_16x16x32_bf16 v[20:23], v[44:47], v[48:51], v[20:23]\n"
    "v_mfma_f32_16x16x32_bf16 v[24:27], v[44:47], v[52:55], v[24:27]\n"
    "s_waitcnt lgkmcnt(10)\n"
    "v_cmp_ne_u32 vcc, 0, v251\n"
    "s_cbranch_vccnz .LfS17\n"
    ".LfB18:\n"
    "ds_read_b128 v[32:35], v6 offset:32768\n"
    "ds_read_b64_tr_b16 v[36:37], v8 offset:16384\n"
    "ds_read_b64_tr_b16 v[38:39], v8 offset:16608\n"
    "ds_read_b64_tr_b16 v[40:41], v8 offset:16400\n"
    "ds_read_b64_tr_b16 v[42:43], v8 offset:16624\n"
    "ds_read_b128 v[44:47], v7 offset:32768\n"
    "ds_read_b64_tr_b16 v[48:49], v8 offset:18176\n"
    "ds_read_b64_tr_b16 v[50:51], v8 offset:18400\n"
    "ds_read_b64_tr_b16 v[52:53], v8 offset:18192\n"
    "ds_read_b64_tr_b16 v[54:55], v8 offset:18416\n"
    "s_waitcnt lgkmcnt(15)\n"
    "v_mfma_f32_16x16x32_bf16 v[20:23], v[56:59], v[60:63], v[20:23]\n"
    "v_mfma_f32_16x16x32_bf16 v[24:27], v[56:59], v[64:67], v[24:27]\n"
    "s_waitcnt lgkmcnt(10)\n"
    "v_mfma_f32_16x16x32_bf16 v[20:23], v[68:71], v[72:75], v[20:23]\n"
    "v_mfma_f32_16x16x32_bf16 v[24:27], v[68:71], v[76:79], v[24:27]\n"
    "ds_read_b32 v251, v243 offset:140\n"
    "ds_read_b128 v[56:59], v6 offset:40960\n"
    "ds_read_b64_tr_b16 v[60:61], v8 offset:20480\n"
    "ds_read_b64_tr_b16 v[62:63], v8 offset:20704\n"
    "ds_read_b64_tr_b16 v[64:65], v8 offset:20496\n"
    "ds_read_b64_tr_b16 v[66:67], v8 offset:20720\n"
    "ds_read_b128 v[68:71], v7 offset:40960\n"
    "ds_read_b64_tr_b16 v[72:73], v8 offset:22272\n"
    "ds_read_b64_tr_b16 v[74:75], v8 offset:22496\n"
    "ds_read_b64_tr_b16 v[76:77], v8 offset:22288\n"
    "ds_read_b64_tr_b16 v[78:79], v8 offset:22512\n"
    "s_waitcnt lgkmcnt(15)\n"
    "v_mfma_f32_16x16x32_bf16 v[20:23], v[32:35], v[36:39], v[20:23]\n"
    "v_mfma_f32_16x16x32_bf16 v[24:27], v[32:35], v[40:43], v[24:27]\n"
    "s_waitcnt lgkmcnt(11)\n"
    "v_mfma_f32_16x16x32_bf16 v[20:23], v[44:47], v[48:51], v[20:23]\n"
    "v_mfma_f32_16x16x32_bf16 v[24:27], v[44:47], v[52:55], v[24:27]\n"
    "s_waitcnt lgkmcnt(10)\n"
    "v_cmp_ne_u32 vcc, 0, v251\n"
    "s_cbranch_vccnz .LfS19\n"
    ".LfB20:\n"
    "ds_read_b128 v[32:35], v6 offset:49152\n"
    "ds_read_b64_tr_b16 v[36:37], v8 offset:24576\n"
    "ds_read_b64_tr_b16 v[38:39], v8 offset:24800\n"
    "ds_read_b64_tr_b16 v[40:41], v8 offset:24592\n"
    "ds_read_b64_tr_b16 v[42:43], v8 offset:24816\n"
    "ds_read_b128 v[44:47], v7 offset:49152\n"
    "ds_read_b64_tr_b16 v[48:49], v8 offset:26368\n"
    "ds_read_b64_tr_b16 v[50:51], v8 offset:26592\n"
    "ds_read_b64_tr_b16 v[52:53], v8 offset:26384\n"
    "ds_read_b64_tr_b16 v[54:55], v8 offset:26608\n"
    "ds_add_u32 v245, v244 offset:0\n"
    "s_waitcnt lgkmcnt(15)\n"
    "v_mfma_f32_16x16x32_bf16 v[20:23], v[56:59], v[60:63], v[20:23]\n"
    "v_mfma_f32_16x16x32_bf16 v[24:27], v[56:59], v[64:67], v[24:27]\n"
    "s_waitcnt lgkmcnt(11)\n"
    "v_mfma_f32_16x16x32_bf16 v[20:23], v[68:71], v[72:75], v[20:23]\n"
    "v_mfma_f32_16x16x32_bf16 v[24:27], v[68:71], v[76:79], v[24:27]\n"
    "s_waitcnt lgkmcnt(8)\n"
    "v_mfma_f32_16x16x32_bf16 v[20:23], v[32:35], v[36:39], v[20:23]\n"
    "s_waitcnt lgkmcnt(6)\n"
    "v_mfma_f32_16x16x32_bf16 v[24:27], v[32:35], v[40:43], v[24:27]\n"
    "s_waitcnt lgkmcnt(3)\n"
    "v_mfma_f32_16x16x32_bf16 v[20:23], v[44:47], v[48:51], v[20:23]\n"
    "s_waitcnt lgkmcnt(1)\n"
    "v_mfma_f32_16x16x32_bf16 v[24:27], v[44:47], v[52:55], v[24:27]\n"
    "s_nop 5\n"
    "v_cvt_pk_bf16_f32 v28, v20, v21\n"
    "v_cvt_pk_bf16_f32 v29, v22, v23\n"
    "v_cvt_pk_bf16_f32 v30, v24, v25\n"
    "v_cvt_pk_bf16_f32 v31, v26, v27\n"
    "s_nop 0\n"
    "v_permlane32_swap_b32 v28, v30\n"
    "v_permlane32_swap_b32 v29, v31\n"
    "v_cndmask_b32 v28, v19, v28, s[78:79]\n"
    "v_cndmask_b32 v29, v19, v29, s[78:79]\n"
    "v_cndmask_b32 v30, v19, v30, s[78:79]\n"
    "v_cndmask_b32 v31, v19, v31, s[78:79]\n"
    ".Lfkvst:\n"
    "v_min_u32 v28, -2, v28\n"
    "v_min_u32 v29, -2, v29\n"
    "v_min_u32 v30, -2, v30\n"
    "v_min_u32 v31, -2, v31\n"
    "buffer_store_dwordx4 v[28:31], v9, s[24:27], s62 offen sc1\n"
    "buffer_load_dwordx2 v[2:3], v1, s[24:27], 0 offen sc1\n"
    "s_lshr_b32 s82, s2, 3\n"
    "s_mul_i32 s58, s82, 37\n"
    "s_lshr_b32 s58, s58, 8\n"
    "s_mul_i32 s58, s58, 7\n"
    "s_sub_u32 s82, s82, s58\n"
    "s_lshl_b32 s59, s40, 13\n"
    "s_add_u32 s59, s59, 0x40000\n"
    "s_and_b32 s58, s2, 7\n"
    "s_lshl_b32 s58, s58, 15\n"
    "s_add_u32 s84, s58, s59\n"
    "s_add_u32 s58, s82, 1\n"
    "s_cmp_gt_u32 s58, 7\n"
    "s_addc_u32 s58, s58, s2\n"
    "s_and_b32 s58, s58, 7\n"
    "s_lshl_b32 s58, s58, 15\n"
    "s_add_u32 s85, s58, s59\n"
    "s_add_u32 s58, s82, 2\n"
    "s_cmp_gt_u32 s58, 7\n"
    "s_addc_u32 s58, s58, s2\n"
    "s_and_b32 s58, s58, 7\n"
    "s_lshl_b32 s58, s58, 15\n"
    "s_add_u32 s86, s58, s59\n"
    "s_add_u32 s58, s82, 3\n"
    "s_cmp_gt_u32 s58, 7\n"
    "s_addc_u32 s58, s58, s2\n"
    "s_and_b32 s58, s58, 7\n"
    "s_lshl_b32 s58, s58, 15\n"
    "s_add_u32 s87, s58, s59\n"
    "s_add_u32 s58, s82, 4\n"
    "s_cmp_gt_u32 s58, 7\n"
    "s_addc_u32 s58, s58, s2\n"
    "s_and_b32 s58, s58, 7\n"
    "s_lshl_b32 s58, s58, 15\n"
    "s_add_u32 s88, s58, s59\n"
    "s_add_u32 s58, s82, 5\n"
    "s_cmp_gt_u32 s58, 7\n"
    "s_addc_u32 s58, s58, s2\n"
    "s_and_b32 s58, s58, 7\n"
    "s_lshl_b32 s58, s58, 15\n"
    "s_add_u32 s89, s58, s59\n"
    "s_add_u32 s58, s82, 6\n"
    "s_cmp_gt_u32 s58, 7\n"
    "s_addc_u32 s58, s58, s2\n"
    "s_and_b32 s58, s58, 7\n"
    "s_lshl_b32 s58, s58, 15\n"
    "s_add_u32 s90, s58, s59\n"
    "s_add_u32 s58, s82, 7\n"
    "s_cmp_gt_u32 s58, 7\n"
    "s_addc_u32 s58, s58, s2\n"
    "s_and_b32 s58, s58, 7\n"
    "s_lshl_b32 s58, s58, 15\n"
    "s_add_u32 s91, s58, s59\n"
    "v_lshrrev_b32 v25, 3, v241\n"
    "v_and_b32 v26, 7, v241\n"
    "v_add_u32 v27, s2, v25\n"
    "s_lshl_b32 s58, s40, 10\n"
    "v_and_b32 v27, 7, v27\n"
    "s_add_u32 s58, s58, 0x8000\n"
    "v_lshl_add_u32 v27, v27, 5, v26\n"
    "v_lshl_add_u32 v13, v27, 7, s58\n"
    "v_add_u32 v12, 4096, v242\n"
    "s_lshl_b32 s58, s40, 14\n"
    "s_add_u32 s58, s58, 0xa800\n"
    "v_add_u32 v249, s58, v242\n"
    "s_lshl_b32 s58, s40, 6\n"
    "s_add_u32 s58, s58, s42\n"
    "s_lshl_b32 s58, s58, 7\n"
    "s_add_u32 s58, s58, 0x30000\n"
    "v_lshl_add_u32 v16, v241, 10, s58\n"
    "v_lshrrev_b32 v25, 2, v241\n"
    "v_and_b32 v26, 3, v241\n"
    "v_lshlrev_b32 v25, 13, v25\n"
    "v_lshl_add_u32 v17, v26, 4, v25\n"
    "v_mov_b32 v11, s54\n"
    "s_lshl_b32 s83, s43, 7\n"
    "s_add_u32 s83, s83, 0x10000\n"
    "s_waitcnt vmcnt(0)\n"
    "v_cmp_ne_u64 vcc, s[44:45], v[2:3]\n"
    "s_cbranch_vccnz .LfS25\n"
    ".LfB26:\n"
    "ds_add_u32 v245, v244 offset:44\n"
    ".LfL27:\n"
    "ds_read_b32 v250, v243 offset:28\n"
    "s_add_u32 s49, s49, 1\n"
    "s_cmp_lt_u32 s49, 0x100000\n"
    "s_cselect_b32 s50, 7, 0\n"
    "s_waitcnt lgkmcnt(0)\n"
    "v_cmp_gt_u32 vcc, s50, v250\n"
    "s_cbranch_vccnz .LfL27\n"
    ".LfL28:\n"
    "ds_read_b32 v250, v243 offset:160\n"
    "s_add_u32 s49, s49, 1\n"
    "s_cmp_lt_u32 s49, 0x100000\n"
    "s_cselect_b32 s50, 0, -1\n"
    "s_waitcnt lgkmcnt(0)\n"
    "v_cmp_lt_u32 vcc, s50, v250\n"
    "s_cbranch_vccnz .LfL28\n"
    "v_mov_b32 v250, s44\n"
    "v_mov_b32 v251, s45\n"
    "s_mov_b64 exec, 1\n"
    "buffer_store_dwordx2 v[250:251], off, s[24:27], s63 sc1\n"
    "s_mov_b64 exec, -1\n"
    ".LfL29:\n"
    "ds_read_b32 v250, v243 offset:172\n"
    "s_add_u32 s49, s49, 1\n"
    "s_cmp_lt_u32 s49, 0x100000\n"
    "s_cselect_b32 s50, 0, -1\n"
    "s_waitcnt lgkmcnt(0)\n"
    "v_cmp_lt_u32 vcc, s50, v250\n"
    "s_cbranch_vccnz .LfL29\n"
    "s_sub_u32 s58, s42, 1\n"
    "s_cmp_lt_u32 s58, 6\n"
    "s_cbranch_scc0 .LfL30\n"
    ".LfL30b:\n"
    "s_sleep 1\n"
    "buffer_load_dwordx4 v[176:179], v242, s[24:27], s84 offen offset:0 sc1\n"
    "buffer_load_dwordx4 v[180:183], v242, s[24:27], s84 offen offset:1024 sc1\n"
    "buffer_load_dwordx4 v[208:211], v242, s[24:27], s84 offen offset:2048 sc1\n"
    "buffer_load_dwordx4 v[212:215], v242, s[24:27], s84 offen offset:3072 sc1\n"
    "buffer_load_dwordx4 v[184:187], v12, s[24:27], s84 offen offset:0 sc1\n"
    "buffer_load_dwordx4 v[188:191], v12, s[24:27], s84 offen offset:1024 sc1\n"
    "buffer_load_dwordx4 v[216:219], v12, s[24:27], s84 offen offset:2048 sc1\n"
    "buffer_load_dwordx4 v[220:223], v12, s[24:27], s84 offen offset:3072 sc1\n"
    "s_sleep 2\n"
    "s_mov_b32 m0, 0x22c00\n"
    "s_nop 0\n"
    "buffer_load_dwordx4 v88, s[4:7], s68 offen offset:0 lds\n"
    "buffer_load_dwordx4 v89, s[4:7], s68 offen offset:1024 lds\n"
    "buffer_load_dwordx4 v90, s[4:7], s68 offen offset:2048 lds\n"
    "buffer_load_dwordx4 v91, s[4:7], s68 offen offset:3072 lds\n"
    "s_mov_b32 m0, 0x23c00\n"
    "s_nop 0\n"
    "buffer_load_dwordx4 v88, s[4:7], s69 offen offset:0 lds\n"
    "buffer_load_dwordx4 v89, s[4:7], s69 offen offset:1024 lds\n"
    "buffer_load_dwordx4 v90, s[4:7], s69 offen offset:2048 lds\n"
    "buffer_load_dwordx4 v91, s[4:7], s69 offen offset:3072 lds\n"
    "v_mov_b32 v29, s54\n"
    "s_mov_b32 m0, 0x27db0\n"
    "s_mov_b64 exec, 1\n"
    "buffer_load_dword v29, s[4:7], 0 offen lds\n"
    "s_mov_b64 exec, -1\n"
    "v_mov_b32 v160, 0\n"
    "v_mov_b32 v161, 0\n"
    "v_mov_b32 v162, 0\n"
    "v_mov_b32 v163, 0\n"
    "v_mov_b32 v240, 0xdddddddd\n"
    "v_mov_b32 v252, s44\n"
    "v_mov_b32 v253, s45\n"
    "v_mfma_f32_32x32x16_bf16 v[64:79], v[160:163], v[160:163], 0\n"
    "v_mfma_f32_32x32x16_bf16 v[80:95], v[160:163], v[160:163], 0\n"
    "v_mfma_f32_32x32x16_bf16 v[96:111], v[160:163], v[160:163], 0\n"
    "v_mfma_f32_32x32x16_bf16 v[112:127], v[160:163], v[160:163], 0\n"
    "s_waitcnt vmcnt(9)\n"
    ".LfCE:\n"
    "v_max3_u32 v250, v176, v177, v178\n"
    "v_max3_u32 v250, v250, v179, v180\n"
    "v_max3_u32 v250, v250, v181, v182\n"
    "v_max3_u32 v250, v250, v183, v184\n"
    "v_max3_u32 v250, v250, v185, v186\n"
    "v_max3_u32 v250, v250, v187, v188\n"
    "v_max3_u32 v250, v250, v189, v190\n"
    "v_max_u32 v250, v250, v191\n"
    "v_max3_u32 v251, v208, v209, v210\n"
    "v_max3_u32 v251, v251, v211, v212\n"
    "v_max3_u32 v251, v251, v213, v214\n"
    "v_max3_u32 v251, v251, v215, v216\n"
    "v_max3_u32 v251, v251, v217, v218\n"
    "v_max3_u32 v251, v251, v219, v220\n"
    "v_max3_u32 v251, v251, v221, v222\n"
    "v_max_u32 v251, v251, v223\n"
    "v_max_u32 v250, v250, v251\n"
    "v_cmp_eq_u32 vcc, -1, v250\n"
    "s_nop 0\n"
    "s_mov_b64 s[78:79], vcc\n"
    ".LfL31:\n"
    "ds_read_b32 v250, v243 offset:4\n"
    "s_add_u32 s49, s49, 1\n"
    "s_cmp_lt_u32 s49, 0x100000\n"
    "s_cselect_b32 s50, 2, 0\n"
    "s_waitcnt lgkmcnt(0)\n"
    "v_cmp_gt_u32 vcc, s50, v250\n"
    "s_cbranch_vccnz .LfL31\n"
    "ds_read_b128 v[32:35], v242 offset:34816\n"
    "ds_read_b128 v[36:39], v242 offset:35840\n"
    "ds_read_b128 v[40:43], v242 offset:38912\n"
    "ds_read_b128 v[44:47], v242 offset:39936\n"
    "ds_read_b128 v[48:51], v242 offset:36864\n"
    "ds_read_b128 v[52:55], v242 offset:37888\n"
    "ds_read_b128 v[56:59], v242 offset:40960\n"
    "ds_read_b128 v[60:63], v242 offset:41984\n"
    "s_mov_b64 vcc, s[78:79]\n"
    "s_branch .LfCHK3\n"
    ".LfS25:\n"
    "s_mov_b64 exec, vcc\n"
    "s_sleep 4\n"
    "buffer_load_dwordx2 v[2:3], v1, s[24:27], 0 offen sc1\n"
    "s_add_u32 s49, s49, 1\n"
    "s_cmp_lt_u32 s49, 0x100000\n"
    "s_cselect_b64 s[52:53], -1, 0\n"
    "s_waitcnt vmcnt(0)\n"
    "v_cmp_ne_u64 vcc, s[44:45], v[2:3]\n"
    "s_nop 0\n"
    "s_and_b64 vcc, vcc, s[52:53]\n"
    "s_cbranch_scc1 .LfS25\n"
    "s_mov_b64 exec, -1\n"
    "s_branch .LfB26\n"
    ".LfL30:\n"
    "s_sleep 8\n"
    "s_branch .LfL30b\n"
    ".LfQ:\n"
    "s_setprio 3\n"
    "v_mov_b32 v244, 1\n"
    "v_lshl_add_u32 v245, v241, 20, v243\n"
    "s_mov_b32 s49, 0\n"
    "s_bfm_b64 s[80:81], 32, 0\n"
    "s_bfm_b64 s[92:93], 8, 0\n"
    "s_mov_b32 s22, 0x380000\n"
    "s_mov_b32 s23, 0x20000\n"
    "v_lshrrev_b32 v13, 5, v241\n"
    "s_and_b32 s59, s40, 1\n"
    "v_and_b32 v12, 31, v241\n"
    "v_lshl_add_u32 v14, s59, 5, v12\n"
    "v_bfe_u32 v15, v14, 1, 3\n"
    "v_xor_b32 v15, v15, v13\n"
    "v_lshlrev_b32 v15, 4, v15\n"
    "v_lshl_add_u32 v2, v14, 7, v15\n"
    "v_add_u32 v2, 0x8800, v2\n"
    "v_xor_b32 v3, 32, v2\n"
    "v_xor_b32 v4, 64, v2\n"
    "v_xor_b32 v5, 0x60, v2\n"
    "v_and_b32 v14, 12, v241\n"
    "v_lshl_add_u32 v14, v13, 1, v14\n"
    "v_mul_u32_u24 v14, 112, v14\n"
    "v_and_b32 v15, 16, v241\n"
    "v_lshl_add_u32 v14, v15, 1, v14\n"
    "v_and_b32 v15, 3, v241\n"
    "v_lshl_add_u32 v14, v15, 3, v14\n"
    "v_add_u32 v6, 0x16800, v14\n"
    "v_lshl_add_u32 v7, s59, 12, v242\n"
    "s_lshr_b32 s82, s2, 3\n"
    "s_mul_i32 s58, s82, 37\n"
    "s_lshr_b32 s58, s58, 8\n"
    "s_mul_i32 s58, s58, 7\n"
    "s_sub_u32 s82, s82, s58\n"
    "s_lshl_b32 s59, s40, 13\n"
    "s_add_u32 s59, s59, 0x40000\n"
    "s_and_b32 s58, s2, 7\n"
    "s_lshl_b32 s58, s58, 15\n"
    "s_add_u32 s84, s58, s59\n"
    "s_add_u32 s58, s82, 1\n"
    "s_cmp_gt_u32 s58, 7\n"
    "s_addc_u32 s58, s58, s2\n"
    "s_and_b32 s58, s58, 7\n"
    "s_lshl_b32 s58, s58, 15\n"
    "s_add_u32 s85, s58, s59\n"
    "s_add_u32 s58, s82, 2\n"
    "s_cmp_gt_u32 s58, 7\n"
    "s_addc_u32 s58, s58, s2\n"
    "s_and_b32 s58, s58, 7\n"
    "s_lshl_b32 s58, s58, 15\n"
    "s_add_u32 s86, s58, s59\n"
    "s_add_u32 s58, s82, 3\n"
    "s_cmp_gt_u32 s58, 7\n"
    "s_addc_u32 s58, s58, s2\n"
    "s_and_b32 s58, s58, 7\n"
    "s_lshl_b32 s58, s58, 15\n"
    "s_add_u32 s87, s58, s59\n"
    "s_add_u32 s58, s82, 4\n"
    "s_cmp_gt_u32 s58, 7\n"
    "s_addc_u32 s58, s58, s2\n"
    "s_and_b32 s58, s58, 7\n"
    "s_lshl_b32 s58, s58, 15\n"
    "s_add_u32 s88, s58, s59\n"
    "s_add_u32 s58, s82, 5\n"
    "s_cmp_gt_u32 s58, 7\n"
    "s_addc_u32 s58, s58, s2\n"
    "s_and_b32 s58, s58, 7\n"
    "s_lshl_b32 s58, s58, 15\n"
    "s_add_u32 s89, s58, s59\n"
    "s_add_u32 s58, s82, 6\n"
    "s_cmp_gt_u32 s58, 7\n"
    "s_addc_u32 s58, s58, s2\n"
    "s_and_b32 s58, s58, 7\n"
    "s_lshl_b32 s58, s58, 15\n"
    "s_add_u32 s90, s58, s59\n"
    "s_add_u32 s58, s82, 7\n"
    "s_cmp_gt_u32 s58, 7\n"
    "s_addc_u32 s58, s58, s2\n"
    "s_and_b32 s58, s58, 7\n"
    "s_lshl_b32 s58, s58, 15\n"
    "s_add_u32 s91, s58, s59\n"
    "v_lshrrev_b32 v25, 3, v241\n"
    "v_and_b32 v26, 7, v241\n"
    "v_add_u32 v27, s2, v25\n"
    "s_lshl_b32 s58, s40, 10\n"
    "v_and_b32 v27, 7, v27\n"
    "s_add_u32 s58, s58, 0x8000\n"
    "v_lshl_add_u32 v27, v27, 5, v26\n"
    "v_lshl_add_u32 v13, v27, 7, s58\n"
    "v_add_u32 v12, 4096, v242\n"
    "s_lshl_b32 s58, s40, 14\n"
    "s_add_u32 s58, s58, 0xa800\n"
    "v_add_u32 v249, s58, v242\n"
    "s_lshl_b32 s58, s40, 6\n"
    "s_add_u32 s58, s58, s42\n"
    "s_lshl_b32 s58, s58, 7\n"
    "s_add_u32 s58, s58, 0x30000\n"
    "v_lshl_add_u32 v16, v241, 10, s58\n"
    "v_lshrrev_b32 v25, 2, v241\n"
    "v_and_b32 v26, 3, v241\n"
    "v_lshlrev_b32 v25, 13, v25\n"
    "v_lshl_add_u32 v17, v26, 4, v25\n"
    "v_mov_b32 v11, s54\n"
    "s_lshl_b32 s83, s43, 7\n"
    "s_add_u32 s83, s83, 0x10000\n"
    "s_branch .LfJ48\n"
    ".LfS33:\n"
    ".LfL49:\n"
    "ds_read_b32 v250, v243 offset:148\n"
    "s_add_u32 s49, s49, 1\n"
    "s_cmp_lt_u32 s49, 0x100000\n"
    "s_cselect_b32 s50, 0, -1\n"
    "s_waitcnt lgkmcnt(0)\n"
    "v_cmp_lt_u32 vcc, s50, v250\n"
    "s_cbranch_vccnz .LfL49\n"
    "s_branch .LfB34\n"
    ".LfS35:\n"
    ".LfL50:\n"
    "ds_read_b32 v250, v243 offset:152\n"
    "s_add_u32 s49, s49, 1\n"
    "s_cmp_lt_u32 s49, 0x100000\n"
    "s_cselect_b32 s50, 0, -1\n"
    "s_waitcnt lgkmcnt(0)\n"
    "v_cmp_lt_u32 vcc, s50, v250\n"
    "s_cbranch_vccnz .LfL50\n"
    "s_branch .LfB36\n"
    ".LfS37:\n"
    ".LfL51:\n"
    "ds_read_b32 v250, v243 offset:156\n"
    "s_add_u32 s49, s49, 1\n"
    "s_cmp_lt_u32 s49, 0x100000\n"
    "s_cselect_b32 s50, 0, -1\n"
    "s_waitcnt lgkmcnt(0)\n"
    "v_cmp_lt_u32 vcc, s50, v250\n"
    "s_cbranch_vccnz .LfL51\n"
    "s_branch .LfB38\n"
    ".LfS39:\n"
    ".LfL52:\n"
    "ds_read_b32 v250, v243 offset:160\n"
    "s_add_u32 s49, s49, 1\n"
    "s_cmp_lt_u32 s49, 0x100000\n"
    "s_cselect_b32 s50, 0, -1\n"
    "s_waitcnt lgkmcnt(0)\n"
    "v_cmp_lt_u32 vcc, s50, v250\n"
    "s_cbranch_vccnz .LfL52\n"
    "s_branch .LfB40\n"
    ".LfS41:\n"
    ".LfL53:\n"
    "ds_read_b32 v250, v243 offset:164\n"
    "s_add_u32 s49, s49, 1\n"
    "s_cmp_lt_u32 s49, 0x100000\n"
    "s_cselect_b32 s50, 0, -1\n"
    "s_waitcnt lgkmcnt(0)\n"
    "v_cmp_lt_u32 vcc, s50, v250\n"
    "s_cbranch_vccnz .LfL53\n"
    "s_branch .LfB42\n"
    ".LfS43:\n"
    ".LfL54:\n"
    "ds_read_b32 v250, v243 offset:168\n"
    "s_add_u32 s49, s49, 1\n"
    "s_cmp_lt_u32 s49, 0x100000\n"
    "s_cselect_b32 s50, 0, -1\n"
    "s_waitcnt lgkmcnt(0)\n"
    "v_cmp_lt_u32 vcc, s50, v250\n"
    "s_cbranch_vccnz .LfL54\n"
    "s_branch .LfB44\n"
    ".LfS45:\n"
    ".LfL55:\n"
    "ds_read_b32 v250, v243 offset:172\n"
    "s_add_u32 s49, s49, 1\n"
    "s_cmp_lt_u32 s49, 0x100000\n"
    "s_cselect_b32 s50, 0, -1\n"
    "s_waitcnt lgkmcnt(0)\n"
    "v_cmp_lt_u32 vcc, s50, v250\n"
    "s_cbranch_vccnz .LfL55\n"
    "s_branch .LfB46\n"
    ".LfCKS:\n"
    ".LfL56:\n"
    "ds_read_b32 v250, v243 offset:44\n"
    "s_add_u32 s49, s49, 1\n"
    "s_cmp_lt_u32 s49, 0x100000\n"
    "s_cselect_b32 s50, 1, 0\n"
    "s_waitcnt lgkmcnt(0)\n"
    "v_cmp_gt_u32 vcc, s50, v250\n"
    "s_cbranch_vccnz .LfL56\n"
    "s_branch .LfCKB\n"
    ".LfL47:\n"
    "s_sleep 4\n"
    "s_branch .LfL47b\n"
    ".LfJ48:\n"
    "s_waitcnt lgkmcnt(0)\n"
    "s_add_u32 s44, s44, 1\n"
    "s_addc_u32 s45, s45, 0\n"
    "ds_add_u32 v245, v244 offset:28\n"
    "v_mov_b32 v160, 0\n"
    "v_mov_b32 v161, 0\n"
    "v_mov_b32 v162, 0\n"
    "v_mov_b32 v163, 0\n"
    "v_mov_b32 v240, 0xdddddddd\n"
    "v_mov_b32 v252, s44\n"
    "v_mov_b32 v253, s45\n"
    "v_mfma_f32_32x32x16_bf16 v[64:79], v[160:163], v[160:163], 0\n"
    "v_mfma_f32_32x32x16_bf16 v[80:95], v[160:163], v[160:163], 0\n"
    "v_mfma_f32_32x32x16_bf16 v[96:111], v[160:163], v[160:163], 0\n"
    "v_mfma_f32_32x32x16_bf16 v[112:127], v[160:163], v[160:163], 0\n"
    ".LfL32:\n"
    "ds_read_b32 v250, v243 offset:144\n"
    "s_add_u32 s49, s49, 1\n"
    "s_cmp_lt_u32 s49, 0x100000\n"
    "s_cselect_b32 s50, 0, -1\n"
    "s_waitcnt lgkmcnt(0)\n"
    "v_cmp_lt_u32 vcc, s50, v250\n"
    "s_cbranch_vccnz .LfL32\n"
    "ds_read_b128 v[32:35], v2 offset:0\n"
    "ds_read_b64_tr_b16 v[36:37], v6 offset:0\n"
    "ds_read_b64_tr_b16 v[38:39], v6 offset:112\n"
    "ds_read_b64_tr_b16 v[40:41], v6 offset:56\n"
    "ds_read_b64_tr_b16 v[42:43], v6 offset:168\n"
    "ds_read_b128 v[44:47], v3 offset:0\n"
    "ds_read_b64_tr_b16 v[48:49], v6 offset:1792\n"
    "ds_read_b64_tr_b16 v[50:51], v6 offset:1904\n"
    "ds_read_b64_tr_b16 v[52:53], v6 offset:1848\n"
    "ds_read_b64_tr_b16 v[54:55], v6 offset:1960\n"
    "ds_read_b128 v[164:167], v4 offset:0\n"
    "ds_read_b64_tr_b16 v[168:169], v6 offset:3584\n"
    "ds_read_b64_tr_b16 v[170:171], v6 offset:3696\n"
    "ds_read_b64_tr_b16 v[172:173], v6 offset:3640\n"
    "ds_read_b64_tr_b16 v[174:175], v6 offset:3752\n"
    "ds_read_b32 v251, v243 offset:148\n"
    "s_waitcnt lgkmcnt(11)\n"
    "v_mfma_f32_32x32x16_bf16 v[128:143], v[36:39], v[32:35], 0\n"
    "ds_read_b128 v[192:195], v5 offset:0\n"
    "ds_read_b64_tr_b16 v[196:197], v6 offset:5376\n"
    "ds_read_b64_tr_b16 v[198:199], v6 offset:5488\n"
    "v_mfma_f32_32x32x16_bf16 v[144:159], v[40:43], v[32:35], 0\n"
    "ds_read_b64_tr_b16 v[200:201], v6 offset:5432\n"
    "ds_read_b64_tr_b16 v[202:203], v6 offset:5544\n"
    "s_waitcnt lgkmcnt(5)\n"
    "v_cmp_ne_u32 vcc, 0, v251\n"
    "s_cbranch_vccnz .LfS33\n"
    ".LfB34:\n"
    "v_mfma_f32_32x32x16_bf16 v[128:143], v[48:51], v[44:47], v[128:143]\n"
    "ds_read_b128 v[224:227], v2 offset:8192\n"
    "ds_read_b64_tr_b16 v[228:229], v6 offset:7168\n"
    "ds_read_b64_tr_b16 v[230:231], v6 offset:7280\n"
    "v_mfma_f32_32x32x16_bf16 v[144:159], v[52:55], v[44:47], v[144:159]\n"
    "ds_read_b64_tr_b16 v[232:233], v6 offset:7224\n"
    "ds_read_b64_tr_b16 v[234:235], v6 offset:7336\n"
    "v_mfma_f32_32x32x16_bf16 v[128:143], v[168:171], v[164:167], v[128:143]\n"
    "ds_read_b128 v[32:35], v3 offset:8192\n"
    "ds_read_b64_tr_b16 v[36:37], v6 offset:8960\n"
    "ds_read_b64_tr_b16 v[38:39], v6 offset:9072\n"
    "v_mfma_f32_32x32x16_bf16 v[144:159], v[172:175], v[164:167], v[144:159]\n"
    "ds_read_b64_tr_b16 v[40:41], v6 offset:9016\n"
    "ds_read_b64_tr_b16 v[42:43], v6 offset:9128\n"
    "s_waitcnt lgkmcnt(10)\n"
    "v_mfma_f32_32x32x16_bf16 v[128:143], v[196:199], v[192:195], v[128:143]\n"
    "ds_read_b128 v[44:47], v4 offset:8192\n"
    "ds_read_b64_tr_b16 v[48:49], v6 offset:10752\n"
    "ds_read_b64_tr_b16 v[50:51], v6 offset:10864\n"
    "v_mfma_f32_32x32x16_bf16 v[144:159], v[200:203], v[192:195], v[144:159]\n"
    "ds_read_b64_tr_b16 v[52:53], v6 offset:10808\n"
    "ds_read_b64_tr_b16 v[54:55], v6 offset:10920\n"
    "ds_read_b32 v251, v243 offset:152\n"
    "s_waitcnt lgkmcnt(11)\n"
    "v_mfma_f32_32x32x16_bf16 v[128:143], v[228:231], v[224:227], v[128:143]\n"
    "ds_read_b128 v[164:167], v5 offset:8192\n"
    "ds_read_b64_tr_b16 v[168:169], v6 offset:12544\n"
    "ds_read_b64_tr_b16 v[170:171], v6 offset:12656\n"
    "v_mfma_f32_32x32x16_bf16 v[144:159], v[232:235], v[224:227], v[144:159]\n"
    "ds_read_b64_tr_b16 v[172:173], v6 offset:12600\n"
    "ds_read_b64_tr_b16 v[174:175], v6 offset:12712\n"
    "s_waitcnt lgkmcnt(5)\n"
    "v_cmp_ne_u32 vcc, 0, v251\n"
    "s_cbranch_vccnz .LfS35\n"
    ".LfB36:\n"
    "v_mfma_f32_32x32x16_bf16 v[128:143], v[36:39], v[32:35], v[128:143]\n"
    "ds_read_b128 v[192:195], v2 offset:16384\n"
    "ds_read_b64_tr_b16 v[196:197], v6 offset:14336\n"
    "ds_read_b64_tr_b16 v[198:199], v6 offset:14448\n"
    "v_mfma_f32_32x32x16_bf16 v[144:159], v[40:43], v[32:35], v[144:159]\n"
    "ds_read_b64_tr_b16 v[200:201], v6 offset:14392\n"
    "ds_read_b64_tr_b16 v[202:203], v6 offset:14504\n"
    "v_mfma_f32_32x32x16_bf16 v[128:143], v[48:51], v[44:47], v[128:143]\n"
    "ds_read_b128 v[224:227], v3 offset:16384\n"
    "ds_read_b64_tr_b16 v[228:229], v6 offset:16128\n"
    "ds_read_b64_tr_b16 v[230:231], v6 offset:16240\n"
    "v_mfma_f32_32x32x16_bf16 v[144:159], v[52:55], v[44:47], v[144:159]\n"
    "ds_read_b64_tr_b16 v[232:233], v6 offset:16184\n"
    "ds_read_b64_tr_b16 v[234:235], v6 offset:16296\n"
    "s_waitcnt lgkmcnt(10)\n"
    "v_mfma_f32_32x32x16_bf16 v[128:143], v[168:171], v[164:167], v[128:143]\n"
    "ds_read_b128 v[32:35], v4 offset:16384\n"
    "ds_read_b64_tr_b16 v[36:37], v6 offset:17920\n"
    "ds_read_b64_tr_b16 v[38:39], v6 offset:18032\n"
    "v_mfma_f32_32x32x16_bf16 v[144:159], v[172:175], v[164:167], v[144:159]\n"
    "ds_read_b64_tr_b16 v[40:41], v6 offset:17976\n"
    "ds_read_b64_tr_b16 v[42:43], v6 offset:18088\n"
    "ds_read_b32 v251, v243 offset:156\n"
    "s_waitcnt lgkmcnt(11)\n"
    "v_mfma_f32_32x32x16_bf16 v[128:143], v[196:199], v[192:195], v[128:143]\n"
    "ds_read_b128 v[44:47], v5 offset:16384\n"
    "ds_read_b64_tr_b16 v[48:49], v6 offset:19712\n"
    "ds_read_b64_tr_b16 v[50:51], v6 offset:19824\n"
    "v_mfma_f32_32x32x16_bf16 v[144:159], v[200:203], v[192:195], v[144:159]\n"
    "ds_read_b64_tr_b16 v[52:53], v6 offset:19768\n"
    "ds_read_b64_tr_b16 v[54:55], v6 offset:19880\n"
    "s_waitcnt lgkmcnt(5)\n"
    "v_cmp_ne_u32 vcc, 0, v251\n"
    "s_cbranch_vccnz .LfS37\n"
    ".LfB38:\n"
    "v_mfma_f32_32x32x16_bf16 v[128:143], v[228:231], v[224:227], v[128:143]\n"
    "ds_read_b128 v[164:167], v2 offset:24576\n"
    "ds_read_b64_tr_b16 v[168:169], v6 offset:21504\n"
    "ds_read_b64_tr_b16 v[170:171], v6 offset:21616\n"
    "v_mfma_f32_32x32x16_bf16 v[144:159], v[232:235], v[224:227], v[144:159]\n"
    "ds_read_b64_tr_b16 v[172:173], v6 offset:21560\n"
    "ds_read_b64_tr_b16 v[174:175], v6 offset:21672\n"
    "v_mfma_f32_32x32x16_bf16 v[128:143], v[36:39], v[32:35], v[128:143]\n"
    "ds_read_b128 v[192:195], v3 offset:24576\n"
    "ds_read_b64_tr_b16 v[196:197], v6 offset:23296\n"
    "ds_read_b64_tr_b16 v[198:199], v6 offset:23408\n"
    "v_mfma_f32_32x32x16_bf16 v[144:159], v[40:43], v[32:35], v[144:159]\n"
    "ds_read_b64_tr_b16 v[200:201], v6 offset:23352\n"
    "ds_read_b64_tr_b16 v[202:203], v6 offset:23464\n"
    "s_waitcnt lgkmcnt(10)\n"
    "v_mfma_f32_32x32x16_bf16 v[128:143], v[48:51], v[44:47], v[128:143]\n"
    "ds_read_b128 v[224:227], v4 offset:24576\n"
    "ds_read_b64_tr_b16 v[228:229], v6 offset:25088\n"
    "ds_read_b64_tr_b16 v[230:231], v6 offset:25200\n"
    "v_mfma_f32_32x32x16_bf16 v[144:159], v[52:55], v[44:47], v[144:159]\n"
    "ds_read_b64_tr_b16 v[232:233], v6 offset:25144\n"
    "ds_read_b64_tr_b16 v[234:235], v6 offset:25256\n"
    "ds_read_b32 v251, v243 offset:160\n"
    "s_waitcnt lgkmcnt(11)\n"
    "v_mfma_f32_32x32x16_bf16 v[128:143], v[168:171], v[164:167], v[128:143]\n"
    "ds_read_b128 v[32:35], v5 offset:24576\n"
    "ds_read_b64_tr_b16 v[36:37], v6 offset:26880\n"
    "ds_read_b64_tr_b16 v[38:39], v6 offset:26992\n"
    "v_mfma_f32_32x32x16_bf16 v[144:159], v[172:175], v[164:167], v[144:159]\n"
    "ds_read_b64_tr_b16 v[40:41], v6 offset:26936\n"
    "ds_read_b64_tr_b16 v[42:43], v6 offset:27048\n"
    "s_waitcnt lgkmcnt(5)\n"
    "v_cmp_ne_u32 vcc, 0, v251\n"
    "s_cbranch_vccnz .LfS39\n"
    ".LfB40:\n"
    "v_mfma_f32_32x32x16_bf16 v[128:143], v[196:199], v[192:195], v[128:143]\n"
    "ds_read_b128 v[44:47], v2 offset:32768\n"
    "ds_read_b64_tr_b16 v[48:49], v6 offset:28672\n"
    "ds_read_b64_tr_b16 v[50:51], v6 offset:28784\n"
    "v_mfma_f32_32x32x16_bf16 v[144:159], v[200:203], v[192:195], v[144:159]\n"
    "ds_read_b64_tr_b16 v[52:53], v6 offset:28728\n"
    "ds_read_b64_tr_b16 v[54:55], v6 offset:28840\n"
    "v_mfma_f32_32x32x16_bf16 v[128:143], v[228:231], v[224:227], v[128:143]\n"
    "ds_read_b128 v[164:167], v3 offset:32768\n"
    "ds_read_b64_tr_b16 v[168:169], v6 offset:30464\n"
    "ds_read_b64_tr_b16 v[170:171], v6 offset:30576\n"
    "v_mfma_f32_32x32x16_bf16 v[144:159], v[232:235], v[224:227], v[144:159]\n"
    "ds_read_b64_tr_b16 v[172:173], v6 offset:30520\n"
    "ds_read_b64_tr_b16 v[174:175], v6 offset:30632\n"
    "s_waitcnt lgkmcnt(10)\n"
    "v_mfma_f32_32x32x16_bf16 v[128:143], v[36:39], v[32:35], v[128:143]\n"
    "ds_read_b128 v[192:195], v4 offset:32768\n"
    "ds_read_b64_tr_b16 v[196:197], v6 offset:32256\n"
    "ds_read_b64_tr_b16 v[198:199], v6 offset:32368\n"
    "v_mfma_f32_32x32x16_bf16 v[144:159], v[40:43], v[32:35], v[144:159]\n"
    "ds_read_b64_tr_b16 v[200:201], v6 offset:32312\n"
    "ds_read_b64_tr_b16 v[202:203], v6 offset:32424\n"
    "ds_read_b32 v251, v243 offset:164\n"
    "s_waitcnt lgkmcnt(11)\n"
    "v_mfma_f32_32x32x16_bf16 v[128:143], v[48:51], v[44:47], v[128:143]\n"
    "ds_read_b128 v[224:227], v5 offset:32768\n"
    "ds_read_b64_tr_b16 v[228:229], v6 offset:34048\n"
    "ds_read_b64_tr_b16 v[230:231], v6 offset:34160\n"
    "v_mfma_f32_32x32x16_bf16 v[144:159], v[52:55], v[44:47], v[144:159]\n"
    "ds_read_b64_tr_b16 v[232:233], v6 offset:34104\n"
    "ds_read_b64_tr_b16 v[234:235], v6 offset:34216\n"
    "s_waitcnt lgkmcnt(5)\n"
    "v_cmp_ne_u32 vcc, 0, v251\n"
    "s_cbranch_vccnz .LfS41\n"
    ".LfB42:\n"
    "v_mfma_f32_32x32x16_bf16 v[128:143], v[168:171], v[164:167], v[128:143]\n"
    "ds_read_b128 v[32:35], v2 offset:40960\n"
    "ds_read_b64_tr_b16 v[36:37], v6 offset:35840\n"
    "ds_read_b64_tr_b16 v[38:39], v6 offset:35952\n"
    "v_mfma_f32_32x32x16_bf16 v[144:159], v[172:175], v[164:167], v[144:159]\n"
    "ds_read_b64_tr_b16 v[40:41], v6 offset:35896\n"
    "ds_read_b64_tr_b16 v[42:43], v6 offset:36008\n"
    "v_mfma_f32_32x32x16_bf16 v[128:143], v[196:199], v[192:195], v[128:143]\n"
    "ds_read_b128 v[44:47], v3 offset:40960\n"
    "ds_read_b64_tr_b16 v[48:49], v6 offset:37632\n"
    "ds_read_b64_tr_b16 v[50:51], v6 offset:37744\n"
    "v_mfma_f32_32x32x16_bf16 v[144:159], v[200:203], v[192:195], v[144:159]\n"
    "ds_read_b64_tr_b16 v[52:53], v6 offset:37688\n"
    "ds_read_b64_tr_b16 v[54:55], v6 offset:37800\n"
    "ds_read_b32 v8, v243 offset:44\n"
    "s_waitcnt lgkmcnt(11)\n"
    "v_mfma_f32_32x32x16_bf16 v[128:143], v[228:231], v[224:227], v[128:143]\n"
    "ds_read_b128 v[164:167], v4 offset:40960\n"
    "ds_read_b64_tr_b16 v[168:169], v6 offset:39424\n"
    "ds_read_b64_tr_b16 v[170:171], v6 offset:39536\n"
    "v_mfma_f32_32x32x16_bf16 v[144:159], v[232:235], v[224:227], v[144:159]\n"
    "ds_read_b64_tr_b16 v[172:173], v6 offset:39480\n"
    "ds_read_b64_tr_b16 v[174:175], v6 offset:39592\n"
    "ds_read_b32 v251, v243 offset:168\n"
    "s_waitcnt lgkmcnt(12)\n"
    "v_mfma_f32_32x32x16_bf16 v[128:143], v[36:39], v[32:35], v[128:143]\n"
    "ds_read_b128 v[192:195], v5 offset:40960\n"
    "ds_read_b64_tr_b16 v[196:197], v6 offset:41216\n"
    "ds_read_b64_tr_b16 v[198:199], v6 offset:41328\n"
    "v_mfma_f32_32x32x16_bf16 v[144:159], v[40:43], v[32:35], v[144:159]\n"
    "ds_read_b64_tr_b16 v[200:201], v6 offset:41272\n"
    "ds_read_b64_tr_b16 v[202:203], v6 offset:41384\n"
    "s_waitcnt lgkmcnt(5)\n"
    "v_cmp_ne_u32 vcc, 0, v251\n"
    "s_cbranch_vccnz .LfS43\n"
    ".LfB44:\n"
    "v_mfma_f32_32x32x16_bf16 v[128:143], v[48:51], v[44:47], v[128:143]\n"
    "ds_read_b128 v[224:227], v2 offset:49152\n"
    "ds_read_b64_tr_b16 v[228:229], v6 offset:43008\n"
    "ds_read_b64_tr_b16 v[230:231], v6 offset:43120\n"
    "v_mfma_f32_32x32x16_bf16 v[144:159], v[52:55], v[44:47], v[144:159]\n"
    "ds_read_b64_tr_b16 v[232:233], v6 offset:43064\n"
    "ds_read_b64_tr_b16 v[234:235], v6 offset:43176\n"
    "ds_read_b32 v251, v243 offset:172\n"
    "v_mfma_f32_32x32x16_bf16 v[128:143], v[168:171], v[164:167], v[128:143]\n"
    "ds_read_b128 v[32:35], v3 offset:49152\n"
    "ds_read_b64_tr_b16 v[36:37], v6 offset:44800\n"
    "ds_read_b64_tr_b16 v[38:39], v6 offset:44912\n"
    "v_mfma_f32_32x32x16_bf16 v[144:159], v[172:175], v[164:167], v[144:159]\n"
    "ds_read_b64_tr_b16 v[40:41], v6 offset:44856\n"
    "ds_read_b64_tr_b16 v[42:43], v6 offset:44968\n"
    "s_waitcnt lgkmcnt(13)\n"
    "v_mfma_f32_32x32x16_bf16 v[128:143], v[196:199], v[192:195], v[128:143]\n"
    "s_waitcnt lgkmcnt(11)\n"
    "v_mfma_f32_32x32x16_bf16 v[144:159], v[200:203], v[192:195], v[144:159]\n"
    "s_waitcnt lgkmcnt(8)\n"
    "v_mfma_f32_32x32x16_bf16 v[128:143], v[228:231], v[224:227], v[128:143]\n"
    "s_waitcnt lgkmcnt(6)\n"
    "v_mfma_f32_32x32x16_bf16 v[144:159], v[232:235], v[224:227], v[144:159]\n"
    "s_waitcnt lgkmcnt(2)\n"
    "v_mfma_f32_32x32x16_bf16 v[128:143], v[36:39], v[32:35], v[128:143]\n"
    "s_waitcnt lgkmcnt(0)\n"
    "v_mfma_f32_32x32x16_bf16 v[144:159], v[40:43], v[32:35], v[144:159]\n"
    "v_cmp_ne_u32 vcc, 0, v251\n"
    "s_cbranch_vccnz .LfS45\n"
    ".LfB46:\n"
    "v_cmp_eq_u32 vcc, 0, v8\n"
    "s_cbranch_vccnz .LfCKS\n"
    ".LfCKB:\n"
    "s_sub_u32 s58, s42, 1\n"
    "s_cmp_lt_u32 s58, 6\n"
    "s_cbranch_scc0 .LfL47\n"
    ".LfL47b:\n"
    "buffer_load_dwordx4 v[176:179], v242, s[24:27], s84 offen offset:0 sc1\n"
    "buffer_load_dwordx4 v[180:183], v242, s[24:27], s84 offen offset:1024 sc1\n"
    "buffer_load_dwordx4 v[208:211], v242, s[24:27], s84 offen offset:2048 sc1\n"
    "buffer_load_dwordx4 v[212:215], v242, s[24:27], s84 offen offset:3072 sc1\n"
    "buffer_load_dwordx4 v[184:187], v12, s[24:27], s84 offen offset:0 sc1\n"
    "buffer_load_dwordx4 v[188:191], v12, s[24:27], s84 offen offset:1024 sc1\n"
    "buffer_load_dwordx4 v[216:219], v12, s[24:27], s84 offen offset:2048 sc1\n"
    "buffer_load_dwordx4 v[220:223], v12, s[24:27], s84 offen offset:3072 sc1\n"
    "ds_read_b128 v[44:47], v4 offset:49152\n"
    "ds_read_b64_tr_b16 v[48:49], v6 offset:46592\n"
    "ds_read_b64_tr_b16 v[50:51], v6 offset:46704\n"
    "ds_read_b64_tr_b16 v[52:53], v6 offset:46648\n"
    "ds_read_b64_tr_b16 v[54:55], v6 offset:46760\n"
    "ds_read_b128 v[164:167], v5 offset:49152\n"
    "ds_read_b64_tr_b16 v[168:169], v6 offset:48384\n"
    "ds_read_b64_tr_b16 v[170:171], v6 offset:48496\n"
    "ds_read_b64_tr_b16 v[172:173], v6 offset:48440\n"
    "ds_read_b64_tr_b16 v[174:175], v6 offset:48552\n"
    "s_waitcnt lgkmcnt(7)\n"
    "v_mfma_f32_32x32x16_bf16 v[128:143], v[48:51], v[44:47], v[128:143]\n"
    "s_waitcnt lgkmcnt(2)\n"
    "v_mfma_f32_32x32x16_bf16 v[128:143], v[168:171], v[164:167], v[128:143]\n"
    "v_mfma_f32_32x32x16_bf16 v[144:159], v[52:55], v[44:47], v[144:159]\n"
    "s_waitcnt lgkmcnt(0)\n"
    "v_mfma_f32_32x32x16_bf16 v[144:159], v[172:175], v[164:167], v[144:159]\n"
    "s_nop 8\n"
    "v_cvt_pk_bf16_f32 v32, v128, v129\n"
    "v_cvt_pk_bf16_f32 v33, v130, v131\n"
    "v_cvt_pk_bf16_f32 v34, v132, v133\n"
    "v_cvt_pk_bf16_f32 v35, v134, v135\n"
    "ds_write_b128 v7, v[32:35] offset:34816\n"
    "v_cvt_pk_bf16_f32 v36, v136, v137\n"
    "v_cvt_pk_bf16_f32 v37, v138, v139\n"
    "v_cvt_pk_bf16_f32 v38, v140, v141\n"
    "v_cvt_pk_bf16_f32 v39, v142, v143\n"
    "v_cndmask_b32 v38, 0, v38, s[80:81]\n"
    "v_cndmask_b32 v39, 0, v39, s[80:81]\n"
    "ds_write_b128 v7, v[36:39] offset:35840\n"
    "v_cvt_pk_bf16_f32 v40, v144, v145\n"
    "v_cvt_pk_bf16_f32 v41, v146, v147\n"
    "v_cvt_pk_bf16_f32 v42, v148, v149\n"
    "v_cvt_pk_bf16_f32 v43, v150, v151\n"
    "ds_write_b128 v7, v[40:43] offset:36864\n"
    "v_cvt_pk_bf16_f32 v44, v152, v153\n"
    "v_cvt_pk_bf16_f32 v45, v154, v155\n"
    "v_cvt_pk_bf16_f32 v46, v156, v157\n"
    "v_cvt_pk_bf16_f32 v47, v158, v159\n"
    "v_cndmask_b32 v46, 0, v46, s[80:81]\n"
    "v_cndmask_b32 v47, 0, v47, s[80:81]\n"
    "ds_write_b128 v7, v[44:47] offset:37888\n"
    "ds_add_u32 v245, v244 offset:4\n"
    ".LfCHK:\n"
    ".LfL57:\n"
    "ds_read_b32 v250, v243 offset:4\n"
    "s_add_u32 s49, s49, 1\n"
    "s_cmp_lt_u32 s49, 0x100000\n"
    "s_cselect_b32 s50, 2, 0\n"
    "s_waitcnt lgkmcnt(0)\n"
    "v_cmp_gt_u32 vcc, s50, v250\n"
    "s_cbranch_vccnz .LfL57\n"
    "ds_read_b128 v[32:35], v242 offset:34816\n"
    "ds_read_b128 v[36:39], v242 offset:35840\n"
    "ds_read_b128 v[40:43], v242 offset:38912\n"
    "ds_read_b128 v[44:47], v242 offset:39936\n"
    "ds_read_b128 v[48:51], v242 offset:36864\n"
    "ds_read_b128 v[52:55], v242 offset:37888\n"
    "ds_read_b128 v[56:59], v242 offset:40960\n"
    "ds_read_b128 v[60:63], v242 offset:41984\n"
    "s_waitcnt vmcnt(0)\n"
    ".LfCHK2:\n"
    "v_max3_u32 v250, v176, v177, v178\n"
    "v_max3_u32 v250, v250, v179, v180\n"
    "v_max3_u32 v250, v250, v181, v182\n"
    "v_max3_u32 v250, v250, v183, v184\n"
    "v_max3_u32 v250, v250, v185, v186\n"
    "v_max3_u32 v250, v250, v187, v188\n"
    "v_max3_u32 v250, v250, v189, v190\n"
    "v_max_u32 v250, v250, v191\n"
    "v_max3_u32 v251, v208, v209, v210\n"
    "v_max3_u32 v251, v251, v211, v212\n"
    "v_max3_u32 v251, v251, v213, v214\n"
    "v_max3_u32 v251, v251, v215, v216\n"
    "v_max3_u32 v251, v251, v217, v218\n"
    "v_max3_u32 v251, v251, v219, v220\n"
    "v_max3_u32 v251, v251, v221, v222\n"
    "v_max_u32 v251, v251, v223\n"
    "v_max_u32 v250, v250, v251\n"
    "v_cmp_eq_u32 vcc, -1, v250\n"
    ".LfCHK3:\n"
    "s_cbranch_vccnz .LfCKS1\n"
    ".LfATT:\n"
    "s_setprio 1\n"
    "s_waitcnt lgkmcnt(6)\n"
    "v_mfma_f32_32x32x16_bf16 v[128:143], v[176:179], v[32:35], 0\n"
    "v_mfma_f32_32x32x16_bf16 v[128:143], v[180:183], v[36:39], v[128:143]\n"
    "buffer_load_dwordx4 v[192:195], v242, s[24:27], s85 offen offset:0 sc1\n"
    "buffer_load_dwordx4 v[196:199], v242, s[24:27], s85 offen offset:1024 sc1\n"
    "buffer_load_dwordx4 v[224:227], v242, s[24:27], s85 offen offset:2048 sc1\n"
    "buffer_load_dwordx4 v[228:231], v242, s[24:27], s85 offen offset:3072 sc1\n"
    "buffer_load_dwordx4 v[200:203], v12, s[24:27], s85 offen offset:0 sc1\n"
    "buffer_load_dwordx4 v[204:207], v12, s[24:27], s85 offen offset:1024 sc1\n"
    "buffer_load_dwordx4 v[232:235], v12, s[24:27], s85 offen offset:2048 sc1\n"
    "buffer_load_dwordx4 v[236:239], v12, s[24:27], s85 offen offset:3072 sc1\n"
    "s_waitcnt lgkmcnt(0)\n"
    "v_mfma_f32_32x32x16_bf16 v[144:159], v[176:179], v[40:43], 0\n"
    "s_nop 1\n"
    "v_exp_f32 v128, v128\n"
    "v_exp_f32 v129, v129\n"
    "v_exp_f32 v130, v130\n"
    "v_exp_f32 v131, v131\n"
    "v_mfma_f32_32x32x16_bf16 v[144:159], v[180:183], v[44:47], v[144:159]\n"
    "v_exp_f32 v132, v132\n"
    "v_exp_f32 v133, v133\n"
    "v_exp_f32 v134, v134\n"
    "v_exp_f32 v135, v135\n"
    "v_exp_f32 v136, v136\n"
    "v_exp_f32 v137, v137\n"
    "v_exp_f32 v138, v138\n"
    "v_exp_f32 v139, v139\n"
    "v_exp_f32 v140, v140\n"
    "v_exp_f32 v141, v141\n"
    "v_exp_f32 v142, v142\n"
    "v_exp_f32 v143, v143\n"
    "v_mfma_f32_32x32x16_bf16 v[160:175], v[176:179], v[48:51], 0\n"
    "v_exp_f32 v144, v144\n"
    "v_exp_f32 v145, v145\n"
    "v_exp_f32 v146, v146\n"
    "v_exp_f32 v147, v147\n"
    "v_mfma_f32_32x32x16_bf16 v[160:175], v[180:183], v[52:55], v[160:175]\n"
    "v_exp_f32 v148, v148\n"
    "v_exp_f32 v149, v149\n"
    "v_exp_f32 v150, v150\n"
    "v_exp_f32 v151, v151\n"
    "v_smfmac_f32_32x32x32_bf16 v[64:79], v[208:211], v[128:135], v240\n"
    "v_exp_f32 v152, v152\n"
    "v_exp_f32 v153, v153\n"
    "v_exp_f32 v154, v154\n"
    "v_exp_f32 v155, v155\n"
    "v_smfmac_f32_32x32x32_bf16 v[64:79], v[212:215], v[136:143], v240\n"
    "v_exp_f32 v156, v156\n"
    "v_exp_f32 v157, v157\n"
    "v_exp_f32 v158, v158\n"
    "v_exp_f32 v159, v159\n"
    "v_mfma_f32_32x32x16_bf16 v[128:143], v[176:179], v[56:59], 0\n"
    "v_exp_f32 v160, v160\n"
    "v_exp_f32 v161, v161\n"
    "v_exp_f32 v162, v162\n"
    "v_exp_f32 v163, v163\n"
    "v_mfma_f32_32x32x16_bf16 v[128:143], v[180:183], v[60:63], v[128:143]\n"
    "v_exp_f32 v164, v164\n"
    "v_exp_f32 v165, v165\n"
    "v_exp_f32 v166, v166\n"
    "v_exp_f32 v167, v167\n"
    "v_smfmac_f32_32x32x32_bf16 v[80:95], v[208:211], v[144:151], v240\n"
    "v_exp_f32 v168, v168\n"
    "v_exp_f32 v169, v169\n"
    "v_exp_f32 v170, v170\n"
    "v_exp_f32 v171, v171\n"
    "v_smfmac_f32_32x32x32_bf16 v[80:95], v[212:215], v[152:159], v240\n"
    "v_exp_f32 v172, v172\n"
    "v_exp_f32 v173, v173\n"
    "v_exp_f32 v174, v174\n"
    "v_exp_f32 v175, v175\n"
    "v_mfma_f32_32x32x16_bf16 v[144:159], v[184:187], v[32:35], 0\n"
    "v_exp_f32 v128, v128\n"
    "v_exp_f32 v129, v129\n"
    "v_exp_f32 v130, v130\n"
    "v_exp_f32 v131, v131\n"
    "v_mfma_f32_32x32x16_bf16 v[144:159], v[188:191], v[36:39], v[144:159]\n"
    "v_exp_f32 v132, v132\n"
    "v_exp_f32 v133, v133\n"
    "v_exp_f32 v134, v134\n"
    "v_exp_f32 v135, v135\n"
    "v_smfmac_f32_32x32x32_bf16 v[96:111], v[208:211], v[160:167], v240\n"
    "buffer_load_dwordx2 v[14:15], v13, s[24:27], 0 offen sc1\n"
    "v_exp_f32 v136, v136\n"
    "v_exp_f32 v137, v137\n"
    "v_exp_f32 v138, v138\n"
    "v_exp_f32 v139, v139\n"
    "v_smfmac_f32_32x32x32_bf16 v[96:111], v[212:215], v[168:175], v240\n"
    "v_exp_f32 v140, v140\n"
    "v_exp_f32 v141, v141\n"
    "v_exp_f32 v142, v142\n"
    "v_exp_f32 v143, v143\n"
    "v_mfma_f32_32x32x16_bf16 v[160:175], v[184:187], v[40:43], 0\n"
    "v_exp_f32 v144, v144\n"
    "v_exp_f32 v145, v145\n"
    "v_exp_f32 v146, v146\n"
    "v_exp_f32 v147, v147\n"
    "v_mfma_f32_32x32x16_bf16 v[160:175], v[188:191], v[44:47], v[160:175]\n"
    "v_exp_f32 v148, v148\n"
    "v_exp_f32 v149, v149\n"
    "v_exp_f32 v150, v150\n"
    "v_exp_f32 v151, v151\n"
    "v_smfmac_f32_32x32x32_bf16 v[112:127], v[208:211], v[128:135], v240\n"
    "v_exp_f32 v152, v152\n"
    "v_exp_f32 v153, v153\n"
    "v_exp_f32 v154, v154\n"
    "v_exp_f32 v155, v155\n"
    "v_smfmac_f32_32x32x32_bf16 v[112:127], v[212:215], v[136:143], v240\n"
    "v_exp_f32 v156, v156\n"
    "v_exp_f32 v157, v157\n"
    "v_exp_f32 v158, v158\n"
    "v_exp_f32 v159, v159\n"
    "v_mfma_f32_32x32x16_bf16 v[128:143], v[184:187], v[48:51], 0\n"
    "v_exp_f32 v160, v160\n"
    "v_exp_f32 v161, v161\n"
    "v_exp_f32 v162, v162\n"
    "v_exp_f32 v163, v163\n"
    "v_mfma_f32_32x32x16_bf16 v[128:143], v[188:191], v[52:55], v[128:143]\n"
    "v_exp_f32 v164, v164\n"
    "v_exp_f32 v165, v165\n"
    "v_exp_f32 v166, v166\n"
    "v_exp_f32 v167, v167\n"
    "v_smfmac_f32_32x32x32_bf16 v[64:79], v[216:219], v[144:151], v240\n"
    "v_exp_f32 v168, v168\n"
    "v_exp_f32 v169, v169\n"
    "v_exp_f32 v170, v170\n"
    "v_exp_f32 v171, v171\n"
    "v_smfmac_f32_32x32x32_bf16 v[64:79], v[220:223], v[152:159], v240\n"
    "v_exp_f32 v172, v172\n"
    "v_exp_f32 v173, v173\n"
    "v_exp_f32 v174, v174\n"
    "v_exp_f32 v175, v175\n"
    "v_mfma_f32_32x32x16_bf16 v[144:159], v[184:187], v[56:59], 0\n"
    "v_exp_f32 v128, v128\n"
    "v_exp_f32 v129, v129\n"
    "v_exp_f32 v130, v130\n"
    "v_exp_f32 v131, v131\n"
    "v_mfma_f32_32x32x16_bf16 v[144:159], v[188:191], v[60:63], v[144:159]\n"
    "v_exp_f32 v132, v132\n"
    "v_exp_f32 v133, v133\n"
    "v_exp_f32 v134, v134\n"
    "v_exp_f32 v135, v135\n"
    "v_smfmac_f32_32x32x32_bf16 v[80:95], v[216:219], v[160:167], v240\n"
    "s_waitcnt vmcnt(1)\n"
    "v_max3_u32 v250, v192, v193, v194\n"
    "v_max3_u32 v250, v250, v195, v196\n"
    "v_max3_u32 v250, v250, v197, v198\n"
    "v_max3_u32 v250, v250, v199, v200\n"
    "v_max3_u32 v250, v250, v201, v202\n"
    "v_max3_u32 v250, v250, v203, v204\n"
    "v_max3_u32 v250, v250, v205, v206\n"
    "v_max_u32 v250, v250, v207\n"
    "v_max3_u32 v251, v224, v225, v226\n"
    "v_max3_u32 v251, v251, v227, v228\n"
    "v_max3_u32 v251, v251, v229, v230\n"
    "v_max3_u32 v251, v251, v231, v232\n"
    "v_max3_u32 v251, v251, v233, v234\n"
    "v_max3_u32 v251, v251, v235, v236\n"
    "v_max3_u32 v251, v251, v237, v238\n"
    "v_max_u32 v251, v251, v239\n"
    "v_max_u32 v250, v250, v251\n"
    "v_cmp_eq_u32 vcc, -1, v250\n"
    "s_cbranch_vccnz .LfCK2S\n"
    ".LfCK2:\n"
    "v_exp_f32 v136, v136\n"
    "v_exp_f32 v137, v137\n"
    "v_exp_f32 v138, v138\n"
    "v_exp_f32 v139, v139\n"
    "v_smfmac_f32_32x32x32_bf16 v[80:95], v[220:223], v[168:175], v240\n"
    "v_exp_f32 v140, v140\n"
    "v_exp_f32 v141, v141\n"
    "v_exp_f32 v142, v142\n"
    "v_exp_f32 v143, v143\n"
    "v_mfma_f32_32x32x16_bf16 v[160:175], v[192:195], v[32:35], 0\n"
    "v_exp_f32 v144, v144\n"
    "v_exp_f32 v145, v145\n"
    "v_exp_f32 v146, v146\n"
    "v_exp_f32 v147, v147\n"
    "v_mfma_f32_32x32x16_bf16 v[160:175], v[196:199], v[36:39], v[160:175]\n"
    "v_exp_f32 v148, v148\n"
    "v_exp_f32 v149, v149\n"
    "v_exp_f32 v150, v150\n"
    "v_exp_f32 v151, v151\n"
    "v_smfmac_f32_32x32x32_bf16 v[96:111], v[216:219], v[128:135], v240\n"
    "v_exp_f32 v152, v152\n"
    "v_exp_f32 v153, v153\n"
    "v_exp_f32 v154, v154\n"
    "v_exp_f32 v155, v155\n"
    "v_smfmac_f32_32x32x32_bf16 v[96:111], v[220:223], v[136:143], v240\n"
    "v_exp_f32 v156, v156\n"
    "v_exp_f32 v157, v157\n"
    "v_exp_f32 v158, v158\n"
    "v_exp_f32 v159, v159\n"
    "v_mfma_f32_32x32x16_bf16 v[128:143], v[192:195], v[40:43], 0\n"
    "v_exp_f32 v160, v160\n"
    "v_exp_f32 v161, v161\n"
    "v_exp_f32 v162, v162\n"
    "v_exp_f32 v163, v163\n"
    "v_mfma_f32_32x32x16_bf16 v[128:143], v[196:199], v[44:47], v[128:143]\n"
    "v_exp_f32 v164, v164\n"
    "v_exp_f32 v165, v165\n"
    "v_exp_f32 v166, v166\n"
    "v_exp_f32 v167, v167\n"
    "v_smfmac_f32_32x32x32_bf16 v[112:127], v[216:219], v[144:151], v240\n"
    "s_waitcnt vmcnt(0)\n"
    "v_cmp_ne_u64 vcc, s[44:45], v[14:15]\n"
    "s_cbranch_vccnz .LfS58\n"
    ".LfB59:\n"
    "buffer_load_dwordx4 v[176:179], v242, s[24:27], s86 offen offset:0 sc1\n"
    "buffer_load_dwordx4 v[180:183], v242, s[24:27], s86 offen offset:1024 sc1\n"
    "buffer_load_dwordx4 v[208:211], v242, s[24:27], s86 offen offset:2048 sc1\n"
    "buffer_load_dwordx4 v[212:215], v242, s[24:27], s86 offen offset:3072 sc1\n"
    "buffer_load_dwordx4 v[184:187], v12, s[24:27], s86 offen offset:0 sc1\n"
    "buffer_load_dwordx4 v[188:191], v12, s[24:27], s86 offen offset:1024 sc1\n"
    "v_exp_f32 v168, v168\n"
    "v_exp_f32 v169, v169\n"
    "v_exp_f32 v170, v170\n"
    "v_exp_f32 v171, v171\n"
    "v_smfmac_f32_32x32x32_bf16 v[112:127], v[220:223], v[152:159], v240\n"
    "buffer_load_dwordx4 v[216:219], v12, s[24:27], s86 offen offset:2048 sc1\n"
    "buffer_load_dwordx4 v[220:223], v12, s[24:27], s86 offen offset:3072 sc1\n"
    "v_exp_f32 v172, v172\n"
    "v_exp_f32 v173, v173\n"
    "v_exp_f32 v174, v174\n"
    "v_exp_f32 v175, v175\n"
    "v_mfma_f32_32x32x16_bf16 v[144:159], v[192:195], v[48:51], 0\n"
    "v_exp_f32 v128, v128\n"
    "v_exp_f32 v129, v129\n"
    "v_exp_f32 v130, v130\n"
    "v_exp_f32 v131, v131\n"
    "v_mfma_f32_32x32x16_bf16 v[144:159], v[196:199], v[52:55], v[144:159]\n"
    "v_exp_f32 v132, v132\n"
    "v_exp_f32 v133, v133\n"
    "v_exp_f32 v134, v134\n"
    "v_exp_f32 v135, v135\n"
    "v_smfmac_f32_32x32x32_bf16 v[64:79], v[224:227], v[160:167], v240\n"
    "v_exp_f32 v136, v136\n"
    "v_exp_f32 v137, v137\n"
    "v_exp_f32 v138, v138\n"
    "v_exp_f32 v139, v139\n"
    "v_smfmac_f32_32x32x32_bf16 v[64:79], v[228:231], v[168:175], v240\n"
    "v_exp_f32 v140, v140\n"
    "v_exp_f32 v141, v141\n"
    "v_exp_f32 v142, v142\n"
    "v_exp_f32 v143, v143\n"
    "v_mfma_f32_32x32x16_bf16 v[160:175], v[192:195], v[56:59], 0\n"
    "v_exp_f32 v144, v144\n"
    "v_exp_f32 v145, v145\n"
    "v_exp_f32 v146, v146\n"
    "v_exp_f32 v147, v147\n"
    "v_mfma_f32_32x32x16_bf16 v[160:175], v[196:199], v[60:63], v[160:175]\n"
    "buffer_load_dwordx4 v[192:195], v242, s[24:27], s87 offen offset:0 sc1\n"
    "buffer_load_dwordx4 v[196:199], v242, s[24:27], s87 offen offset:1024 sc1\n"
    "v_exp_f32 v148, v148\n"
    "v_exp_f32 v149, v149\n"
    "v_exp_f32 v150, v150\n"
    "v_exp_f32 v151, v151\n"
    "v_smfmac_f32_32x32x32_bf16 v[80:95], v[224:227], v[128:135], v240\n"
    "v_exp_f32 v152, v152\n"
    "v_exp_f32 v153, v153\n"
    "v_exp_f32 v154, v154\n"
    "v_exp_f32 v155, v155\n"
    "v_smfmac_f32_32x32x32_bf16 v[80:95], v[228:231], v[136:143], v240\n"
    "v_exp_f32 v156, v156\n"
    "v_exp_f32 v157, v157\n"
    "v_exp_f32 v158, v158\n"
    "v_exp_f32 v159, v159\n"
    "v_mfma_f32_32x32x16_bf16 v[128:143], v[200:203], v[32:35], 0\n"
    "v_exp_f32 v160, v160\n"
    "v_exp_f32 v161, v161\n"
    "v_exp_f32 v162, v162\n"
    "v_exp_f32 v163, v163\n"
    "v_mfma_f32_32x32x16_bf16 v[128:143], v[204:207], v[36:39], v[128:143]\n"
    "v_exp_f32 v164, v164\n"
    "v_exp_f32 v165, v165\n"
    "v_exp_f32 v166, v166\n"
    "v_exp_f32 v167, v167\n"
    "v_smfmac_f32_32x32x32_bf16 v[96:111], v[224:227], v[144:151], v240\n"
    "v_exp_f32 v168, v168\n"
    "v_exp_f32 v169, v169\n"
    "v_exp_f32 v170, v170\n"
    "v_exp_f32 v171, v171\n"
    "v_smfmac_f32_32x32x32_bf16 v[96:111], v[228:231], v[152:159], v240\n"
    "v_exp_f32 v172, v172\n"
    "v_exp_f32 v173, v173\n"
    "v_exp_f32 v174, v174\n"
    "v_exp_f32 v175, v175\n"
    "v_mfma_f32_32x32x16_bf16 v[144:159], v[200:203], v[40:43], 0\n"
    "v_exp_f32 v128, v128\n"
    "v_exp_f32 v129, v129\n"
    "v_exp_f32 v130, v130\n"
    "v_exp_f32 v131, v131\n"
    "v_mfma_f32_32x32x16_bf16 v[144:159], v[204:207], v[44:47], v[144:159]\n"
    "v_exp_f32 v132, v132\n"
    "v_exp_f32 v133, v133\n"
    "v_exp_f32 v134, v134\n"
    "v_exp_f32 v135, v135\n"
    "v_smfmac_f32_32x32x32_bf16 v[112:127], v[224:227], v[160:167], v240\n"
    "ds_add_u32 v245, v244 offset:48\n"
    "v_exp_f32 v136, v136\n"
    "v_exp_f32 v137, v137\n"
    "v_exp_f32 v138, v138\n"
    "v_exp_f32 v139, v139\n"
    "v_smfmac_f32_32x32x32_bf16 v[112:127], v[228:231], v[168:175], v240\n"
    "buffer_load_dwordx4 v[224:227], v242, s[24:27], s87 offen offset:2048 sc1\n"
    "buffer_load_dwordx4 v[228:231], v242, s[24:27], s87 offen offset:3072 sc1\n"
    "v_exp_f32 v140, v140\n"
    "v_exp_f32 v141, v141\n"
    "v_exp_f32 v142, v142\n"
    "v_exp_f32 v143, v143\n"
    "v_mfma_f32_32x32x16_bf16 v[160:175], v[200:203], v[48:51], 0\n"
    "v_exp_f32 v144, v144\n"
    "v_exp_f32 v145, v145\n"
    "v_exp_f32 v146, v146\n"
    "v_exp_f32 v147, v147\n"
    "v_mfma_f32_32x32x16_bf16 v[160:175], v[204:207], v[52:55], v[160:175]\n"
    "v_exp_f32 v148, v148\n"
    "v_exp_f32 v149, v149\n"
    "v_exp_f32 v150, v150\n"
    "v_exp_f32 v151, v151\n"
    "v_smfmac_f32_32x32x32_bf16 v[64:79], v[232:235], v[128:135], v240\n"
    "v_exp_f32 v152, v152\n"
    "v_exp_f32 v153, v153\n"
    "v_exp_f32 v154, v154\n"
    "v_exp_f32 v155, v155\n"
    "v_smfmac_f32_32x32x32_bf16 v[64:79], v[236:239], v[136:143], v240\n"
    "v_exp_f32 v156, v156\n"
    "v_exp_f32 v157, v157\n"
    "v_exp_f32 v158, v158\n"
    "v_exp_f32 v159, v159\n"
    "v_mfma_f32_32x32x16_bf16 v[128:143], v[200:203], v[56:59], 0\n"
    "v_exp_f32 v160, v160\n"
    "v_exp_f32 v161, v161\n"
    "v_exp_f32 v162, v162\n"
    "v_exp_f32 v163, v163\n"
    "v_mfma_f32_32x32x16_bf16 v[128:143], v[204:207], v[60:63], v[128:143]\n"
    "buffer_load_dwordx4 v[200:203], v12, s[24:27], s87 offen offset:0 sc1\n"
    "buffer_load_dwordx4 v[204:207], v12, s[24:27], s87 offen offset:1024 sc1\n"
    "v_exp_f32 v164, v164\n"
    "v_exp_f32 v165, v165\n"
    "v_exp_f32 v166, v166\n"
    "v_exp_f32 v167, v167\n"
    "v_smfmac_f32_32x32x32_bf16 v[80:95], v[232:235], v[144:151], v240\n"
    "v_exp_f32 v168, v168\n"
    "v_exp_f32 v169, v169\n"
    "v_exp_f32 v170, v170\n"
    "v_exp_f32 v171, v171\n"
    "v_smfmac_f32_32x32x32_bf16 v[80:95], v[236:239], v[152:159], v240\n"
    "v_exp_f32 v172, v172\n"
    "v_exp_f32 v173, v173\n"
    "v_exp_f32 v174, v174\n"
    "v_exp_f32 v175, v175\n"
    "s_waitcnt vmcnt(12)\n"
    "v_mfma_f32_32x32x16_bf16 v[144:159], v[176:179], v[32:35], 0\n"
    "v_exp_f32 v128, v128\n"
    "v_exp_f32 v129, v129\n"
    "v_exp_f32 v130, v130\n"
    "v_exp_f32 v131, v131\n"
    "v_mfma_f32_32x32x16_bf16 v[144:159], v[180:183], v[36:39], v[144:159]\n"
    "v_exp_f32 v132, v132\n"
    "v_exp_f32 v133, v133\n"
    "v_exp_f32 v134, v134\n"
    "v_exp_f32 v135, v135\n"
    "v_smfmac_f32_32x32x32_bf16 v[96:111], v[232:235], v[160:167], v240\n"
    "v_exp_f32 v136, v136\n"
    "v_exp_f32 v137, v137\n"
    "v_exp_f32 v138, v138\n"
    "v_exp_f32 v139, v139\n"
    "v_smfmac_f32_32x32x32_bf16 v[96:111], v[236:239], v[168:175], v240\n"
    "v_exp_f32 v140, v140\n"
    "v_exp_f32 v141, v141\n"
    "v_exp_f32 v142, v142\n"
    "v_exp_f32 v143, v143\n"
    "v_mfma_f32_32x32x16_bf16 v[160:175], v[176:179], v[40:43], 0\n"
    "v_exp_f32 v144, v144\n"
    "v_exp_f32 v145, v145\n"
    "v_exp_f32 v146, v146\n"
    "v_exp_f32 v147, v147\n"
    "v_mfma_f32_32x32x16_bf16 v[160:175], v[180:183], v[44:47], v[160:175]\n"
    "v_exp_f32 v148, v148\n"
    "v_exp_f32 v149, v149\n"
    "v_exp_f32 v150, v150\n"
    "v_exp_f32 v151, v151\n"
    "v_smfmac_f32_32x32x32_bf16 v[112:127], v[232:235], v[128:135], v240\n"
    "v_exp_f32 v152, v152\n"
    "v_exp_f32 v153, v153\n"
    "v_exp_f32 v154, v154\n"
    "v_exp_f32 v155, v155\n"
    "v_smfmac_f32_32x32x32_bf16 v[112:127], v[236:239], v[136:143], v240\n"
    "buffer_load_dwordx4 v[232:235], v12, s[24:27], s87 offen offset:2048 sc1\n"
    "buffer_load_dwordx4 v[236:239], v12, s[24:27], s87 offen offset:3072 sc1\n"
    "v_exp_f32 v156, v156\n"
    "v_exp_f32 v157, v157\n"
    "v_exp_f32 v158, v158\n"
    "v_exp_f32 v159, v159\n"
    "v_mfma_f32_32x32x16_bf16 v[128:143], v[176:179], v[48:51], 0\n"
    "v_exp_f32 v160, v160\n"
    "v_exp_f32 v161, v161\n"
    "v_exp_f32 v162, v162\n"
    "v_exp_f32 v163, v163\n"
    "v_mfma_f32_32x32x16_bf16 v[128:143], v[180:183], v[52:55], v[128:143]\n"
    "v_exp_f32 v164, v164\n"
    "v_exp_f32 v165, v165\n"
    "v_exp_f32 v166, v166\n"
    "v_exp_f32 v167, v167\n"
    "s_waitcnt vmcnt(12)\n"
    "v_smfmac_f32_32x32x32_bf16 v[64:79], v[208:211], v[144:151], v240\n"
    "v_exp_f32 v168, v168\n"
    "v_exp_f32 v169, v169\n"
    "v_exp_f32 v170, v170\n"
    "v_exp_f32 v171, v171\n"
    "v_smfmac_f32_32x32x32_bf16 v[64:79], v[212:215], v[152:159], v240\n"
    "v_exp_f32 v172, v172\n"
    "v_exp_f32 v173, v173\n"
    "v_exp_f32 v174, v174\n"
    "v_exp_f32 v175, v175\n"
    "v_mfma_f32_32x32x16_bf16 v[144:159], v[176:179], v[56:59], 0\n"
    "v_exp_f32 v128, v128\n"
    "v_exp_f32 v129, v129\n"
    "v_exp_f32 v130, v130\n"
    "v_exp_f32 v131, v131\n"
    "v_mfma_f32_32x32x16_bf16 v[144:159], v[180:183], v[60:63], v[144:159]\n"
    "buffer_load_dwordx4 v[176:179], v242, s[24:27], s88 offen offset:0 sc1\n"
    "buffer_load_dwordx4 v[180:183], v242, s[24:27], s88 offen offset:1024 sc1\n"
    "v_exp_f32 v132, v132\n"
    "v_exp_f32 v133, v133\n"
    "v_exp_f32 v134, v134\n"
    "v_exp_f32 v135, v135\n"
    "v_smfmac_f32_32x32x32_bf16 v[80:95], v[208:211], v[160:167], v240\n"
    "v_exp_f32 v136, v136\n"
    "v_exp_f32 v137, v137\n"
    "v_exp_f32 v138, v138\n"
    "v_exp_f32 v139, v139\n"
    "v_smfmac_f32_32x32x32_bf16 v[80:95], v[212:215], v[168:175], v240\n"
    "v_exp_f32 v140, v140\n"
    "v_exp_f32 v141, v141\n"
    "v_exp_f32 v142, v142\n"
    "v_exp_f32 v143, v143\n"
    "s_waitcnt vmcnt(12)\n"
    "v_mfma_f32_32x32x16_bf16 v[160:175], v[184:187], v[32:35], 0\n"
    "v_exp_f32 v144, v144\n"
    "v_exp_f32 v145, v145\n"
    "v_exp_f32 v146, v146\n"
    "v_exp_f32 v147, v147\n"
    "v_mfma_f32_32x32x16_bf16 v[160:175], v[188:191], v[36:39], v[160:175]\n"
    "v_exp_f32 v148, v148\n"
    "v_exp_f32 v149, v149\n"
    "v_exp_f32 v150, v150\n"
    "v_exp_f32 v151, v151\n"
    "v_smfmac_f32_32x32x32_bf16 v[96:111], v[208:211], v[128:135], v240\n"
    "v_exp_f32 v152, v152\n"
    "v_exp_f32 v153, v153\n"
    "v_exp_f32 v154, v154\n"
    "v_exp_f32 v155, v155\n"
    "v_smfmac_f32_32x32x32_bf16 v[96:111], v[212:215], v[136:143], v240\n"
    "v_exp_f32 v156, v156\n"
    "v_exp_f32 v157, v157\n"
    "v_exp_f32 v158, v158\n"
    "v_exp_f32 v159, v159\n"
    "v_mfma_f32_32x32x16_bf16 v[128:143], v[184:187], v[40:43], 0\n"
    "v_exp_f32 v160, v160\n"
    "v_exp_f32 v161, v161\n"
    "v_exp_f32 v162, v162\n"
    "v_exp_f32 v163, v163\n"
    "v_mfma_f32_32x32x16_bf16 v[128:143], v[188:191], v[44:47], v[128:143]\n"
    "v_exp_f32 v164, v164\n"
    "v_exp_f32 v165, v165\n"
    "v_exp_f32 v166, v166\n"
    "v_exp_f32 v167, v167\n"
    "v_smfmac_f32_32x32x32_bf16 v[112:127], v[208:211], v[144:151], v240\n"
    "v_exp_f32 v168, v168\n"
    "v_exp_f32 v169, v169\n"
    "v_exp_f32 v170, v170\n"
    "v_exp_f32 v171, v171\n"
    "v_smfmac_f32_32x32x32_bf16 v[112:127], v[212:215], v[152:159], v240\n"
    "buffer_load_dwordx4 v[208:211], v242, s[24:27], s88 offen offset:2048 sc1\n"
    "buffer_load_dwordx4 v[212:215], v242, s[24:27], s88 offen offset:3072 sc1\n"
    "v_exp_f32 v172, v172\n"
    "v_exp_f32 v173, v173\n"
    "v_exp_f32 v174, v174\n"
    "v_exp_f32 v175, v175\n"
    "v_mfma_f32_32x32x16_bf16 v[144:159], v[184:187], v[48:51], 0\n"
    "v_exp_f32 v128, v128\n"
    "v_exp_f32 v129, v129\n"
    "v_exp_f32 v130, v130\n"
    "v_exp_f32 v131, v131\n"
    "v_mfma_f32_32x32x16_bf16 v[144:159], v[188:191], v[52:55], v[144:159]\n"
    "v_exp_f32 v132, v132\n"
    "v_exp_f32 v133, v133\n"
    "v_exp_f32 v134, v134\n"
    "v_exp_f32 v135, v135\n"
    "s_waitcnt vmcnt(12)\n"
    "v_smfmac_f32_32x32x32_bf16 v[64:79], v[216:219], v[160:167], v240\n"
    "v_exp_f32 v136, v136\n"
    "v_exp_f32 v137, v137\n"
    "v_exp_f32 v138, v138\n"
    "v_exp_f32 v139, v139\n"
    "v_smfmac_f32_32x32x32_bf16 v[64:79], v[220:223], v[168:175], v240\n"
    "v_exp_f32 v140, v140\n"
    "v_exp_f32 v141, v141\n"
    "v_exp_f32 v142, v142\n"
    "v_exp_f32 v143, v143\n"
    "v_mfma_f32_32x32x16_bf16 v[160:175], v[184:187], v[56:59], 0\n"
    "v_exp_f32 v144, v144\n"
    "v_exp_f32 v145, v145\n"
    "v_exp_f32 v146, v146\n"
    "v_exp_f32 v147, v147\n"
    "v_mfma_f32_32x32x16_bf16 v[160:175], v[188:191], v[60:63], v[160:175]\n"
    "buffer_load_dwordx4 v[184:187], v12, s[24:27], s88 offen offset:0 sc1\n"
    "buffer_load_dwordx4 v[188:191], v12, s[24:27], s88 offen offset:1024 sc1\n"
    "v_exp_f32 v148, v148\n"
    "v_exp_f32 v149, v149\n"
    "v_exp_f32 v150, v150\n"
    "v_exp_f32 v151, v151\n"
    "v_smfmac_f32_32x32x32_bf16 v[80:95], v[216:219], v[128:135], v240\n"
    "v_exp_f32 v152, v152\n"
    "v_exp_f32 v153, v153\n"
    "v_exp_f32 v154, v154\n"
    "v_exp_f32 v155, v155\n"
    "v_smfmac_f32_32x32x32_bf16 v[80:95], v[220:223], v[136:143], v240\n"
    "v_exp_f32 v156, v156\n"
    "v_exp_f32 v157, v157\n"
    "v_exp_f32 v158, v158\n"
    "v_exp_f32 v159, v159\n"
    "s_waitcnt vmcnt(12)\n"
    "v_mfma_f32_32x32x16_bf16 v[128:143], v[192:195], v[32:35], 0\n"
    "v_exp_f32 v160, v160\n"
    "v_exp_f32 v161, v161\n"
    "v_exp_f32 v162, v162\n"
    "v_exp_f32 v163, v163\n"
    "v_mfma_f32_32x32x16_bf16 v[128:143], v[196:199], v[36:39], v[128:143]\n"
    "v_exp_f32 v164, v164\n"
    "v_exp_f32 v165, v165\n"
    "v_exp_f32 v166, v166\n"
    "v_exp_f32 v167, v167\n"
    "v_smfmac_f32_32x32x32_bf16 v[96:111], v[216:219], v[144:151], v240\n"
    "v_exp_f32 v168, v168\n"
    "v_exp_f32 v169, v169\n"
    "v_exp_f32 v170, v170\n"
    "v_exp_f32 v171, v171\n"
    "v_smfmac_f32_32x32x32_bf16 v[96:111], v[220:223], v[152:159], v240\n"
    "v_exp_f32 v172, v172\n"
    "v_exp_f32 v173, v173\n"
    "v_exp_f32 v174, v174\n"
    "v_exp_f32 v175, v175\n"
    "v_mfma_f32_32x32x16_bf16 v[144:159], v[192:195], v[40:43], 0\n"
    "v_exp_f32 v128, v128\n"
    "v_exp_f32 v129, v129\n"
    "v_exp_f32 v130, v130\n"
    "v_exp_f32 v131, v131\n"
    "v_mfma_f32_32x32x16_bf16 v[144:159], v[196:199], v[44:47], v[144:159]\n"
    "v_exp_f32 v132, v132\n"
    "v_exp_f32 v133, v133\n"
    "v_exp_f32 v134, v134\n"
    "v_exp_f32 v135, v135\n"
    "v_smfmac_f32_32x32x32_bf16 v[112:127], v[216:219], v[160:167], v240\n"
    "v_exp_f32 v136, v136\n"
    "v_exp_f32 v137, v137\n"
    "v_exp_f32 v138, v138\n"
    "v_exp_f32 v139, v139\n"
    "v_smfmac_f32_32x32x32_bf16 v[112:127], v[220:223], v[168:175], v240\n"
    "buffer_load_dwordx4 v[216:219], v12, s[24:27], s88 offen offset:2048 sc1\n"
    "buffer_load_dwordx4 v[220:223], v12, s[24:27], s88 offen offset:3072 sc1\n"
    "v_exp_f32 v140, v140\n"
    "v_exp_f32 v141, v141\n"
    "v_exp_f32 v142, v142\n"
    "v_exp_f32 v143, v143\n"
    "v_mfma_f32_32x32x16_bf16 v[160:175], v[192:195], v[48:51], 0\n"
    "v_exp_f32 v144, v144\n"
    "v_exp_f32 v145, v145\n"
    "v_exp_f32 v146, v146\n"
    "v_exp_f32 v147, v147\n"
    "v_mfma_f32_32x32x16_bf16 v[160:175], v[196:199], v[52:55], v[160:175]\n"
    "v_exp_f32 v148, v148\n"
    "v_exp_f32 v149, v149\n"
    "v_exp_f32 v150, v150\n"
    "v_exp_f32 v151, v151\n"
    "s_waitcnt vmcnt(12)\n"
    "v_smfmac_f32_32x32x32_bf16 v[64:79], v[224:227], v[128:135], v240\n"
    "v_exp_f32 v152, v152\n"
    "v_exp_f32 v153, v153\n"
    "v_exp_f32 v154, v154\n"
    "v_exp_f32 v155, v155\n"
    "v_smfmac_f32_32x32x32_bf16 v[64:79], v[228:231], v[136:143], v240\n"
    "v_exp_f32 v156, v156\n"
    "v_exp_f32 v157, v157\n"
    "v_exp_f32 v158, v158\n"
    "v_exp_f32 v159, v159\n"
    "v_mfma_f32_32x32x16_bf16 v[128:143], v[192:195], v[56:59], 0\n"
    "v_exp_f32 v160, v160\n"
    "v_exp_f32 v161, v161\n"
    "v_exp_f32 v162, v162\n"
    "v_exp_f32 v163, v163\n"
    "v_mfma_f32_32x32x16_bf16 v[128:143], v[196:199], v[60:63], v[128:143]\n"
    "buffer_load_dwordx4 v[192:195], v242, s[24:27], s89 offen offset:0 sc1\n"
    "buffer_load_dwordx4 v[196:199], v242, s[24:27], s89 offen offset:1024 sc1\n"
    "v_exp_f32 v164, v164\n"
    "v_exp_f32 v165, v165\n"
    "v_exp_f32 v166, v166\n"
    "v_exp_f32 v167, v167\n"
    "v_smfmac_f32_32x32x32_bf16 v[80:95], v[224:227], v[144:151], v240\n"
    "v_exp_f32 v168, v168\n"
    "v_exp_f32 v169, v169\n"
    "v_exp_f32 v170, v170\n"
    "v_exp_f32 v171, v171\n"
    "v_smfmac_f32_32x32x32_bf16 v[80:95], v[228:231], v[152:159], v240\n"
    "v_exp_f32 v172, v172\n"
    "v_exp_f32 v173, v173\n"
    "v_exp_f32 v174, v174\n"
    "v_exp_f32 v175, v175\n"
    "s_waitcnt vmcnt(12)\n"
    "v_mfma_f32_32x32x16_bf16 v[144:159], v[200:203], v[32:35], 0\n"
    "v_exp_f32 v128, v128\n"
    "v_exp_f32 v129, v129\n"
    "v_exp_f32 v130, v130\n"
    "v_exp_f32 v131, v131\n"
    "v_mfma_f32_32x32x16_bf16 v[144:159], v[204:207], v[36:39], v[144:159]\n"
    "v_exp_f32 v132, v132\n"
    "v_exp_f32 v133, v133\n"
    "v_exp_f32 v134, v134\n"
    "v_exp_f32 v135, v135\n"
    "v_smfmac_f32_32x32x32_bf16 v[96:111], v[224:227], v[160:167], v240\n"
    "v_exp_f32 v136, v136\n"
    "v_exp_f32 v137, v137\n"
    "v_exp_f32 v138, v138\n"
    "v_exp_f32 v139, v139\n"
    "v_smfmac_f32_32x32x32_bf16 v[96:111], v[228:231], v[168:175], v240\n"
    "v_exp_f32 v140, v140\n"
    "v_exp_f32 v141, v141\n"
    "v_exp_f32 v142, v142\n"
    "v_exp_f32 v143, v143\n"
    "v_mfma_f32_32x32x16_bf16 v[160:175], v[200:203], v[40:43], 0\n"
    "v_exp_f32 v144, v144\n"
    "v_exp_f32 v145, v145\n"
    "v_exp_f32 v146, v146\n"
    "v_exp_f32 v147, v147\n"
    "v_mfma_f32_32x32x16_bf16 v[160:175], v[204:207], v[44:47], v[160:175]\n"
    "v_exp_f32 v148, v148\n"
    "v_exp_f32 v149, v149\n"
    "v_exp_f32 v150, v150\n"
    "v_exp_f32 v151, v151\n"
    "v_smfmac_f32_32x32x32_bf16 v[112:127], v[224:227], v[128:135], v240\n"
    "v_exp_f32 v152, v152\n"
    "v_exp_f32 v153, v153\n"
    "v_exp_f32 v154, v154\n"
    "v_exp_f32 v155, v155\n"
    "v_smfmac_f32_32x32x32_bf16 v[112:127], v[228:231], v[136:143], v240\n"
    "buffer_load_dwordx4 v[224:227], v242, s[24:27], s89 offen offset:2048 sc1\n"
    "buffer_load_dwordx4 v[228:231], v242, s[24:27], s89 offen offset:3072 sc1\n"
    "v_exp_f32 v156, v156\n"
    "v_exp_f32 v157, v157\n"
    "v_exp_f32 v158, v158\n"
    "v_exp_f32 v159, v159\n"
    "v_mfma_f32_32x32x16_bf16 v[128:143], v[200:203], v[48:51], 0\n"
    "v_exp_f32 v160, v160\n"
    "v_exp_f32 v161, v161\n"
    "v_exp_f32 v162, v162\n"
    "v_exp_f32 v163, v163\n"
    "v_mfma_f32_32x32x16_bf16 v[128:143], v[204:207], v[52:55], v[128:143]\n"
    "v_exp_f32 v164, v164\n"
    "v_exp_f32 v165, v165\n"
    "v_exp_f32 v166, v166\n"
    "v_exp_f32 v167, v167\n"
    "s_waitcnt vmcnt(12)\n"
    "v_smfmac_f32_32x32x32_bf16 v[64:79], v[232:235], v[144:151], v240\n"
    "v_exp_f32 v168, v168\n"
    "v_exp_f32 v169, v169\n"
    "v_exp_f32 v170, v170\n"
    "v_exp_f32 v171, v171\n"
    "v_smfmac_f32_32x32x32_bf16 v[64:79], v[236:239], v[152:159], v240\n"
    "v_exp_f32 v172, v172\n"
    "v_exp_f32 v173, v173\n"
    "v_exp_f32 v174, v174\n"
    "v_exp_f32 v175, v175\n"
    "v_mfma_f32_32x32x16_bf16 v[144:159], v[200:203], v[56:59], 0\n"
    "v_exp_f32 v128, v128\n"
    "v_exp_f32 v129, v129\n"
    "v_exp_f32 v130, v130\n"
    "v_exp_f32 v131, v131\n"
    "v_mfma_f32_32x32x16_bf16 v[144:159], v[204:207], v[60:63], v[144:159]\n"
    "buffer_load_dwordx4 v[200:203], v12, s[24:27], s89 offen offset:0 sc1\n"
    "buffer_load_dwordx4 v[204:207], v12, s[24:27], s89 offen offset:1024 sc1\n"
    "v_exp_f32 v132, v132\n"
    "v_exp_f32 v133, v133\n"
    "v_exp_f32 v134, v134\n"
    "v_exp_f32 v135, v135\n"
    "v_smfmac_f32_32x32x32_bf16 v[80:95], v[232:235], v[160:167], v240\n"
    "v_exp_f32 v136, v136\n"
    "v_exp_f32 v137, v137\n"
    "v_exp_f32 v138, v138\n"
    "v_exp_f32 v139, v139\n"
    "v_smfmac_f32_32x32x32_bf16 v[80:95], v[236:239], v[168:175], v240\n"
    "v_exp_f32 v140, v140\n"
    "v_exp_f32 v141, v141\n"
    "v_exp_f32 v142, v142\n"
    "v_exp_f32 v143, v143\n"
    "s_waitcnt vmcnt(12)\n"
    "v_mfma_f32_32x32x16_bf16 v[160:175], v[176:179], v[32:35], 0\n"
    "v_exp_f32 v144, v144\n"
    "v_exp_f32 v145, v145\n"
    "v_exp_f32 v146, v146\n"
    "v_exp_f32 v147, v147\n"
    "v_mfma_f32_32x32x16_bf16 v[160:175], v[180:183], v[36:39], v[160:175]\n"
    "v_exp_f32 v148, v148\n"
    "v_exp_f32 v149, v149\n"
    "v_exp_f32 v150, v150\n"
    "v_exp_f32 v151, v151\n"
    "v_smfmac_f32_32x32x32_bf16 v[96:111], v[232:235], v[128:135], v240\n"
    "v_exp_f32 v152, v152\n"
    "v_exp_f32 v153, v153\n"
    "v_exp_f32 v154, v154\n"
    "v_exp_f32 v155, v155\n"
    "v_smfmac_f32_32x32x32_bf16 v[96:111], v[236:239], v[136:143], v240\n"
    "v_exp_f32 v156, v156\n"
    "v_exp_f32 v157, v157\n"
    "v_exp_f32 v158, v158\n"
    "v_exp_f32 v159, v159\n"
    "v_mfma_f32_32x32x16_bf16 v[128:143], v[176:179], v[40:43], 0\n"
    "v_exp_f32 v160, v160\n"
    "v_exp_f32 v161, v161\n"
    "v_exp_f32 v162, v162\n"
    "v_exp_f32 v163, v163\n"
    "v_mfma_f32_32x32x16_bf16 v[128:143], v[180:183], v[44:47], v[128:143]\n"
    "v_exp_f32 v164, v164\n"
    "v_exp_f32 v165, v165\n"
    "v_exp_f32 v166, v166\n"
    "v_exp_f32 v167, v167\n"
    "v_smfmac_f32_32x32x32_bf16 v[112:127], v[232:235], v[144:151], v240\n"
    "v_exp_f32 v168, v168\n"
    "v_exp_f32 v169, v169\n"
    "v_exp_f32 v170, v170\n"
    "v_exp_f32 v171, v171\n"
    "v_smfmac_f32_32x32x32_bf16 v[112:127], v[236:239], v[152:159], v240\n"
    "buffer_load_dwordx4 v[232:235], v12, s[24:27], s89 offen offset:2048 sc1\n"
    "buffer_load_dwordx4 v[236:239], v12, s[24:27], s89 offen offset:3072 sc1\n"
    "v_exp_f32 v172, v172\n"
    "v_exp_f32 v173, v173\n"
    "v_exp_f32 v174, v174\n"
    "v_exp_f32 v175, v175\n"
    "v_mfma_f32_32x32x16_bf16 v[144:159], v[176:179], v[48:51], 0\n"
    "v_exp_f32 v128, v128\n"
    "v_exp_f32 v129, v129\n"
    "v_exp_f32 v130, v130\n"
    "v_exp_f32 v131, v131\n"
    "v_mfma_f32_32x32x16_bf16 v[144:159], v[180:183], v[52:55], v[144:159]\n"
    "v_exp_f32 v132, v132\n"
    "v_exp_f32 v133, v133\n"
    "v_exp_f32 v134, v134\n"
    "v_exp_f32 v135, v135\n"
    "s_waitcnt vmcnt(12)\n"
    "v_smfmac_f32_32x32x32_bf16 v[64:79], v[208:211], v[160:167], v240\n"
    "v_exp_f32 v136, v136\n"
    "v_exp_f32 v137, v137\n"
    "v_exp_f32 v138, v138\n"
    "v_exp_f32 v139, v139\n"
    "v_smfmac_f32_32x32x32_bf16 v[64:79], v[212:215], v[168:175], v240\n"
    "v_exp_f32 v140, v140\n"
    "v_exp_f32 v141, v141\n"
    "v_exp_f32 v142, v142\n"
    "v_exp_f32 v143, v143\n"
    "v_mfma_f32_32x32x16_bf16 v[160:175], v[176:179], v[56:59], 0\n"
    "v_exp_f32 v144, v144\n"
    "v_exp_f32 v145, v145\n"
    "v_exp_f32 v146, v146\n"
    "v_exp_f32 v147, v147\n"
    "v_mfma_f32_32x32x16_bf16 v[160:175], v[180:183], v[60:63], v[160:175]\n"
    "buffer_load_dwordx4 v[176:179], v242, s[24:27], s90 offen offset:0 sc1\n"
    "buffer_load_dwordx4 v[180:183], v242, s[24:27], s90 offen offset:1024 sc1\n"
    "v_exp_f32 v148, v148\n"
    "v_exp_f32 v149, v149\n"
    "v_exp_f32 v150, v150\n"
    "v_exp_f32 v151, v151\n"
    "v_smfmac_f32_32x32x32_bf16 v[80:95], v[208:211], v[128:135], v240\n"
    "v_exp_f32 v152, v152\n"
    "v_exp_f32 v153, v153\n"
    "v_exp_f32 v154, v154\n"
    "v_exp_f32 v155, v155\n"
    "v_smfmac_f32_32x32x32_bf16 v[80:95], v[212:215], v[136:143], v240\n"
    "v_exp_f32 v156, v156\n"
    "v_exp_f32 v157, v157\n"
    "v_exp_f32 v158, v158\n"
    "v_exp_f32 v159, v159\n"
    "s_waitcnt vmcnt(12)\n"
    "v_mfma_f32_32x32x16_bf16 v[128:143], v[184:187], v[32:35], 0\n"
    "v_exp_f32 v160, v160\n"
    "v_exp_f32 v161, v161\n"
    "v_exp_f32 v162, v162\n"
    "v_exp_f32 v163, v163\n"
    "v_mfma_f32_32x32x16_bf16 v[128:143], v[188:191], v[36:39], v[128:143]\n"
    "v_exp_f32 v164, v164\n"
    "v_exp_f32 v165, v165\n"
    "v_exp_f32 v166, v166\n"
    "v_exp_f32 v167, v167\n"
    "v_smfmac_f32_32x32x32_bf16 v[96:111], v[208:211], v[144:151], v240\n"
    "v_exp_f32 v168, v168\n"
    "v_exp_f32 v169, v169\n"
    "v_exp_f32 v170, v170\n"
    "v_exp_f32 v171, v171\n"
    "v_smfmac_f32_32x32x32_bf16 v[96:111], v[212:215], v[152:159], v240\n"
    "v_exp_f32 v172, v172\n"
    "v_exp_f32 v173, v173\n"
    "v_exp_f32 v174, v174\n"
    "v_exp_f32 v175, v175\n"
    "v_mfma_f32_32x32x16_bf16 v[144:159], v[184:187], v[40:43], 0\n"
    "v_exp_f32 v128, v128\n"
    "v_exp_f32 v129, v129\n"
    "v_exp_f32 v130, v130\n"
    "v_exp_f32 v131, v131\n"
    "v_mfma_f32_32x32x16_bf16 v[144:159], v[188:191], v[44:47], v[144:159]\n"
    "v_exp_f32 v132, v132\n"
    "v_exp_f32 v133, v133\n"
    "v_exp_f32 v134, v134\n"
    "v_exp_f32 v135, v135\n"
    "v_smfmac_f32_32x32x32_bf16 v[112:127], v[208:211], v[160:167], v240\n"
    "v_exp_f32 v136, v136\n"
    "v_exp_f32 v137, v137\n"
    "v_exp_f32 v138, v138\n"
    "v_exp_f32 v139, v139\n"
    "v_smfmac_f32_32x32x32_bf16 v[112:127], v[212:215], v[168:175], v240\n"
    "buffer_load_dwordx4 v[208:211], v242, s[24:27], s90 offen offset:2048 sc1\n"
    "buffer_load_dwordx4 v[212:215], v242, s[24:27], s90 offen offset:3072 sc1\n"
    "v_exp_f32 v140, v140\n"
    "v_exp_f32 v141, v141\n"
    "v_exp_f32 v142, v142\n"
    "v_exp_f32 v143, v143\n"
    "v_mfma_f32_32x32x16_bf16 v[160:175], v[184:187], v[48:51], 0\n"
    "v_exp_f32 v144, v144\n"
    "v_exp_f32 v145, v145\n"
    "v_exp_f32 v146, v146\n"
    "v_exp_f32 v147, v147\n"
    "v_mfma_f32_32x32x16_bf16 v[160:175], v[188:191], v[52:55], v[160:175]\n"
    "v_exp_f32 v148, v148\n"
    "v_exp_f32 v149, v149\n"
    "v_exp_f32 v150, v150\n"
    "v_exp_f32 v151, v151\n"
    "s_waitcnt vmcnt(12)\n"
    "v_smfmac_f32_32x32x32_bf16 v[64:79], v[216:219], v[128:135], v240\n"
    "v_exp_f32 v152, v152\n"
    "v_exp_f32 v153, v153\n"
    "v_exp_f32 v154, v154\n"
    "v_exp_f32 v155, v155\n"
    "v_smfmac_f32_32x32x32_bf16 v[64:79], v[220:223], v[136:143], v240\n"
    "v_exp_f32 v156, v156\n"
    "v_exp_f32 v157, v157\n"
    "v_exp_f32 v158, v158\n"
    "v_exp_f32 v159, v159\n"
    "v_mfma_f32_32x32x16_bf16 v[128:143], v[184:187], v[56:59], 0\n"
    "v_exp_f32 v160, v160\n"
    "v_exp_f32 v161, v161\n"
    "v_exp_f32 v162, v162\n"
    "v_exp_f32 v163, v163\n"
    "v_mfma_f32_32x32x16_bf16 v[128:143], v[188:191], v[60:63], v[128:143]\n"
    "buffer_load_dwordx4 v[184:187], v12, s[24:27], s90 offen offset:0 sc1\n"
    "buffer_load_dwordx4 v[188:191], v12, s[24:27], s90 offen offset:1024 sc1\n"
    "v_exp_f32 v164, v164\n"
    "v_exp_f32 v165, v165\n"
    "v_exp_f32 v166, v166\n"
    "v_exp_f32 v167, v167\n"
    "v_smfmac_f32_32x32x32_bf16 v[80:95], v[216:219], v[144:151], v240\n"
    "v_exp_f32 v168, v168\n"
    "v_exp_f32 v169, v169\n"
    "v_exp_f32 v170, v170\n"
    "v_exp_f32 v171, v171\n"
    "v_smfmac_f32_32x32x32_bf16 v[80:95], v[220:223], v[152:159], v240\n"
    "v_exp_f32 v172, v172\n"
    "v_exp_f32 v173, v173\n"
    "v_exp_f32 v174, v174\n"
    "v_exp_f32 v175, v175\n"
    "s_waitcnt vmcnt(12)\n"
    "v_mfma_f32_32x32x16_bf16 v[144:159], v[192:195], v[32:35], 0\n"
    "v_exp_f32 v128, v128\n"
    "v_exp_f32 v129, v129\n"
    "v_exp_f32 v130, v130\n"
    "v_exp_f32 v131, v131\n"
    "v_mfma_f32_32x32x16_bf16 v[144:159], v[196:199], v[36:39], v[144:159]\n"
    "v_exp_f32 v132, v132\n"
    "v_exp_f32 v133, v133\n"
    "v_exp_f32 v134, v134\n"
    "v_exp_f32 v135, v135\n"
    "v_smfmac_f32_32x32x32_bf16 v[96:111], v[216:219], v[160:167], v240\n"
    "v_exp_f32 v136, v136\n"
    "v_exp_f32 v137, v137\n"
    "v_exp_f32 v138, v138\n"
    "v_exp_f32 v139, v139\n"
    "v_smfmac_f32_32x32x32_bf16 v[96:111], v[220:223], v[168:175], v240\n"
    "v_exp_f32 v140, v140\n"
    "v_exp_f32 v141, v141\n"
    "v_exp_f32 v142, v142\n"
    "v_exp_f32 v143, v143\n"
    "v_mfma_f32_32x32x16_bf16 v[160:175], v[192:195], v[40:43], 0\n"
    "v_exp_f32 v144, v144\n"
    "v_exp_f32 v145, v145\n"
    "v_exp_f32 v146, v146\n"
    "v_exp_f32 v147, v147\n"
    "v_mfma_f32_32x32x16_bf16 v[160:175], v[196:199], v[44:47], v[160:175]\n"
    "v_exp_f32 v148, v148\n"
    "v_exp_f32 v149, v149\n"
    "v_exp_f32 v150, v150\n"
    "v_exp_f32 v151, v151\n"
    "v_smfmac_f32_32x32x32_bf16 v[112:127], v[216:219], v[128:135], v240\n"
    "v_exp_f32 v152, v152\n"
    "v_exp_f32 v153, v153\n"
    "v_exp_f32 v154, v154\n"
    "v_exp_f32 v155, v155\n"
    "v_smfmac_f32_32x32x32_bf16 v[112:127], v[220:223], v[136:143], v240\n"
    "buffer_load_dwordx4 v[216:219], v12, s[24:27], s90 offen offset:2048 sc1\n"
    "buffer_load_dwordx4 v[220:223], v12, s[24:27], s90 offen offset:3072 sc1\n"
    "v_exp_f32 v156, v156\n"
    "v_exp_f32 v157, v157\n"
    "v_exp_f32 v158, v158\n"
    "v_exp_f32 v159, v159\n"
    "v_mfma_f32_32x32x16_bf16 v[128:143], v[192:195], v[48:51], 0\n"
    "v_exp_f32 v160, v160\n"
    "v_exp_f32 v161, v161\n"
    "v_exp_f32 v162, v162\n"
    "v_exp_f32 v163, v163\n"
    "v_mfma_f32_32x32x16_bf16 v[128:143], v[196:199], v[52:55], v[128:143]\n"
    "v_exp_f32 v164, v164\n"
    "v_exp_f32 v165, v165\n"
    "v_exp_f32 v166, v166\n"
    "v_exp_f32 v167, v167\n"
    "s_waitcnt vmcnt(12)\n"
    "v_smfmac_f32_32x32x32_bf16 v[64:79], v[224:227], v[144:151], v240\n"
    "v_exp_f32 v168, v168\n"
    "v_exp_f32 v169, v169\n"
    "v_exp_f32 v170, v170\n"
    "v_exp_f32 v171, v171\n"
    "v_smfmac_f32_32x32x32_bf16 v[64:79], v[228:231], v[152:159], v240\n"
    "v_exp_f32 v172, v172\n"
    "v_exp_f32 v173, v173\n"
    "v_exp_f32 v174, v174\n"
    "v_exp_f32 v175, v175\n"
    "v_mfma_f32_32x32x16_bf16 v[144:159], v[192:195], v[56:59], 0\n"
    "v_exp_f32 v128, v128\n"
    "v_exp_f32 v129, v129\n"
    "v_exp_f32 v130, v130\n"
    "v_exp_f32 v131, v131\n"
    "v_mfma_f32_32x32x16_bf16 v[144:159], v[196:199], v[60:63], v[144:159]\n"
    "buffer_load_dwordx4 v[192:195], v242, s[24:27], s91 offen offset:0 sc1\n"
    "buffer_load_dwordx4 v[196:199], v242, s[24:27], s91 offen offset:1024 sc1\n"
    "v_exp_f32 v132, v132\n"
    "v_exp_f32 v133, v133\n"
    "v_exp_f32 v134, v134\n"
    "v_exp_f32 v135, v135\n"
    "v_smfmac_f32_32x32x32_bf16 v[80:95], v[224:227], v[160:167], v240\n"
    "v_exp_f32 v136, v136\n"
    "v_exp_f32 v137, v137\n"
    "v_exp_f32 v138, v138\n"
    "v_exp_f32 v139, v139\n"
    "v_smfmac_f32_32x32x32_bf16 v[80:95], v[228:231], v[168:175], v240\n"
    "v_exp_f32 v140, v140\n"
    "v_exp_f32 v141, v141\n"
    "v_exp_f32 v142, v142\n"
    "v_exp_f32 v143, v143\n"
    "s_waitcnt vmcnt(12)\n"
    "v_mfma_f32_32x32x16_bf16 v[160:175], v[200:203], v[32:35], 0\n"
    "v_exp_f32 v144, v144\n"
    "v_exp_f32 v145, v145\n"
    "v_exp_f32 v146, v146\n"
    "v_exp_f32 v147, v147\n"
    "v_mfma_f32_32x32x16_bf16 v[160:175], v[204:207], v[36:39], v[160:175]\n"
    "v_exp_f32 v148, v148\n"
    "v_exp_f32 v149, v149\n"
    "v_exp_f32 v150, v150\n"
    "v_exp_f32 v151, v151\n"
    "v_smfmac_f32_32x32x32_bf16 v[96:111], v[224:227], v[128:135], v240\n"
    "v_exp_f32 v152, v152\n"
    "v_exp_f32 v153, v153\n"
    "v_exp_f32 v154, v154\n"
    "v_exp_f32 v155, v155\n"
    "v_smfmac_f32_32x32x32_bf16 v[96:111], v[228:231], v[136:143], v240\n"
    "v_exp_f32 v156, v156\n"
    "v_exp_f32 v157, v157\n"
    "v_exp_f32 v158, v158\n"
    "v_exp_f32 v159, v159\n"
    "v_mfma_f32_32x32x16_bf16 v[128:143], v[200:203], v[40:43], 0\n"
    "v_exp_f32 v160, v160\n"
    "v_exp_f32 v161, v161\n"
    "v_exp_f32 v162, v162\n"
    "v_exp_f32 v163, v163\n"
    "v_mfma_f32_32x32x16_bf16 v[128:143], v[204:207], v[44:47], v[128:143]\n"
    "v_exp_f32 v164, v164\n"
    "v_exp_f32 v165, v165\n"
    "v_exp_f32 v166, v166\n"
    "v_exp_f32 v167, v167\n"
    "v_smfmac_f32_32x32x32_bf16 v[112:127], v[224:227], v[144:151], v240\n"
    "v_exp_f32 v168, v168\n"
    "v_exp_f32 v169, v169\n"
    "v_exp_f32 v170, v170\n"
    "v_exp_f32 v171, v171\n"
    "v_smfmac_f32_32x32x32_bf16 v[112:127], v[228:231], v[152:159], v240\n"
    "buffer_load_dwordx4 v[224:227], v242, s[24:27], s91 offen offset:2048 sc1\n"
    "buffer_load_dwordx4 v[228:231], v242, s[24:27], s91 offen offset:3072 sc1\n"
    "v_exp_f32 v172, v172\n"
    "v_exp_f32 v173, v173\n"
    "v_exp_f32 v174, v174\n"
    "v_exp_f32 v175, v175\n"
    "v_mfma_f32_32x32x16_bf16 v[144:159], v[200:203], v[48:51], 0\n"
    "v_exp_f32 v128, v128\n"
    "v_exp_f32 v129, v129\n"
    "v_exp_f32 v130, v130\n"
    "v_exp_f32 v131, v131\n"
    "v_mfma_f32_32x32x16_bf16 v[144:159], v[204:207], v[52:55], v[144:159]\n"
    "v_exp_f32 v132, v132\n"
    "v_exp_f32 v133, v133\n"
    "v_exp_f32 v134, v134\n"
    "v_exp_f32 v135, v135\n"
    "s_waitcnt vmcnt(12)\n"
    "v_smfmac_f32_32x32x32_bf16 v[64:79], v[232:235], v[160:167], v240\n"
    "v_exp_f32 v136, v136\n"
    "v_exp_f32 v137, v137\n"
    "v_exp_f32 v138, v138\n"
    "v_exp_f32 v139, v139\n"
    "v_smfmac_f32_32x32x32_bf16 v[64:79], v[236:239], v[168:175], v240\n"
    "v_exp_f32 v140, v140\n"
    "v_exp_f32 v141, v141\n"
    "v_exp_f32 v142, v142\n"
    "v_exp_f32 v143, v143\n"
    "v_mfma_f32_32x32x16_bf16 v[160:175], v[200:203], v[56:59], 0\n"
    "v_exp_f32 v144, v144\n"
    "v_exp_f32 v145, v145\n"
    "v_exp_f32 v146, v146\n"
    "v_exp_f32 v147, v147\n"
    "v_mfma_f32_32x32x16_bf16 v[160:175], v[204:207], v[60:63], v[160:175]\n"
    "buffer_load_dwordx4 v[200:203], v12, s[24:27], s91 offen offset:0 sc1\n"
    "buffer_load_dwordx4 v[204:207], v12, s[24:27], s91 offen offset:1024 sc1\n"
    "v_exp_f32 v148, v148\n"
    "v_exp_f32 v149, v149\n"
    "v_exp_f32 v150, v150\n"
    "v_exp_f32 v151, v151\n"
    "v_smfmac_f32_32x32x32_bf16 v[80:95], v[232:235], v[128:135], v240\n"
    "v_exp_f32 v152, v152\n"
    "v_exp_f32 v153, v153\n"
    "v_exp_f32 v154, v154\n"
    "v_exp_f32 v155, v155\n"
    "v_smfmac_f32_32x32x32_bf16 v[80:95], v[236:239], v[136:143], v240\n"
    "v_exp_f32 v156, v156\n"
    "v_exp_f32 v157, v157\n"
    "v_exp_f32 v158, v158\n"
    "v_exp_f32 v159, v159\n"
    "s_waitcnt vmcnt(12)\n"
    "v_mfma_f32_32x32x16_bf16 v[128:143], v[176:179], v[32:35], 0\n"
    "v_exp_f32 v160, v160\n"
    "v_exp_f32 v161, v161\n"
    "v_exp_f32 v162, v162\n"
    "v_exp_f32 v163, v163\n"
    "v_mfma_f32_32x32x16_bf16 v[128:143], v[180:183], v[36:39], v[128:143]\n"
    "v_exp_f32 v164, v164\n"
    "v_exp_f32 v165, v165\n"
    "v_exp_f32 v166, v166\n"
    "v_exp_f32 v167, v167\n"
    "v_smfmac_f32_32x32x32_bf16 v[96:111], v[232:235], v[144:151], v240\n"
    "v_exp_f32 v168, v168\n"
    "v_exp_f32 v169, v169\n"
    "v_exp_f32 v170, v170\n"
    "v_exp_f32 v171, v171\n"
    "v_smfmac_f32_32x32x32_bf16 v[96:111], v[236:239], v[152:159], v240\n"
    "v_exp_f32 v172, v172\n"
    "v_exp_f32 v173, v173\n"
    "v_exp_f32 v174, v174\n"
    "v_exp_f32 v175, v175\n"
    "s_waitcnt vmcnt(8)\n"
    "v_mfma_f32_32x32x16_bf16 v[144:159], v[184:187], v[32:35], 0\n"
    "v_exp_f32 v128, v128\n"
    "v_exp_f32 v129, v129\n"
    "v_exp_f32 v130, v130\n"
    "v_exp_f32 v131, v131\n"
    "v_mfma_f32_32x32x16_bf16 v[144:159], v[188:191], v[36:39], v[144:159]\n"
    "v_exp_f32 v132, v132\n"
    "v_exp_f32 v133, v133\n"
    "v_exp_f32 v134, v134\n"
    "v_exp_f32 v135, v135\n"
    "v_smfmac_f32_32x32x32_bf16 v[112:127], v[232:235], v[160:167], v240\n"
    "v_exp_f32 v136, v136\n"
    "v_exp_f32 v137, v137\n"
    "v_exp_f32 v138, v138\n"
    "v_exp_f32 v139, v139\n"
    "v_smfmac_f32_32x32x32_bf16 v[112:127], v[236:239], v[168:175], v240\n"
    "buffer_load_dwordx4 v[232:235], v12, s[24:27], s91 offen offset:2048 sc1\n"
    "buffer_load_dwordx4 v[236:239], v12, s[24:27], s91 offen offset:3072 sc1\n"
    "v_exp_f32 v140, v140\n"
    "v_exp_f32 v141, v141\n"
    "v_exp_f32 v142, v142\n"
    "v_exp_f32 v143, v143\n"
    "s_waitcnt vmcnt(6)\n"
    "v_mfma_f32_32x32x16_bf16 v[160:175], v[192:195], v[32:35], 0\n"
    "v_exp_f32 v144, v144\n"
    "v_exp_f32 v145, v145\n"
    "v_exp_f32 v146, v146\n"
    "v_exp_f32 v147, v147\n"
    "v_mfma_f32_32x32x16_bf16 v[160:175], v[196:199], v[36:39], v[160:175]\n"
    "v_exp_f32 v148, v148\n"
    "v_exp_f32 v149, v149\n"
    "v_exp_f32 v150, v150\n"
    "v_exp_f32 v151, v151\n"
    "v_smfmac_f32_32x32x32_bf16 v[64:79], v[208:211], v[128:135], v240\n"
    "v_exp_f32 v152, v152\n"
    "v_exp_f32 v153, v153\n"
    "v_exp_f32 v154, v154\n"
    "v_exp_f32 v155, v155\n"
    "v_smfmac_f32_32x32x32_bf16 v[64:79], v[212:215], v[136:143], v240\n"
    "v_exp_f32 v156, v156\n"
    "v_exp_f32 v157, v157\n"
    "v_exp_f32 v158, v158\n"
    "v_exp_f32 v159, v159\n"
    "s_waitcnt vmcnt(2)\n"
    "v_mfma_f32_32x32x16_bf16 v[128:143], v[200:203], v[32:35], 0\n"
    "v_exp_f32 v160, v160\n"
    "v_exp_f32 v161, v161\n"
    "v_exp_f32 v162, v162\n"
    "v_exp_f32 v163, v163\n"
    "v_mfma_f32_32x32x16_bf16 v[128:143], v[204:207], v[36:39], v[128:143]\n"
    "v_exp_f32 v164, v164\n"
    "v_exp_f32 v165, v165\n"
    "v_exp_f32 v166, v166\n"
    "v_exp_f32 v167, v167\n"
    "v_smfmac_f32_32x32x32_bf16 v[64:79], v[216:219], v[144:151], v240\n"
    "v_exp_f32 v168, v168\n"
    "v_exp_f32 v169, v169\n"
    "v_exp_f32 v170, v170\n"
    "v_exp_f32 v171, v171\n"
    "v_smfmac_f32_32x32x32_bf16 v[64:79], v[220:223], v[152:159], v240\n"
    "v_exp_f32 v172, v172\n"
    "v_exp_f32 v173, v173\n"
    "v_exp_f32 v174, v174\n"
    "v_exp_f32 v175, v175\n"
    "v_mfma_f32_32x32x16_bf16 v[144:159], v[176:179], v[40:43], 0\n"
    "v_exp_f32 v128, v128\n"
    "v_exp_f32 v129, v129\n"
    "v_exp_f32 v130, v130\n"
    "v_exp_f32 v131, v131\n"
    "v_mfma_f32_32x32x16_bf16 v[144:159], v[180:183], v[44:47], v[144:159]\n"
    "v_exp_f32 v132, v132\n"
    "v_exp_f32 v133, v133\n"
    "v_exp_f32 v134, v134\n"
    "v_exp_f32 v135, v135\n"
    "v_smfmac_f32_32x32x32_bf16 v[64:79], v[224:227], v[160:167], v240\n"
    "v_exp_f32 v136, v136\n"
    "v_exp_f32 v137, v137\n"
    "v_exp_f32 v138, v138\n"
    "v_exp_f32 v139, v139\n"
    "v_smfmac_f32_32x32x32_bf16 v[64:79], v[228:231], v[168:175], v240\n"
    "v_exp_f32 v140, v140\n"
    "v_exp_f32 v141, v141\n"
    "v_exp_f32 v142, v142\n"
    "v_exp_f32 v143, v143\n"
    "v_mfma_f32_32x32x16_bf16 v[160:175], v[184:187], v[40:43], 0\n"
    "v_exp_f32 v144, v144\n"
    "v_exp_f32 v145, v145\n"
    "v_exp_f32 v146, v146\n"
    "v_exp_f32 v147, v147\n"
    "v_mfma_f32_32x32x16_bf16 v[160:175], v[188:191], v[44:47], v[160:175]\n"
    "v_exp_f32 v148, v148\n"
    "v_exp_f32 v149, v149\n"
    "v_exp_f32 v150, v150\n"
    "v_exp_f32 v151, v151\n"
    "s_waitcnt vmcnt(0)\n"
    "v_smfmac_f32_32x32x32_bf16 v[64:79], v[232:235], v[128:135], v240\n"
    "v_exp_f32 v152, v152\n"
    "v_exp_f32 v153, v153\n"
    "v_exp_f32 v154, v154\n"
    "v_exp_f32 v155, v155\n"
    "v_smfmac_f32_32x32x32_bf16 v[64:79], v[236:239], v[136:143], v240\n"
    "v_exp_f32 v156, v156\n"
    "v_exp_f32 v157, v157\n"
    "v_exp_f32 v158, v158\n"
    "v_exp_f32 v159, v159\n"
    "v_mfma_f32_32x32x16_bf16 v[128:143], v[192:195], v[40:43], 0\n"
    "v_exp_f32 v160, v160\n"
    "v_exp_f32 v161, v161\n"
    "v_exp_f32 v162, v162\n"
    "v_exp_f32 v163, v163\n"
    "v_mfma_f32_32x32x16_bf16 v[128:143], v[196:199], v[44:47], v[128:143]\n"
    "v_exp_f32 v164, v164\n"
    "v_exp_f32 v165, v165\n"
    "v_exp_f32 v166, v166\n"
    "v_exp_f32 v167, v167\n"
    "v_smfmac_f32_32x32x32_bf16 v[80:95], v[208:211], v[144:151], v240\n"
    "v_exp_f32 v168, v168\n"
    "v_exp_f32 v169, v169\n"
    "v_exp_f32 v170, v170\n"
    "v_exp_f32 v171, v171\n"
    "v_smfmac_f32_32x32x32_bf16 v[80:95], v[212:215], v[152:159], v240\n"
    "ds_write_b128 v249, v[64:67] offset:0\n"
    "v_exp_f32 v172, v172\n"
    "v_exp_f32 v173, v173\n"
    "v_exp_f32 v174, v174\n"
    "v_exp_f32 v175, v175\n"
    "v_mfma_f32_32x32x16_bf16 v[144:159], v[200:203], v[40:43], 0\n"
    "v_exp_f32 v128, v128\n"
    "v_exp_f32 v129, v129\n"
    "v_exp_f32 v130, v130\n"
    "v_exp_f32 v131, v131\n"
    "v_mfma_f32_32x32x16_bf16 v[144:159], v[204:207], v[44:47], v[144:159]\n"
    "ds_write_b128 v249, v[68:71] offset:1024\n"
    "v_exp_f32 v132, v132\n"
    "v_exp_f32 v133, v133\n"
    "v_exp_f32 v134, v134\n"
    "v_exp_f32 v135, v135\n"
    "v_smfmac_f32_32x32x32_bf16 v[80:95], v[216:219], v[160:167], v240\n"
    "v_exp_f32 v136, v136\n"
    "v_exp_f32 v137, v137\n"
    "v_exp_f32 v138, v138\n"
    "v_exp_f32 v139, v139\n"
    "v_smfmac_f32_32x32x32_bf16 v[80:95], v[220:223], v[168:175], v240\n"
    "ds_write_b128 v249, v[72:75] offset:2048\n"
    "v_exp_f32 v140, v140\n"
    "v_exp_f32 v141, v141\n"
    "v_exp_f32 v142, v142\n"
    "v_exp_f32 v143, v143\n"
    "v_mfma_f32_32x32x16_bf16 v[160:175], v[176:179], v[48:51], 0\n"
    "v_exp_f32 v144, v144\n"
    "v_exp_f32 v145, v145\n"
    "v_exp_f32 v146, v146\n"
    "v_exp_f32 v147, v147\n"
    "v_mfma_f32_32x32x16_bf16 v[160:175], v[180:183], v[52:55], v[160:175]\n"
    "ds_write_b128 v249, v[76:79] offset:3072\n"
    "v_exp_f32 v148, v148\n"
    "v_exp_f32 v149, v149\n"
    "v_exp_f32 v150, v150\n"
    "v_exp_f32 v151, v151\n"
    "v_smfmac_f32_32x32x32_bf16 v[80:95], v[224:227], v[128:135], v240\n"
    "v_exp_f32 v152, v152\n"
    "v_exp_f32 v153, v153\n"
    "v_exp_f32 v154, v154\n"
    "v_exp_f32 v155, v155\n"
    "v_smfmac_f32_32x32x32_bf16 v[80:95], v[228:231], v[136:143], v240\n"
    "v_exp_f32 v156, v156\n"
    "v_exp_f32 v157, v157\n"
    "v_exp_f32 v158, v158\n"
    "v_exp_f32 v159, v159\n"
    "v_mfma_f32_32x32x16_bf16 v[128:143], v[184:187], v[48:51], 0\n"
    "v_exp_f32 v160, v160\n"
    "v_exp_f32 v161, v161\n"
    "v_exp_f32 v162, v162\n"
    "v_exp_f32 v163, v163\n"
    "v_mfma_f32_32x32x16_bf16 v[128:143], v[188:191], v[52:55], v[128:143]\n"
    "v_exp_f32 v164, v164\n"
    "v_exp_f32 v165, v165\n"
    "v_exp_f32 v166, v166\n"
    "v_exp_f32 v167, v167\n"
    "v_smfmac_f32_32x32x32_bf16 v[80:95], v[232:235], v[144:151], v240\n"
    "s_mov_b64 exec, s[92:93]\n"
    "buffer_load_dwordx2 v[14:15], v16, s[24:27], 0 offen sc1\n"
    "s_mov_b64 exec, -1\n"
    "v_exp_f32 v168, v168\n"
    "v_exp_f32 v169, v169\n"
    "v_exp_f32 v170, v170\n"
    "v_exp_f32 v171, v171\n"
    "v_smfmac_f32_32x32x32_bf16 v[80:95], v[236:239], v[152:159], v240\n"
    "v_exp_f32 v172, v172\n"
    "v_exp_f32 v173, v173\n"
    "v_exp_f32 v174, v174\n"
    "v_exp_f32 v175, v175\n"
    "v_mfma_f32_32x32x16_bf16 v[144:159], v[192:195], v[48:51], 0\n"
    "v_exp_f32 v128, v128\n"
    "v_exp_f32 v129, v129\n"
    "v_exp_f32 v130, v130\n"
    "v_exp_f32 v131, v131\n"
    "v_mfma_f32_32x32x16_bf16 v[144:159], v[196:199], v[52:55], v[144:159]\n"
    "v_exp_f32 v132, v132\n"
    "v_exp_f32 v133, v133\n"
    "v_exp_f32 v134, v134\n"
    "v_exp_f32 v135, v135\n"
    "v_smfmac_f32_32x32x32_bf16 v[96:111], v[208:211], v[160:167], v240\n"
    "v_exp_f32 v136, v136\n"
    "v_exp_f32 v137, v137\n"
    "v_exp_f32 v138, v138\n"
    "v_exp_f32 v139, v139\n"
    "v_smfmac_f32_32x32x32_bf16 v[96:111], v[212:215], v[168:175], v240\n"
    "ds_write_b128 v249, v[80:83] offset:4096\n"
    "v_exp_f32 v140, v140\n"
    "v_exp_f32 v141, v141\n"
    "v_exp_f32 v142, v142\n"
    "v_exp_f32 v143, v143\n"
    "v_mfma_f32_32x32x16_bf16 v[160:175], v[200:203], v[48:51], 0\n"
    "v_exp_f32 v144, v144\n"
    "v_exp_f32 v145, v145\n"
    "v_exp_f32 v146, v146\n"
    "v_exp_f32 v147, v147\n"
    "v_mfma_f32_32x32x16_bf16 v[160:175], v[204:207], v[52:55], v[160:175]\n"
    "ds_write_b128 v249, v[84:87] offset:5120\n"
    "v_exp_f32 v148, v148\n"
    "v_exp_f32 v149, v149\n"
    "v_exp_f32 v150, v150\n"
    "v_exp_f32 v151, v151\n"
    "v_smfmac_f32_32x32x32_bf16 v[96:111], v[216:219], v[128:135], v240\n"
    "v_exp_f32 v152, v152\n"
    "v_exp_f32 v153, v153\n"
    "v_exp_f32 v154, v154\n"
    "v_exp_f32 v155, v155\n"
    "v_smfmac_f32_32x32x32_bf16 v[96:111], v[220:223], v[136:143], v240\n"
    "ds_write_b128 v249, v[88:91] offset:6144\n"
    "v_exp_f32 v156, v156\n"
    "v_exp_f32 v157, v157\n"
    "v_exp_f32 v158, v158\n"
    "v_exp_f32 v159, v159\n"
    "v_mfma_f32_32x32x16_bf16 v[128:143], v[176:179], v[56:59], 0\n"
    "v_exp_f32 v160, v160\n"
    "v_exp_f32 v161, v161\n"
    "v_exp_f32 v162, v162\n"
    "v_exp_f32 v163, v163\n"
    "v_mfma_f32_32x32x16_bf16 v[128:143], v[180:183], v[60:63], v[128:143]\n"
    "ds_write_b128 v249, v[92:95] offset:7168\n"
    "ds_add_u32 v245, v244 offset:32\n"
    "v_exp_f32 v164, v164\n"
    "v_exp_f32 v165, v165\n"
    "v_exp_f32 v166, v166\n"
    "v_exp_f32 v167, v167\n"
    "v_smfmac_f32_32x32x32_bf16 v[96:111], v[224:227], v[144:151], v240\n"
    "v_exp_f32 v168, v168\n"
    "v_exp_f32 v169, v169\n"
    "v_exp_f32 v170, v170\n"
    "v_exp_f32 v171, v171\n"
    "v_smfmac_f32_32x32x32_bf16 v[96:111], v[228:231], v[152:159], v240\n"
    "v_exp_f32 v172, v172\n"
    "v_exp_f32 v173, v173\n"
    "v_exp_f32 v174, v174\n"
    "v_exp_f32 v175, v175\n"
    "v_mfma_f32_32x32x16_bf16 v[144:159], v[184:187], v[56:59], 0\n"
    "v_exp_f32 v128, v128\n"
    "v_exp_f32 v129, v129\n"
    "v_exp_f32 v130, v130\n"
    "v_exp_f32 v131, v131\n"
    "v_mfma_f32_32x32x16_bf16 v[144:159], v[188:191], v[60:63], v[144:159]\n"
    "v_exp_f32 v132, v132\n"
    "v_exp_f32 v133, v133\n"
    "v_exp_f32 v134, v134\n"
    "v_exp_f32 v135, v135\n"
    "v_smfmac_f32_32x32x32_bf16 v[96:111], v[232:235], v[160:167], v240\n"
    "v_exp_f32 v136, v136\n"
    "v_exp_f32 v137, v137\n"
    "v_exp_f32 v138, v138\n"
    "v_exp_f32 v139, v139\n"
    "v_smfmac_f32_32x32x32_bf16 v[96:111], v[236:239], v[168:175], v240\n"
    "v_exp_f32 v140, v140\n"
    "v_exp_f32 v141, v141\n"
    "v_exp_f32 v142, v142\n"
    "v_exp_f32 v143, v143\n"
    "v_mfma_f32_32x32x16_bf16 v[160:175], v[192:195], v[56:59], 0\n"
    "v_exp_f32 v144, v144\n"
    "v_exp_f32 v145, v145\n"
    "v_exp_f32 v146, v146\n"
    "v_exp_f32 v147, v147\n"
    "v_mfma_f32_32x32x16_bf16 v[160:175], v[196:199], v[60:63], v[160:175]\n"
    "v_exp_f32 v148, v148\n"
    "v_exp_f32 v149, v149\n"
    "v_exp_f32 v150, v150\n"
    "v_exp_f32 v151, v151\n"
    "v_smfmac_f32_32x32x32_bf16 v[112:127], v[208:211], v[128:135], v240\n"
    "v_exp_f32 v152, v152\n"
    "v_exp_f32 v153, v153\n"
    "v_exp_f32 v154, v154\n"
    "v_exp_f32 v155, v155\n"
    "v_smfmac_f32_32x32x32_bf16 v[112:127], v[212:215], v[136:143], v240\n"
    "ds_write_b128 v249, v[96:99] offset:8192\n"
    "v_exp_f32 v156, v156\n"
    "v_exp_f32 v157, v157\n"
    "v_exp_f32 v158, v158\n"
    "v_exp_f32 v159, v159\n"
    "v_mfma_f32_32x32x16_bf16 v[128:143], v[200:203], v[56:59], 0\n"
    "v_exp_f32 v160, v160\n"
    "v_exp_f32 v161, v161\n"
    "v_exp_f32 v162, v162\n"
    "v_exp_f32 v163, v163\n"
    "v_mfma_f32_32x32x16_bf16 v[128:143], v[204:207], v[60:63], v[128:143]\n"
    "ds_write_b128 v249, v[100:103] offset:9216\n"
    "v_exp_f32 v164, v164\n"
    "v_exp_f32 v165, v165\n"
    "v_exp_f32 v166, v166\n"
    "v_exp_f32 v167, v167\n"
    "v_smfmac_f32_32x32x32_bf16 v[112:127], v[216:219], v[144:151], v240\n"
    "v_exp_f32 v168, v168\n"
    "v_exp_f32 v169, v169\n"
    "v_exp_f32 v170, v170\n"
    "v_exp_f32 v171, v171\n"
    "v_smfmac_f32_32x32x32_bf16 v[112:127], v[220:223], v[152:159], v240\n"
    "ds_write_b128 v249, v[104:107] offset:10240\n"
    "v_exp_f32 v172, v172\n"
    "v_exp_f32 v173, v173\n"
    "v_exp_f32 v174, v174\n"
    "v_exp_f32 v175, v175\n"
    "ds_write_b128 v249, v[108:111] offset:11264\n"
    "v_exp_f32 v128, v128\n"
    "v_exp_f32 v129, v129\n"
    "v_exp_f32 v130, v130\n"
    "v_exp_f32 v131, v131\n"
    "v_smfmac_f32_32x32x32_bf16 v[112:127], v[224:227], v[160:167], v240\n"
    "v_exp_f32 v132, v132\n"
    "v_exp_f32 v133, v133\n"
    "v_exp_f32 v134, v134\n"
    "v_exp_f32 v135, v135\n"
    "v_smfmac_f32_32x32x32_bf16 v[112:127], v[228:231], v[168:175], v240\n"
    "v_exp_f32 v136, v136\n"
    "v_exp_f32 v137, v137\n"
    "v_exp_f32 v138, v138\n"
    "v_exp_f32 v139, v139\n"
    "v_smfmac_f32_32x32x32_bf16 v[112:127], v[232:235], v[128:135], v240\n"
    "v_exp_f32 v140, v140\n"
    "v_exp_f32 v141, v141\n"
    "v_exp_f32 v142, v142\n"
    "v_exp_f32 v143, v143\n"
    "s_nop 1\n"
    "v_smfmac_f32_32x32x32_bf16 v[112:127], v[236:239], v[136:143], v240\n"
    "s_nop 11\n"
    "ds_write_b128 v249, v[112:115] offset:12288\n"
    "ds_write_b128 v249, v[116:119] offset:13312\n"
    "ds_write_b128 v249, v[120:123] offset:14336\n"
    "ds_write_b128 v249, v[124:127] offset:15360\n"
    "ds_add_u32 v245, v244 offset:36\n"
    "s_setprio 0\n"
    "s_cmp_eq_u32 s42, 7\n"
    "s_cbranch_scc1 .LfEND\n"
    "s_lshl_b32 s60, s42, 18\n"
    "s_lshl2_add_u32 s61, s41, s40\n"
    "s_lshl_b32 s62, s61, 6\n"
    "s_add_u32 s60, s60, s62\n"
    "s_add_u32 s60, s60, 0x300000\n"
    "s_add_u32 s62, s60, 0x20000\n"
    "s_mul_i32 s61, s61, 28672\n"
    "s_lshl_b32 s63, s42, 7\n"
    "s_add_u32 s61, s61, s63\n"
    "s_add_u32 s61, s61, 896\n"
    "s_add_u32 s63, s61, 14336\n"
    "s_waitcnt vmcnt(0)\n"
    "v_cmp_ne_u64 vcc, s[44:45], v[14:15]\n"
    "s_nop 0\n"
    "s_and_b64 vcc, vcc, s[92:93]\n"
    "s_cbranch_scc1 .LfS60\n"
    ".LfB61:\n"
    "ds_add_u32 v245, v244 offset:64\n"
    ".LfL62:\n"
    "s_sleep 1\n"
    "ds_read_b32 v250, v243 offset:64\n"
    "s_add_u32 s49, s49, 1\n"
    "s_cmp_lt_u32 s49, 0x100000\n"
    "s_cselect_b32 s50, 8, 0\n"
    "s_waitcnt lgkmcnt(0)\n"
    "v_cmp_gt_u32 vcc, s50, v250\n"
    "s_cbranch_vccnz .LfL62\n"
    "buffer_load_dwordx4 v[40:43], v17, s[24:27], s60 offen sc1\n"
    "buffer_load_dwordx4 v[44:47], v17, s[24:27], s62 offen sc1\n"
    "v_lshrrev_b32 v25, 3, v241\n"
    "v_and_b32 v26, 7, v241\n"
    "v_mul_u32_u24 v27, 1792, v25\n"
    "v_lshl_add_u32 v24, v26, 4, v27\n"
    "s_lshl_b32 s58, s40, 11\n"
    "s_add_u32 s58, s58, 0x25000\n"
    "v_lshrrev_b32 v27, 5, v241\n"
    "v_xor_b32 v27, v27, v26\n"
    "v_lshlrev_b32 v27, 4, v27\n"
    "v_lshl_add_u32 v27, v26, 8, v27\n"
    "v_and_b32 v25, 3, v25\n"
    "v_lshl_add_u32 v27, v25, 2, v27\n"
    "v_add_u32 v20, s58, v27\n"
    "v_xor_b32 v21, 64, v20\n"
    "v_xor_b32 v22, 32, v20\n"
    "v_xor_b32 v23, 0x60, v20\n"
    "v_lshrrev_b32 v25, 4, v241\n"
    "v_xor_b32 v25, v25, v241\n"
    "v_lshl_add_u32 v18, v25, 4, s58\n"
    "v_xor_b32 v19, 64, v18\n"
    "s_waitcnt vmcnt(0)\n"
    "ds_write_b128 v18, v[40:43]\n"
    "ds_write_b128 v19, v[44:47] offset:1024\n"
    "ds_read_b32 v48, v20 offset:0\n"
    "ds_read_b32 v49, v21 offset:0\n"
    "ds_read_b32 v50, v20 offset:128\n"
    "ds_read_b32 v51, v21 offset:128\n"
    "ds_read_b32 v52, v22 offset:0\n"
    "ds_read_b32 v53, v23 offset:0\n"
    "ds_read_b32 v54, v22 offset:128\n"
    "ds_read_b32 v55, v23 offset:128\n"
    "s_waitcnt lgkmcnt(4)\n"
    "buffer_store_dwordx4 v[48:51], v24, s[20:23], s61 offen sc1\n"
    "s_waitcnt lgkmcnt(0)\n"
    "buffer_store_dwordx4 v[52:55], v24, s[20:23], s63 offen sc1\n"
    "s_branch .LfEND\n"
    ".LfCKS1:\n"
    "s_mov_b64 exec, s[92:93]\n"
    "buffer_load_dwordx2 v[14:15], v13, s[24:27], 0 offen sc1\n"
    "s_mov_b64 exec, -1\n"
    "s_waitcnt vmcnt(0)\n"
    "v_cmp_ne_u64 vcc, s[44:45], v[14:15]\n"
    "s_nop 0\n"
    "s_and_b64 vcc, vcc, s[92:93]\n"
    "s_cbranch_scc1 .LfS63\n"
    ".LfB64:\n"
    "buffer_load_dwordx4 v[176:179], v242, s[24:27], s84 offen offset:0 sc1\n"
    "buffer_load_dwordx4 v[180:183], v242, s[24:27], s84 offen offset:1024 sc1\n"
    "buffer_load_dwordx4 v[208:211], v242, s[24:27], s84 offen offset:2048 sc1\n"
    "buffer_load_dwordx4 v[212:215], v242, s[24:27], s84 offen offset:3072 sc1\n"
    "buffer_load_dwordx4 v[184:187], v12, s[24:27], s84 offen offset:0 sc1\n"
    "buffer_load_dwordx4 v[188:191], v12, s[24:27], s84 offen offset:1024 sc1\n"
    "buffer_load_dwordx4 v[216:219], v12, s[24:27], s84 offen offset:2048 sc1\n"
    "buffer_load_dwordx4 v[220:223], v12, s[24:27], s84 offen offset:3072 sc1\n"
    "s_waitcnt vmcnt(0)\n"
    "s_branch .LfATT\n"
    ".LfCK2S:\n"
    "s_waitcnt vmcnt(0)\n"
    "v_cmp_ne_u64 vcc, s[44:45], v[14:15]\n"
    "s_cbranch_vccnz .LfS65\n"
    ".LfB66:\n"
    "buffer_load_dwordx4 v[192:195], v242, s[24:27], s85 offen offset:0 sc1\n"
    "buffer_load_dwordx4 v[196:199], v242, s[24:27], s85 offen offset:1024 sc1\n"
    "buffer_load_dwordx4 v[224:227], v242, s[24:27], s85 offen offset:2048 sc1\n"
    "buffer_load_dwordx4 v[228:231], v242, s[24:27], s85 offen offset:3072 sc1\n"
    "buffer_load_dwordx4 v[200:203], v12, s[24:27], s85 offen offset:0 sc1\n"
    "buffer_load_dwordx4 v[204:207], v12, s[24:27], s85 offen offset:1024 sc1\n"
    "buffer_load_dwordx4 v[232:235], v12, s[24:27], s85 offen offset:2048 sc1\n"
    "buffer_load_dwordx4 v[236:239], v12, s[24:27], s85 offen offset:3072 sc1\n"
    "s_waitcnt vmcnt(0)\n"
    "s_branch .LfCK2\n"
    ".LfS58:\n"
    "s_mov_b64 exec, vcc\n"
    "s_sleep 4\n"
    "buffer_load_dwordx2 v[14:15], v13, s[24:27], 0 offen sc1\n"
    "s_add_u32 s49, s49, 1\n"
    "s_cmp_lt_u32 s49, 0x100000\n"
    "s_cselect_b64 s[52:53], -1, 0\n"
    "s_waitcnt vmcnt(0)\n"
    "v_cmp_ne_u64 vcc, s[44:45], v[14:15]\n"
    "s_nop 0\n"
    "s_and_b64 vcc, vcc, s[52:53]\n"
    "s_cbranch_scc1 .LfS58\n"
    "s_mov_b64 exec, -1\n"
    "s_branch .LfB59\n"
    ".LfS60:\n"
    "s_mov_b64 exec, vcc\n"
    "s_sleep 4\n"
    "buffer_load_dwordx2 v[14:15], v16, s[24:27], 0 offen sc1\n"
    "s_add_u32 s49, s49, 1\n"
    "s_cmp_lt_u32 s49, 0x100000\n"
    "s_cselect_b64 s[52:53], -1, 0\n"
    "s_waitcnt vmcnt(0)\n"
    "v_cmp_ne_u64 vcc, s[44:45], v[14:15]\n"
    "s_nop 0\n"
    "s_and_b64 vcc, vcc, s[52:53]\n"
    "s_cbranch_scc1 .LfS60\n"
    "s_mov_b64 exec, -1\n"
    "s_branch .LfB61\n"
    ".LfS63:\n"
    "s_mov_b64 exec, vcc\n"
    "s_sleep 4\n"
    "buffer_load_dwordx2 v[14:15], v13, s[24:27], 0 offen sc1\n"
    "s_add_u32 s49, s49, 1\n"
    "s_cmp_lt_u32 s49, 0x100000\n"
    "s_cselect_b64 s[52:53], -1, 0\n"
    "s_waitcnt vmcnt(0)\n"
    "v_cmp_ne_u64 vcc, s[44:45], v[14:15]\n"
    "s_nop 0\n"
    "s_and_b64 vcc, vcc, s[52:53]\n"
    "s_cbranch_scc1 .LfS63\n"
    "s_mov_b64 exec, -1\n"
    "s_branch .LfB64\n"
    ".LfS65:\n"
    "s_mov_b64 exec, vcc\n"
    "s_sleep 4\n"
    "buffer_load_dwordx2 v[14:15], v13, s[24:27], 0 offen sc1\n"
    "s_add_u32 s49, s49, 1\n"
    "s_cmp_lt_u32 s49, 0x100000\n"
    "s_cselect_b64 s[52:53], -1, 0\n"
    "s_waitcnt vmcnt(0)\n"
    "v_cmp_ne_u64 vcc, s[44:45], v[14:15]\n"
    "s_nop 0\n"
    "s_and_b64 vcc, vcc, s[52:53]\n"
    "s_cbranch_scc1 .LfS65\n"
    "s_mov_b64 exec, -1\n"
    "s_branch .LfB66\n"
    ".LfV:\n"
    "s_lshr_b32 s55, s47, 1\n"
    "s_and_b32 s56, s47, 1\n"
    "s_setprio 2\n"
    "v_mov_b32 v244, 1\n"
    "v_lshl_add_u32 v245, v241, 20, v243\n"
    "s_mov_b32 s49, 0\n"
    "s_bfm_b64 s[80:81], 32, 0\n"
    "s_bfm_b64 s[92:93], 8, 0\n"
    "s_mov_b32 s22, 0x380000\n"
    "s_mov_b32 s23, 0x20000\n"
    "s_mul_i32 s71, s47, 8704\n"
    "v_and_b32 v18, 31, v241\n"
    "v_lshrrev_b32 v19, 5, v241\n"
    "v_mul_u32_u24 v20, 272, v18\n"
    "v_lshl_add_u32 v20, v19, 4, v20\n"
    "v_add_u32 v240, s71, v20\n"
    "v_mov_b32 v249, 0x3f80\n"
    "v_lshl_add_u32 v17, s56, 6, v241\n"
    "s_mul_i32 s58, s55, 1376\n"
    "s_add_u32 s60, s58, 36800\n"
    "s_mul_i32 s59, s56, 2816\n"
    "v_lshlrev_b32 v20, 1, v18\n"
    "v_add_u32 v21, v20, v19\n"
    "v_lshl_add_u32 v21, v21, 2, s60\n"
    "v_lshrrev_b32 v22, 5, v21\n"
    "v_add_u32 v21, v21, v22\n"
    "v_lshl_add_u32 v52, v21, 2, s59\n"
    "v_lshlrev_b32 v21, 2, v18\n"
    "v_add_u32 v20, 0x80, v20\n"
    "v_cndmask_b32 v20, v20, v21, s[80:81]\n"
    "s_add_u32 s61, s60, 256\n"
    "v_add_u32 v20, s61, v20\n"
    "v_lshrrev_b32 v22, 5, v20\n"
    "v_add_u32 v20, v20, v22\n"
    "v_lshl_add_u32 v53, v20, 2, s59\n"
    "v_lshlrev_b32 v21, 8, v19\n"
    "v_add3_u32 v54, v53, v21, 8\n"
    "s_mul_i32 s58, s55, 5676\n"
    "s_add_u32 s58, s58, s59\n"
    "s_add_u32 s61, s58, 153912\n"
    "v_lshl_add_u32 v20, v18, 2, s61\n"
    "v_lshl_add_u32 v253, v19, 20, v20\n"
    "v_mul_u32_u24 v21, v18, v19\n"
    "v_lshl_add_u32 v20, v21, 20, v20\n"
    "v_mul_u32_u24 v21, 528, v19\n"
    "v_add_u32 v55, v20, v21\n"
    "s_add_u32 s61, s58, 151668\n"
    "v_lshl_add_u32 v20, v18, 2, s61\n"
    "v_mov_b32 v21, 0\n"
    "ds_write_b32 v20, v21\n"
    "s_sub_u32 s61, s60, 17024\n"
    "v_lshlrev_b32 v18, 3, v17\n"
    "s_waitcnt lgkmcnt(0)\n"
    "s_add_u32 s44, s44, 1\n"
    "s_addc_u32 s45, s45, 0\n"
    "ds_add_u32 v245, v244 offset:28\n"
    "s_lshl_b32 s70, s55, 4\n"
    "s_lshr_b32 s68, s68, s70\n"
    "s_lshr_b32 s69, s69, s70\n"
    "s_lshl_b32 s68, s68, 16\n"
    "v_mov_b32 v56, 0x3fb8aa3b\n"
    "s_lshl_b32 s69, s69, 16\n"
    "v_mul_f32 v56, s68, v56\n"
    "s_cmp_eq_u32 s56, 0\n"
    "v_exp_f32 v56, v56\n"
    "s_cselect_b32 s69, s69, 0\n"
    "v_mov_b32 v252, 0\n"
    "v_cvt_pk_bf16_f32 v56, v56, v56\n"
    "v_writelane_b32 v252, s69, 0\n"
    "v_lshlrev_b32 v72, 16, v56\n"
    "v_add_f32 v74, v72, v72\n"
    "v_mul_f32 v75, -2.0, v72\n"
    "s_waitcnt vmcnt(2)\n"
    "v_lshlrev_b32 v56, 16, v32\n"
    "v_lshlrev_b32 v57, 16, v33\n"
    "v_mul_f32 v56, 0x3fb8aa3b, v56\n"
    "v_mul_f32 v57, v57, v72\n"
    "v_exp_f32 v56, v56\n"
    "v_mul_f32 v57, 0.15915494, v57\n"
    "v_cvt_pk_bf16_f32 v56, v56, v56\n"
    "v_mul_f32 v59, 0x43800000, v57\n"
    "v_lshlrev_b32 v56, 16, v56\n"
    "v_fract_f32 v58, v57\n"
    "v_mul_f32 v56, v56, v72\n"
    "v_fract_f32 v59, v59\n"
    "v_mul_f32 v56, 0xbfb8aa3b, v56\n"
    "v_cos_f32 v76, v58\n"
    "v_mul_f32 v57, 0x41000000, v57\n"
    "v_sin_f32 v77, v58\n"
    "v_mul_f32 v62, 0x43800000, v56\n"
    "v_exp_f32 v60, v56\n"
    "v_mul_f32 v56, 0x41000000, v56\n"
    "v_cos_f32 v78, v59\n"
    "v_fract_f32 v57, v57\n"
    "v_exp_f32 v62, v62\n"
    "s_waitcnt vmcnt(0)\n"
    "v_lshlrev_b32 v84, 16, v34\n"
    "v_sin_f32 v79, v59\n"
    "v_lshlrev_b32 v85, 16, v35\n"
    "v_exp_f32 v56, v56\n"
    "v_pk_mul_f32 v[76:77], v[76:77], v[60:61] op_sel_hi:[1,0]\n"
    "v_cos_f32 v80, v57\n"
    "v_pk_mul_f32 v[78:79], v[78:79], v[62:63] op_sel_hi:[1,0]\n"
    "v_sin_f32 v81, v57\n"
    "v_pk_mul_f32 v[84:85], v[84:85], v[74:75]\n"
    "v_pk_mul_f32 v[82:83], v[76:77], v[76:77] op_sel_hi:[1,0]\n"
    "v_pk_mul_f32 v[80:81], v[80:81], v[56:57] op_sel_hi:[1,0]\n"
    "v_pk_fma_f32 v[82:83], v[76:77], v[76:77], v[82:83] op_sel:[1,1,0] op_sel_hi:[0,1,1] neg_lo:[1,0,0]\n"
    "v_pk_mul_f32 v[86:87], v[84:85], v[76:77] op_sel_hi:[1,0]\n"
    "v_pk_mul_f32 v[100:101], v[84:85], v[78:79] op_sel_hi:[1,0]\n"
    "v_pk_mul_f32 v[88:89], v[84:85], v[82:83] op_sel_hi:[1,0]\n"
    "v_pk_fma_f32 v[86:87], v[84:85], v[76:77], v[86:87] op_sel:[1,1,0] op_sel_hi:[0,1,1] neg_hi:[1,0,0]\n"
    "v_pk_fma_f32 v[100:101], v[84:85], v[78:79], v[100:101] op_sel:[1,1,0] op_sel_hi:[0,1,1] neg_hi:[1,0,0]\n"
    "v_pk_fma_f32 v[88:89], v[84:85], v[82:83], v[88:89] op_sel:[1,1,0] op_sel_hi:[0,1,1] neg_hi:[1,0,0]\n"
    "v_cvt_pk_bf16_f32 v136, v84, v85\n"
    "v_pk_mul_f32 v[90:91], v[86:87], v[82:83] op_sel_hi:[1,0]\n"
    "v_pk_mul_f32 v[102:103], v[100:101], v[78:79] op_sel_hi:[1,0]\n"
    "v_pk_mul_f32 v[92:93], v[88:89], v[82:83] op_sel_hi:[1,0]\n"
    "v_pk_mul_f32 v[116:117], v[88:89], v[78:79] op_sel_hi:[1,0]\n"
    "v_pk_fma_f32 v[90:91], v[86:87], v[82:83], v[90:91] op_sel:[1,1,0] op_sel_hi:[0,1,1] neg_hi:[1,0,0]\n"
    "v_pk_fma_f32 v[102:103], v[100:101], v[78:79], v[102:103] op_sel:[1,1,0] op_sel_hi:[0,1,1] neg_hi:[1,0,0]\n"
    "v_pk_fma_f32 v[92:93], v[88:89], v[82:83], v[92:93] op_sel:[1,1,0] op_sel_hi:[0,1,1] neg_hi:[1,0,0]\n"
    "v_pk_fma_f32 v[116:117], v[88:89], v[78:79], v[116:117] op_sel:[1,1,0] op_sel_hi:[0,1,1] neg_hi:[1,0,0]\n"
    "v_cvt_pk_bf16_f32 v137, v86, v87\n"
    "v_cvt_pk_bf16_f32 v144, v100, v101\n"
    "v_cvt_pk_bf16_f32 v138, v88, v89\n"
    "v_pk_mul_f32 v[94:95], v[90:91], v[82:83] op_sel_hi:[1,0]\n"
    "v_pk_mul_f32 v[104:105], v[102:103], v[78:79] op_sel_hi:[1,0]\n"
    "v_pk_mul_f32 v[96:97], v[92:93], v[82:83] op_sel_hi:[1,0]\n"
    "v_pk_mul_f32 v[118:119], v[92:93], v[78:79] op_sel_hi:[1,0]\n"
    "v_pk_fma_f32 v[94:95], v[90:91], v[82:83], v[94:95] op_sel:[1,1,0] op_sel_hi:[0,1,1] neg_hi:[1,0,0]\n"
    "v_pk_fma_f32 v[104:105], v[102:103], v[78:79], v[104:105] op_sel:[1,1,0] op_sel_hi:[0,1,1] neg_hi:[1,0,0]\n"
    "v_pk_fma_f32 v[96:97], v[92:93], v[82:83], v[96:97] op_sel:[1,1,0] op_sel_hi:[0,1,1] neg_hi:[1,0,0]\n"
    "v_pk_fma_f32 v[118:119], v[92:93], v[78:79], v[118:119] op_sel:[1,1,0] op_sel_hi:[0,1,1] neg_hi:[1,0,0]\n"
    "v_cvt_pk_bf16_f32 v139, v90, v91\n"
    "v_cvt_pk_bf16_f32 v148, v102, v103\n"
    "v_cvt_pk_bf16_f32 v140, v92, v93\n"
    "v_cvt_pk_bf16_f32 v145, v116, v117\n"
    "v_pk_mul_f32 v[98:99], v[94:95], v[82:83] op_sel_hi:[1,0]\n"
    "v_pk_mul_f32 v[106:107], v[104:105], v[78:79] op_sel_hi:[1,0]\n"
    "v_pk_mul_f32 v[120:121], v[96:97], v[78:79] op_sel_hi:[1,0]\n"
    "v_pk_mul_f32 v[122:123], v[118:119], v[78:79] op_sel_hi:[1,0]\n"
    "v_pk_fma_f32 v[98:99], v[94:95], v[82:83], v[98:99] op_sel:[1,1,0] op_sel_hi:[0,1,1] neg_hi:[1,0,0]\n"
    "v_pk_fma_f32 v[106:107], v[104:105], v[78:79], v[106:107] op_sel:[1,1,0] op_sel_hi:[0,1,1] neg_hi:[1,0,0]\n"
    "v_pk_fma_f32 v[120:121], v[96:97], v[78:79], v[120:121] op_sel:[1,1,0] op_sel_hi:[0,1,1] neg_hi:[1,0,0]\n"
    "v_pk_fma_f32 v[122:123], v[118:119], v[78:79], v[122:123] op_sel:[1,1,0] op_sel_hi:[0,1,1] neg_hi:[1,0,0]\n"
    "v_cvt_pk_bf16_f32 v141, v94, v95\n"
    "v_cvt_pk_bf16_f32 v150, v104, v105\n"
    "v_cvt_pk_bf16_f32 v142, v96, v97\n"
    "v_cvt_pk_bf16_f32 v146, v118, v119\n"
    "v_pk_mul_f32 v[84:85], v[80:81], v[80:81] op_sel_hi:[1,0]\n"
    "v_pk_mul_f32 v[108:109], v[106:107], v[78:79] op_sel_hi:[1,0]\n"
    "v_pk_mul_f32 v[124:125], v[122:123], v[78:79] op_sel_hi:[1,0]\n"
    "v_pk_fma_f32 v[108:109], v[106:107], v[78:79], v[108:109] op_sel:[1,1,0] op_sel_hi:[0,1,1] neg_hi:[1,0,0]\n"
    "v_pk_fma_f32 v[124:125], v[122:123], v[78:79], v[124:125] op_sel:[1,1,0] op_sel_hi:[0,1,1] neg_hi:[1,0,0]\n"
    "v_pk_fma_f32 v[84:85], v[80:81], v[80:81], v[84:85] op_sel:[1,1,0] op_sel_hi:[0,1,1] neg_lo:[1,0,0]\n"
    "v_cvt_pk_bf16_f32 v143, v98, v99\n"
    "v_cvt_pk_bf16_f32 v152, v106, v107\n"
    "v_cvt_pk_bf16_f32 v147, v120, v121\n"
    "v_cvt_pk_bf16_f32 v149, v122, v123\n"
    "v_pk_mul_f32 v[110:111], v[108:109], v[78:79] op_sel_hi:[1,0]\n"
    "v_pk_mul_f32 v[86:87], v[84:85], v[80:81] op_sel_hi:[1,0]\n"
    "v_pk_mul_f32 v[76:77], v[84:85], v[84:85] op_sel_hi:[1,0]\n"
    "v_pk_fma_f32 v[110:111], v[108:109], v[78:79], v[110:111] op_sel:[1,1,0] op_sel_hi:[0,1,1] neg_hi:[1,0,0]\n"
    "v_pk_fma_f32 v[86:87], v[84:85], v[80:81], v[86:87] op_sel:[1,1,0] op_sel_hi:[0,1,1] neg_lo:[1,0,0]\n"
    "v_pk_fma_f32 v[76:77], v[84:85], v[84:85], v[76:77] op_sel:[1,1,0] op_sel_hi:[0,1,1] neg_lo:[1,0,0]\n"
    "v_cvt_pk_bf16_f32 v153, v108, v109\n"
    "v_cvt_pk_bf16_f32 v151, v124, v125\n"
    "v_pk_mul_f32 v[112:113], v[110:111], v[78:79] op_sel_hi:[1,0]\n"
    "v_pk_mul_f32 v[90:91], v[80:81], v[76:77] op_sel_hi:[1,0]\n"
    "v_pk_mul_f32 v[92:93], v[84:85], v[76:77] op_sel_hi:[1,0]\n"
    "v_pk_mul_f32 v[94:95], v[86:87], v[76:77] op_sel_hi:[1,0]\n"
    "v_pk_fma_f32 v[112:113], v[110:111], v[78:79], v[112:113] op_sel:[1,1,0] op_sel_hi:[0,1,1] neg_hi:[1,0,0]\n"
    "v_pk_fma_f32 v[90:91], v[80:81], v[76:77], v[90:91] op_sel:[1,1,0] op_sel_hi:[0,1,1] neg_lo:[1,0,0]\n"
    "v_pk_fma_f32 v[92:93], v[84:85], v[76:77], v[92:93] op_sel:[1,1,0] op_sel_hi:[0,1,1] neg_lo:[1,0,0]\n"
    "v_pk_fma_f32 v[94:95], v[86:87], v[76:77], v[94:95] op_sel:[1,1,0] op_sel_hi:[0,1,1] neg_lo:[1,0,0]\n"
    "v_cvt_pk_bf16_f32 v154, v110, v111\n"
    "v_cvt_pk_bf16_f32 v157, v80, v81\n"
    "v_cvt_pk_bf16_f32 v158, v84, v85\n"
    "v_cvt_pk_bf16_f32 v159, v86, v87\n"
    "v_pk_mul_f32 v[114:115], v[112:113], v[78:79] op_sel_hi:[1,0]\n"
    "v_pk_mul_f32 v[116:117], v[76:77], v[76:77] op_sel_hi:[1,0]\n"
    "v_pk_mul_f32 v[118:119], v[90:91], v[76:77] op_sel_hi:[1,0]\n"
    "v_pk_mul_f32 v[84:85], v[92:93], v[76:77] op_sel_hi:[1,0]\n"
    "v_pk_mul_f32 v[86:87], v[94:95], v[76:77] op_sel_hi:[1,0]\n"
    "v_pk_fma_f32 v[114:115], v[112:113], v[78:79], v[114:115] op_sel:[1,1,0] op_sel_hi:[0,1,1] neg_hi:[1,0,0]\n"
    "v_pk_fma_f32 v[116:117], v[76:77], v[76:77], v[116:117] op_sel:[1,1,0] op_sel_hi:[0,1,1] neg_lo:[1,0,0]\n"
    "v_pk_fma_f32 v[118:119], v[90:91], v[76:77], v[118:119] op_sel:[1,1,0] op_sel_hi:[0,1,1] neg_lo:[1,0,0]\n"
    "v_pk_fma_f32 v[84:85], v[92:93], v[76:77], v[84:85] op_sel:[1,1,0] op_sel_hi:[0,1,1] neg_lo:[1,0,0]\n"
    "v_pk_fma_f32 v[86:87], v[94:95], v[76:77], v[86:87] op_sel:[1,1,0] op_sel_hi:[0,1,1] neg_lo:[1,0,0]\n"
    "v_cvt_pk_bf16_f32 v155, v112, v113\n"
    "v_cvt_pk_bf16_f32 v160, v76, v77\n"
    "v_cvt_pk_bf16_f32 v161, v90, v91\n"
    "v_cvt_pk_bf16_f32 v162, v92, v93\n"
    "v_cvt_pk_bf16_f32 v163, v94, v95\n"
    "v_pk_mul_f32 v[88:89], v[116:117], v[76:77] op_sel_hi:[1,0]\n"
    "v_pk_mul_f32 v[90:91], v[118:119], v[76:77] op_sel_hi:[1,0]\n"
    "v_pk_mul_f32 v[92:93], v[84:85], v[76:77] op_sel_hi:[1,0]\n"
    "v_pk_mul_f32 v[94:95], v[86:87], v[76:77] op_sel_hi:[1,0]\n"
    "v_pk_fma_f32 v[88:89], v[116:117], v[76:77], v[88:89] op_sel:[1,1,0] op_sel_hi:[0,1,1] neg_lo:[1,0,0]\n"
    "v_pk_fma_f32 v[90:91], v[118:119], v[76:77], v[90:91] op_sel:[1,1,0] op_sel_hi:[0,1,1] neg_lo:[1,0,0]\n"
    "v_pk_fma_f32 v[92:93], v[84:85], v[76:77], v[92:93] op_sel:[1,1,0] op_sel_hi:[0,1,1] neg_lo:[1,0,0]\n"
    "v_pk_fma_f32 v[94:95], v[86:87], v[76:77], v[94:95] op_sel:[1,1,0] op_sel_hi:[0,1,1] neg_lo:[1,0,0]\n"
    "v_cvt_pk_bf16_f32 v156, v114, v115\n"
    "v_cvt_pk_bf16_f32 v164, v116, v117\n"
    "v_cvt_pk_bf16_f32 v165, v118, v119\n"
    "v_cvt_pk_bf16_f32 v166, v84, v85\n"
    "v_cvt_pk_bf16_f32 v167, v86, v87\n"
    "v_pk_mul_f32 v[116:117], v[88:89], v[76:77] op_sel_hi:[1,0]\n"
    "v_pk_mul_f32 v[118:119], v[90:91], v[76:77] op_sel_hi:[1,0]\n"
    "v_pk_mul_f32 v[84:85], v[92:93], v[76:77] op_sel_hi:[1,0]\n"
    "v_pk_mul_f32 v[86:87], v[94:95], v[76:77] op_sel_hi:[1,0]\n"
    "v_pk_fma_f32 v[116:117], v[88:89], v[76:77], v[116:117] op_sel:[1,1,0] op_sel_hi:[0,1,1] neg_lo:[1,0,0]\n"
    "v_pk_fma_f32 v[118:119], v[90:91], v[76:77], v[118:119] op_sel:[1,1,0] op_sel_hi:[0,1,1] neg_lo:[1,0,0]\n"
    "v_pk_fma_f32 v[84:85], v[92:93], v[76:77], v[84:85] op_sel:[1,1,0] op_sel_hi:[0,1,1] neg_lo:[1,0,0]\n"
    "v_pk_fma_f32 v[86:87], v[94:95], v[76:77], v[86:87] op_sel:[1,1,0] op_sel_hi:[0,1,1] neg_lo:[1,0,0]\n"
    "v_cvt_pk_bf16_f32 v168, v88, v89\n"
    "v_cvt_pk_bf16_f32 v169, v90, v91\n"
    "v_cvt_pk_bf16_f32 v170, v92, v93\n"
    "v_cvt_pk_bf16_f32 v171, v94, v95\n"
    "v_pk_mul_f32 v[88:89], v[116:117], v[76:77] op_sel_hi:[1,0]\n"
    "v_pk_mul_f32 v[90:91], v[118:119], v[76:77] op_sel_hi:[1,0]\n"
    "v_pk_mul_f32 v[92:93], v[84:85], v[76:77] op_sel_hi:[1,0]\n"
    "v_pk_mul_f32 v[94:95], v[86:87], v[76:77] op_sel_hi:[1,0]\n"
    "v_pk_fma_f32 v[88:89], v[116:117], v[76:77], v[88:89] op_sel:[1,1,0] op_sel_hi:[0,1,1] neg_lo:[1,0,0]\n"
    "v_pk_fma_f32 v[90:91], v[118:119], v[76:77], v[90:91] op_sel:[1,1,0] op_sel_hi:[0,1,1] neg_lo:[1,0,0]\n"
    "v_pk_fma_f32 v[92:93], v[84:85], v[76:77], v[92:93] op_sel:[1,1,0] op_sel_hi:[0,1,1] neg_lo:[1,0,0]\n"
    "v_pk_fma_f32 v[94:95], v[86:87], v[76:77], v[94:95] op_sel:[1,1,0] op_sel_hi:[0,1,1] neg_lo:[1,0,0]\n"
    "v_cvt_pk_bf16_f32 v172, v116, v117\n"
    "v_cvt_pk_bf16_f32 v173, v118, v119\n"
    "v_cvt_pk_bf16_f32 v174, v84, v85\n"
    "v_cvt_pk_bf16_f32 v175, v86, v87\n"
    "v_pk_mul_f32 v[116:117], v[88:89], v[76:77] op_sel_hi:[1,0]\n"
    "v_pk_mul_f32 v[118:119], v[90:91], v[76:77] op_sel_hi:[1,0]\n"
    "v_pk_mul_f32 v[84:85], v[92:93], v[76:77] op_sel_hi:[1,0]\n"
    "v_pk_mul_f32 v[86:87], v[94:95], v[76:77] op_sel_hi:[1,0]\n"
    "v_pk_fma_f32 v[116:117], v[88:89], v[76:77], v[116:117] op_sel:[1,1,0] op_sel_hi:[0,1,1] neg_lo:[1,0,0]\n"
    "v_pk_fma_f32 v[118:119], v[90:91], v[76:77], v[118:119] op_sel:[1,1,0] op_sel_hi:[0,1,1] neg_lo:[1,0,0]\n"
    "v_pk_fma_f32 v[84:85], v[92:93], v[76:77], v[84:85] op_sel:[1,1,0] op_sel_hi:[0,1,1] neg_lo:[1,0,0]\n"
    "v_pk_fma_f32 v[86:87], v[94:95], v[76:77], v[86:87] op_sel:[1,1,0] op_sel_hi:[0,1,1] neg_lo:[1,0,0]\n"
    "v_cvt_pk_bf16_f32 v176, v88, v89\n"
    "v_cvt_pk_bf16_f32 v177, v90, v91\n"
    "v_cvt_pk_bf16_f32 v178, v92, v93\n"
    "v_cvt_pk_bf16_f32 v179, v94, v95\n"
    "v_pk_mul_f32 v[88:89], v[116:117], v[76:77] op_sel_hi:[1,0]\n"
    "v_pk_mul_f32 v[90:91], v[118:119], v[76:77] op_sel_hi:[1,0]\n"
    "v_pk_mul_f32 v[92:93], v[84:85], v[76:77] op_sel_hi:[1,0]\n"
    "v_pk_mul_f32 v[94:95], v[86:87], v[76:77] op_sel_hi:[1,0]\n"
    "v_pk_fma_f32 v[88:89], v[116:117], v[76:77], v[88:89] op_sel:[1,1,0] op_sel_hi:[0,1,1] neg_lo:[1,0,0]\n"
    "v_pk_fma_f32 v[90:91], v[118:119], v[76:77], v[90:91] op_sel:[1,1,0] op_sel_hi:[0,1,1] neg_lo:[1,0,0]\n"
    "v_pk_fma_f32 v[92:93], v[84:85], v[76:77], v[92:93] op_sel:[1,1,0] op_sel_hi:[0,1,1] neg_lo:[1,0,0]\n"
    "v_pk_fma_f32 v[94:95], v[86:87], v[76:77], v[94:95] op_sel:[1,1,0] op_sel_hi:[0,1,1] neg_lo:[1,0,0]\n"
    "v_cvt_pk_bf16_f32 v180, v116, v117\n"
    "v_cvt_pk_bf16_f32 v181, v118, v119\n"
    "v_cvt_pk_bf16_f32 v182, v84, v85\n"
    "v_cvt_pk_bf16_f32 v183, v86, v87\n"
    "v_cvt_pk_bf16_f32 v184, v88, v89\n"
    "v_cvt_pk_bf16_f32 v185, v90, v91\n"
    "v_cvt_pk_bf16_f32 v186, v92, v93\n"
    "v_cvt_pk_bf16_f32 v187, v94, v95\n"
    "v_and_b32 v56, 0xffff0000, v32\n"
    "v_and_b32 v57, 0xffff0000, v33\n"
    "v_mul_f32 v56, 0x3fb8aa3b, v56\n"
    "v_mul_f32 v57, v57, v72\n"
    "v_exp_f32 v56, v56\n"
    "v_mul_f32 v57, 0.15915494, v57\n"
    "v_cvt_pk_bf16_f32 v56, v56, v56\n"
    "v_mul_f32 v59, 0x43800000, v57\n"
    "v_lshlrev_b32 v56, 16, v56\n"
    "v_fract_f32 v58, v57\n"
    "v_mul_f32 v56, v56, v72\n"
    "v_fract_f32 v59, v59\n"
    "v_mul_f32 v56, 0xbfb8aa3b, v56\n"
    "v_cos_f32 v76, v58\n"
    "v_mul_f32 v57, 0x41000000, v57\n"
    "v_sin_f32 v77, v58\n"
    "v_mul_f32 v62, 0x43800000, v56\n"
    "v_exp_f32 v60, v56\n"
    "v_mul_f32 v56, 0x41000000, v56\n"
    "v_cos_f32 v78, v59\n"
    "v_fract_f32 v57, v57\n"
    "v_exp_f32 v62, v62\n"
    "v_and_b32 v84, 0xffff0000, v34\n"
    "v_sin_f32 v79, v59\n"
    "v_and_b32 v85, 0xffff0000, v35\n"
    "v_exp_f32 v56, v56\n"
    "v_mul_f32 v76, v76, v60\n"
    "v_mul_f32 v77, v77, v60\n"
    "v_cos_f32 v80, v57\n"
    "v_mul_f32 v78, v78, v62\n"
    "v_mul_f32 v79, v79, v62\n"
    "v_sin_f32 v81, v57\n"
    "v_mul_f32 v84, v84, v74\n"
    "v_mul_f32 v85, v85, v75\n"
    "v_mul_f32 v82, v76, v76\n"
    "v_mul_f32 v83, v77, v76\n"
    "v_mul_f32 v80, v80, v56\n"
    "v_mul_f32 v81, v81, v56\n"
    "v_fma_f32 v82, -v77, v77, v82\n"
    "v_fmac_f32 v83, v76, v77\n"
    "v_mul_f32 v86, v84, v76\n"
    "v_mul_f32 v87, v85, v76\n"
    "v_mul_f32 v100, v84, v78\n"
    "v_mul_f32 v101, v85, v78\n"
    "v_mul_f32 v88, v84, v82\n"
    "v_mul_f32 v89, v85, v82\n"
    "v_fmac_f32 v86, v85, v77\n"
    "v_fma_f32 v87, -v84, v77, v87\n"
    "v_fmac_f32 v100, v85, v79\n"
    "v_fma_f32 v101, -v84, v79, v101\n"
    "v_fmac_f32 v88, v85, v83\n"
    "v_fma_f32 v89, -v84, v83, v89\n"
    "v_cvt_pk_bf16_f32 v188, v84, v85\n"
    "v_mul_f32 v90, v86, v82\n"
    "v_mul_f32 v91, v87, v82\n"
    "v_mul_f32 v102, v100, v78\n"
    "v_mul_f32 v103, v101, v78\n"
    "v_mul_f32 v92, v88, v82\n"
    "v_mul_f32 v93, v89, v82\n"
    "v_mul_f32 v116, v88, v78\n"
    "v_mul_f32 v117, v89, v78\n"
    "v_fmac_f32 v90, v87, v83\n"
    "v_fma_f32 v91, -v86, v83, v91\n"
    "v_fmac_f32 v102, v101, v79\n"
    "v_fma_f32 v103, -v100, v79, v103\n"
    "v_fmac_f32 v92, v89, v83\n"
    "v_fma_f32 v93, -v88, v83, v93\n"
    "v_fmac_f32 v116, v89, v79\n"
    "v_fma_f32 v117, -v88, v79, v117\n"
    "v_cvt_pk_bf16_f32 v189, v86, v87\n"
    "v_cvt_pk_bf16_f32 v196, v100, v101\n"
    "v_cvt_pk_bf16_f32 v190, v88, v89\n"
    "v_mul_f32 v94, v90, v82\n"
    "v_mul_f32 v95, v91, v82\n"
    "v_mul_f32 v104, v102, v78\n"
    "v_mul_f32 v105, v103, v78\n"
    "v_mul_f32 v96, v92, v82\n"
    "v_mul_f32 v97, v93, v82\n"
    "v_mul_f32 v118, v92, v78\n"
    "v_mul_f32 v119, v93, v78\n"
    "v_fmac_f32 v94, v91, v83\n"
    "v_fma_f32 v95, -v90, v83, v95\n"
    "v_fmac_f32 v104, v103, v79\n"
    "v_fma_f32 v105, -v102, v79, v105\n"
    "v_fmac_f32 v96, v93, v83\n"
    "v_fma_f32 v97, -v92, v83, v97\n"
    "v_fmac_f32 v118, v93, v79\n"
    "v_fma_f32 v119, -v92, v79, v119\n"
    "v_cvt_pk_bf16_f32 v191, v90, v91\n"
    "v_cvt_pk_bf16_f32 v200, v102, v103\n"
    "v_cvt_pk_bf16_f32 v192, v92, v93\n"
    "v_cvt_pk_bf16_f32 v197, v116, v117\n"
    "v_mul_f32 v98, v94, v82\n"
    "v_mul_f32 v99, v95, v82\n"
    "v_mul_f32 v106, v104, v78\n"
    "v_mul_f32 v107, v105, v78\n"
    "v_mul_f32 v120, v96, v78\n"
    "v_mul_f32 v121, v97, v78\n"
    "v_mul_f32 v122, v118, v78\n"
    "v_mul_f32 v123, v119, v78\n"
    "v_fmac_f32 v98, v95, v83\n"
    "v_fma_f32 v99, -v94, v83, v99\n"
    "v_fmac_f32 v106, v105, v79\n"
    "v_fma_f32 v107, -v104, v79, v107\n"
    "v_fmac_f32 v120, v97, v79\n"
    "v_fma_f32 v121, -v96, v79, v121\n"
    "v_fmac_f32 v122, v119, v79\n"
    "v_fma_f32 v123, -v118, v79, v123\n"
    "v_cvt_pk_bf16_f32 v193, v94, v95\n"
    "v_cvt_pk_bf16_f32 v202, v104, v105\n"
    "v_cvt_pk_bf16_f32 v194, v96, v97\n"
    "v_cvt_pk_bf16_f32 v198, v118, v119\n"
    "v_mul_f32 v84, v80, v80\n"
    "v_mul_f32 v85, v81, v80\n"
    "v_mul_f32 v108, v106, v78\n"
    "v_mul_f32 v109, v107, v78\n"
    "v_mul_f32 v124, v122, v78\n"
    "v_mul_f32 v125, v123, v78\n"
    "v_fmac_f32 v108, v107, v79\n"
    "v_fma_f32 v109, -v106, v79, v109\n"
    "v_fmac_f32 v124, v123, v79\n"
    "v_fma_f32 v125, -v122, v79, v125\n"
    "v_fma_f32 v84, -v81, v81, v84\n"
    "v_fmac_f32 v85, v80, v81\n"
    "v_cvt_pk_bf16_f32 v195, v98, v99\n"
    "v_cvt_pk_bf16_f32 v204, v106, v107\n"
    "v_cvt_pk_bf16_f32 v199, v120, v121\n"
    "v_cvt_pk_bf16_f32 v201, v122, v123\n"
    "v_mul_f32 v110, v108, v78\n"
    "v_mul_f32 v111, v109, v78\n"
    "v_mul_f32 v86, v84, v80\n"
    "v_mul_f32 v87, v85, v80\n"
    "v_mul_f32 v76, v84, v84\n"
    "v_mul_f32 v77, v85, v84\n"
    "v_fmac_f32 v110, v109, v79\n"
    "v_fma_f32 v111, -v108, v79, v111\n"
    "v_fma_f32 v86, -v85, v81, v86\n"
    "v_fmac_f32 v87, v84, v81\n"
    "v_fma_f32 v76, -v85, v85, v76\n"
    "v_fmac_f32 v77, v84, v85\n"
    "v_cvt_pk_bf16_f32 v205, v108, v109\n"
    "v_cvt_pk_bf16_f32 v203, v124, v125\n"
    "v_mul_f32 v112, v110, v78\n"
    "v_mul_f32 v113, v111, v78\n"
    "v_mul_f32 v90, v80, v76\n"
    "v_mul_f32 v91, v81, v76\n"
    "v_mul_f32 v92, v84, v76\n"
    "v_mul_f32 v93, v85, v76\n"
    "v_mul_f32 v94, v86, v76\n"
    "v_mul_f32 v95, v87, v76\n"
    "v_fmac_f32 v112, v111, v79\n"
    "v_fma_f32 v113, -v110, v79, v113\n"
    "v_fma_f32 v90, -v81, v77, v90\n"
    "v_fmac_f32 v91, v80, v77\n"
    "v_fma_f32 v92, -v85, v77, v92\n"
    "v_fmac_f32 v93, v84, v77\n"
    "v_fma_f32 v94, -v87, v77, v94\n"
    "v_fmac_f32 v95, v86, v77\n"
    "v_cvt_pk_bf16_f32 v206, v110, v111\n"
    "v_cvt_pk_bf16_f32 v209, v80, v81\n"
    "v_cvt_pk_bf16_f32 v210, v84, v85\n"
    "v_cvt_pk_bf16_f32 v211, v86, v87\n"
    "v_mul_f32 v114, v112, v78\n"
    "v_mul_f32 v115, v113, v78\n"
    "v_mul_f32 v116, v76, v76\n"
    "v_mul_f32 v117, v77, v76\n"
    "v_mul_f32 v118, v90, v76\n"
    "v_mul_f32 v119, v91, v76\n"
    "v_mul_f32 v84, v92, v76\n"
    "v_mul_f32 v85, v93, v76\n"
    "v_mul_f32 v86, v94, v76\n"
    "v_mul_f32 v87, v95, v76\n"
    "v_fmac_f32 v114, v113, v79\n"
    "v_fma_f32 v115, -v112, v79, v115\n"
    "v_fma_f32 v116, -v77, v77, v116\n"
    "v_fmac_f32 v117, v76, v77\n"
    "v_fma_f32 v118, -v91, v77, v118\n"
    "v_fmac_f32 v119, v90, v77\n"
    "v_fma_f32 v84, -v93, v77, v84\n"
    "v_fmac_f32 v85, v92, v77\n"
    "v_fma_f32 v86, -v95, v77, v86\n"
    "v_fmac_f32 v87, v94, v77\n"
    "v_cvt_pk_bf16_f32 v207, v112, v113\n"
    "v_cvt_pk_bf16_f32 v212, v76, v77\n"
    "v_cvt_pk_bf16_f32 v213, v90, v91\n"
    "v_cvt_pk_bf16_f32 v214, v92, v93\n"
    "v_cvt_pk_bf16_f32 v215, v94, v95\n"
    "v_mul_f32 v88, v116, v76\n"
    "v_mul_f32 v89, v117, v76\n"
    "v_mul_f32 v90, v118, v76\n"
    "v_mul_f32 v91, v119, v76\n"
    "v_mul_f32 v92, v84, v76\n"
    "v_mul_f32 v93, v85, v76\n"
    "v_mul_f32 v94, v86, v76\n"
    "v_mul_f32 v95, v87, v76\n"
    "v_fma_f32 v88, -v117, v77, v88\n"
    "v_fmac_f32 v89, v116, v77\n"
    "v_fma_f32 v90, -v119, v77, v90\n"
    "v_fmac_f32 v91, v118, v77\n"
    "v_fma_f32 v92, -v85, v77, v92\n"
    "v_fmac_f32 v93, v84, v77\n"
    "v_fma_f32 v94, -v87, v77, v94\n"
    "v_fmac_f32 v95, v86, v77\n"
    "v_cvt_pk_bf16_f32 v208, v114, v115\n"
    "v_cvt_pk_bf16_f32 v216, v116, v117\n"
    "v_cvt_pk_bf16_f32 v217, v118, v119\n"
    "v_cvt_pk_bf16_f32 v218, v84, v85\n"
    "v_cvt_pk_bf16_f32 v219, v86, v87\n"
    "v_mul_f32 v116, v88, v76\n"
    "v_mul_f32 v117, v89, v76\n"
    "v_mul_f32 v118, v90, v76\n"
    "v_mul_f32 v119, v91, v76\n"
    "v_mul_f32 v84, v92, v76\n"
    "v_mul_f32 v85, v93, v76\n"
    "v_mul_f32 v86, v94, v76\n"
    "v_mul_f32 v87, v95, v76\n"
    "v_fma_f32 v116, -v89, v77, v116\n"
    "v_fmac_f32 v117, v88, v77\n"
    "v_fma_f32 v118, -v91, v77, v118\n"
    "v_fmac_f32 v119, v90, v77\n"
    "v_fma_f32 v84, -v93, v77, v84\n"
    "v_fmac_f32 v85, v92, v77\n"
    "v_fma_f32 v86, -v95, v77, v86\n"
    "v_fmac_f32 v87, v94, v77\n"
    "v_cvt_pk_bf16_f32 v220, v88, v89\n"
    "v_cvt_pk_bf16_f32 v221, v90, v91\n"
    "v_cvt_pk_bf16_f32 v222, v92, v93\n"
    "v_cvt_pk_bf16_f32 v223, v94, v95\n"
    "v_mul_f32 v88, v116, v76\n"
    "v_mul_f32 v89, v117, v76\n"
    "v_mul_f32 v90, v118, v76\n"
    "v_mul_f32 v91, v119, v76\n"
    "v_mul_f32 v92, v84, v76\n"
    "v_mul_f32 v93, v85, v76\n"
    "v_mul_f32 v94, v86, v76\n"
    "v_mul_f32 v95, v87, v76\n"
    "v_fma_f32 v88, -v117, v77, v88\n"
    "v_fmac_f32 v89, v116, v77\n"
    "v_fma_f32 v90, -v119, v77, v90\n"
    "v_fmac_f32 v91, v118, v77\n"
    "v_fma_f32 v92, -v85, v77, v92\n"
    "v_fmac_f32 v93, v84, v77\n"
    "v_fma_f32 v94, -v87, v77, v94\n"
    "v_fmac_f32 v95, v86, v77\n"
    "v_cvt_pk_bf16_f32 v224, v116, v117\n"
    "v_cvt_pk_bf16_f32 v225, v118, v119\n"
    "v_cvt_pk_bf16_f32 v226, v84, v85\n"
    "v_cvt_pk_bf16_f32 v227, v86, v87\n"
    "v_mul_f32 v116, v88, v76\n"
    "v_mul_f32 v117, v89, v76\n"
    "v_mul_f32 v118, v90, v76\n"
    "v_mul_f32 v119, v91, v76\n"
    "v_mul_f32 v84, v92, v76\n"
    "v_mul_f32 v85, v93, v76\n"
    "v_mul_f32 v86, v94, v76\n"
    "v_mul_f32 v87, v95, v76\n"
    "v_fma_f32 v116, -v89, v77, v116\n"
    "v_fmac_f32 v117, v88, v77\n"
    "v_fma_f32 v118, -v91, v77, v118\n"
    "v_fmac_f32 v119, v90, v77\n"
    "v_fma_f32 v84, -v93, v77, v84\n"
    "v_fmac_f32 v85, v92, v77\n"
    "v_fma_f32 v86, -v95, v77, v86\n"
    "v_fmac_f32 v87, v94, v77\n"
    "v_cvt_pk_bf16_f32 v228, v88, v89\n"
    "v_cvt_pk_bf16_f32 v229, v90, v91\n"
    "v_cvt_pk_bf16_f32 v230, v92, v93\n"
    "v_cvt_pk_bf16_f32 v231, v94, v95\n"
    "v_mul_f32 v88, v116, v76\n"
    "v_mul_f32 v89, v117, v76\n"
    "v_mul_f32 v90, v118, v76\n"
    "v_mul_f32 v91, v119, v76\n"
    "v_mul_f32 v92, v84, v76\n"
    "v_mul_f32 v93, v85, v76\n"
    "v_mul_f32 v94, v86, v76\n"
    "v_mul_f32 v95, v87, v76\n"
    "v_fma_f32 v88, -v117, v77, v88\n"
    "v_fmac_f32 v89, v116, v77\n"
    "v_fma_f32 v90, -v119, v77, v90\n"
    "v_fmac_f32 v91, v118, v77\n"
    "v_fma_f32 v92, -v85, v77, v92\n"
    "v_fmac_f32 v93, v84, v77\n"
    "v_fma_f32 v94, -v87, v77, v94\n"
    "v_fmac_f32 v95, v86, v77\n"
    "v_cvt_pk_bf16_f32 v232, v116, v117\n"
    "v_cvt_pk_bf16_f32 v233, v118, v119\n"
    "v_cvt_pk_bf16_f32 v234, v84, v85\n"
    "v_cvt_pk_bf16_f32 v235, v86, v87\n"
    "v_cvt_pk_bf16_f32 v236, v88, v89\n"
    "v_cvt_pk_bf16_f32 v237, v90, v91\n"
    "v_cvt_pk_bf16_f32 v238, v92, v93\n"
    "v_cvt_pk_bf16_f32 v239, v94, v95\n"
    "s_mov_b32 m0, s71\n"
    "v_max_u32 v19, 0x88, v18\n"
    "v_sub_u32 v20, v18, v19\n"
    "v_cvt_f32_u32 v19, v19\n"
    "v_add_u32 v20, s61, v20\n"
    "s_add_u32 s72, s58, 153904\n"
    "s_sub_u32 s72, s72, s59\n"
    "s_add_u32 s61, s60, 513\n"
    "v_lshl_add_u32 v21, v241, 1, s61\n"
    "v_lshrrev_b32 v22, 5, v21\n"
    "v_add_lshl_u32 v30, v21, v22, 2\n"
    "v_add_u32 v21, 1, v21\n"
    "v_lshrrev_b32 v22, 5, v21\n"
    "v_add_lshl_u32 v31, v21, v22, 2\n"
    "v_add_f32 v23, 0xc0400000, v19\n"
    "v_add_f32 v24, 0xc0000000, v19\n"
    "v_cvt_pk_bf16_f32 v23, v23, v24\n"
    "v_add_u32_sdwa v24, v23, v20 dst_sel:DWORD dst_unused:UNUSED_PAD src0_sel:WORD_0 src1_sel:DWORD\n"
    "v_lshrrev_b32 v25, 5, v24\n"
    "v_add_lshl_u32 v36, v24, v25, 2\n"
    "v_add_u32_sdwa v24, v23, v20 dst_sel:DWORD dst_unused:UNUSED_PAD src0_sel:WORD_1 src1_sel:DWORD\n"
    "v_lshrrev_b32 v25, 5, v24\n"
    "v_add_lshl_u32 v37, v24, v25, 2\n"
    "v_add_f32 v23, 0xbf800000, v19\n"
    "v_add_f32 v24, 0x0, v19\n"
    "v_cvt_pk_bf16_f32 v23, v23, v24\n"
    "v_add_u32_sdwa v24, v23, v20 dst_sel:DWORD dst_unused:UNUSED_PAD src0_sel:WORD_0 src1_sel:DWORD\n"
    "v_lshrrev_b32 v25, 5, v24\n"
    "v_add_lshl_u32 v38, v24, v25, 2\n"
    "v_add_u32_sdwa v24, v23, v20 dst_sel:DWORD dst_unused:UNUSED_PAD src0_sel:WORD_1 src1_sel:DWORD\n"
    "v_lshrrev_b32 v25, 5, v24\n"
    "v_add_lshl_u32 v39, v24, v25, 2\n"
    "v_add_f32 v23, 0x3f800000, v19\n"
    "v_add_f32 v24, 0x40000000, v19\n"
    "v_cvt_pk_bf16_f32 v23, v23, v24\n"
    "v_add_u32_sdwa v24, v23, v20 dst_sel:DWORD dst_unused:UNUSED_PAD src0_sel:WORD_0 src1_sel:DWORD\n"
    "v_lshrrev_b32 v25, 5, v24\n"
    "v_add_lshl_u32 v40, v24, v25, 2\n"
    "v_add_u32_sdwa v24, v23, v20 dst_sel:DWORD dst_unused:UNUSED_PAD src0_sel:WORD_1 src1_sel:DWORD\n"
    "v_lshrrev_b32 v25, 5, v24\n"
    "v_add_lshl_u32 v41, v24, v25, 2\n"
    "v_add_f32 v23, 0x40400000, v19\n"
    "v_add_f32 v24, 0x40800000, v19\n"
    "v_cvt_pk_bf16_f32 v23, v23, v24\n"
    "v_add_u32_sdwa v24, v23, v20 dst_sel:DWORD dst_unused:UNUSED_PAD src0_sel:WORD_0 src1_sel:DWORD\n"
    "v_lshrrev_b32 v25, 5, v24\n"
    "v_add_lshl_u32 v42, v24, v25, 2\n"
    "v_add_u32_sdwa v24, v23, v20 dst_sel:DWORD dst_unused:UNUSED_PAD src0_sel:WORD_1 src1_sel:DWORD\n"
    "v_lshrrev_b32 v25, 5, v24\n"
    "v_add_lshl_u32 v43, v24, v25, 2\n"
    "v_add_f32 v23, 0x40a00000, v19\n"
    "v_add_f32 v24, 0x40c00000, v19\n"
    "v_cvt_pk_bf16_f32 v23, v23, v24\n"
    "v_add_u32_sdwa v24, v23, v20 dst_sel:DWORD dst_unused:UNUSED_PAD src0_sel:WORD_0 src1_sel:DWORD\n"
    "v_lshrrev_b32 v25, 5, v24\n"
    "v_add_lshl_u32 v44, v24, v25, 2\n"
    "v_add_u32_sdwa v24, v23, v20 dst_sel:DWORD dst_unused:UNUSED_PAD src0_sel:WORD_1 src1_sel:DWORD\n"
    "v_lshrrev_b32 v25, 5, v24\n"
    "v_add_lshl_u32 v45, v24, v25, 2\n"
    "v_add_f32 v23, 0x40e00000, v19\n"
    "v_cvt_pk_bf16_f32 v23, v23, v24\n"
    "v_add_u32_sdwa v24, v23, v20 dst_sel:DWORD dst_unused:UNUSED_PAD src0_sel:WORD_0 src1_sel:DWORD\n"
    "v_lshrrev_b32 v25, 5, v24\n"
    "v_add_lshl_u32 v46, v24, v25, 2\n"
    "s_mul_i32 s62, s55, 17408\n"
    "v_add_u32 v20, 2, v17\n"
    "v_bfe_u32 v21, v20, 1, 2\n"
    "v_lshl_add_u32 v20, v20, 2, v21\n"
    "v_lshl_add_u32 v26, v20, 4, s62\n"
    "v_xor_b32 v27, 16, v26\n"
    "v_xor_b32 v28, 32, v26\n"
    "v_xor_b32 v29, 48, v26\n"
    "s_lshl2_add_u32 s63, s57, s47\n"
    "s_lshl_b32 s63, s63, 11\n"
    "s_add_u32 s64, s63, 0x300000\n"
    "v_and_b32 v20, 15, v241\n"
    "v_lshrrev_b32 v21, 4, v241\n"
    "s_lshl_b32 s58, s56, 5\n"
    "s_add_u32 s58, s58, 32\n"
    "v_mad_i32_i24 v22, v21, -8, v20\n"
    "v_add_u32 v22, s58, v22\n"
    "v_lshrrev_b32 v23, 3, v22\n"
    "v_xor_b32 v24, v23, v22\n"
    "v_and_b32 v25, 1, v22\n"
    "v_and_b32 v24, 6, v24\n"
    "v_lshl_add_u32 v23, v23, 3, v25\n"
    "v_add_u32 v23, v23, v24\n"
    "v_lshl_add_u32 v246, v23, 3, s62\n"
    "v_add_u32 v22, -4, v22\n"
    "v_lshrrev_b32 v23, 3, v22\n"
    "v_xor_b32 v24, v23, v22\n"
    "v_and_b32 v25, 1, v22\n"
    "v_and_b32 v24, 6, v24\n"
    "v_lshl_add_u32 v23, v23, 3, v25\n"
    "v_add_u32 v23, v23, v24\n"
    "v_lshl_add_u32 v247, v23, 3, s62\n"
    "v_add_u32 v22, 1040, v241\n"
    "v_lshrrev_b32 v23, 3, v22\n"
    "v_xor_b32 v24, v23, v22\n"
    "v_and_b32 v25, 1, v22\n"
    "v_and_b32 v24, 6, v24\n"
    "v_lshl_add_u32 v23, v23, 3, v25\n"
    "v_add_u32 v23, v23, v24\n"
    "v_lshl_add_u32 v18, v23, 3, s62\n"
    "s_mul_i32 s58, s55, 4608\n"
    "s_lshl_b32 s59, s56, 6\n"
    "s_sub_u32 s58, s58, s59\n"
    "s_add_u32 s58, s58, 0x22c60\n"
    "v_lshl_add_u32 v22, v20, 1, v21\n"
    "v_lshl_add_u32 v248, v22, 4, s58\n"
    "s_lshr_b32 s58, s42, 1\n"
    "s_lshl2_add_u32 s58, s58, s47\n"
    "v_mov_b32 v22, -1\n"
    "s_cmp_gt_u32 s58, 13\n"
    "s_cselect_b32 s59, s54, 0\n"
    "s_lshl_b32 s58, s47, 11\n"
    "s_add_u32 s58, s58, 0xa800\n"
    "v_and_b32 v22, s59, v22\n"
    "v_add_u32 v6, s58, v242\n"
    "v_or_b32 v3, v22, v242\n"
    "v_lshlrev_b32 v23, 4, v20\n"
    "v_lshl_add_u32 v23, v21, 9, v23\n"
    "v_add_u32 v7, 0x1a800, v23\n"
    "v_mul_u32_u24 v23, 7168, v21\n"
    "v_lshl_add_u32 v23, v20, 2, v23\n"
    "v_or_b32 v8, v22, v23\n"
    "s_and_b32 s83, s41, 3\n"
    "s_xor_b32 s58, s83, s47\n"
    "s_lshl2_add_u32 s58, s41, s58\n"
    "s_lshl_b32 s58, s58, 8\n"
    "s_add_u32 s58, s58, 0x20000\n"
    "v_lshl_add_u32 v9, v241, 7, s58\n"
    "s_lshl_b32 s58, s47, 6\n"
    "s_add_u32 s59, s58, s42\n"
    "v_lshl_add_u32 v22, v241, 3, s59\n"
    "s_lshr_b32 s59, s42, 2\n"
    "s_lshl3_add_u32 s58, s59, s58\n"
    "s_sub_u32 s58, s58, 121\n"
    "v_lshl_add_u32 v23, v241, 4, s58\n"
    "s_mov_b32 s58, 0x10000\n"
    "v_cndmask_b32 v22, v23, v22, s[92:93]\n"
    "s_bfm_b64 s[94:95], 12, 0\n"
    "v_lshl_add_u32 v0, v22, 7, s58\n"
    "s_lshl_b32 s58, s47, 13\n"
    "v_bfe_u32 v22, v241, 1, 2\n"
    "s_add_u32 s58, s58, 0x1a800\n"
    "v_xor_b32 v22, v22, v21\n"
    "v_lshl_add_u32 v22, v20, 2, v22\n"
    "v_lshl_add_u32 v4, v22, 4, s58\n"
    "v_bfe_u32 v22, v241, 3, 2\n"
    "s_lshl_b32 s58, s47, 11\n"
    "v_xor_b32 v22, v22, v241\n"
    "s_add_u32 s58, s58, 0x1a800\n"
    "v_lshl_add_u32 v5, v22, 4, s58\n"
    "s_lshl_b32 s58, s47, 10\n"
    "s_add_u32 s58, s58, 0x22e00\n"
    "v_lshl_add_u32 v1, v241, 3, s58\n"
    "s_lshl_b32 s58, s56, 6\n"
    "s_sub_u32 s58, s62, s58\n"
    "s_add_u32 s58, s58, 9332\n"
    "v_mad_i32_i24 v22, v21, -4, v20\n"
    "v_lshl_add_u32 v2, v22, 2, s58\n"
    "v_mov_b32 v47, 0x0c0c0c0c\n"
    "v_mov_b32 v15, 0x0c0c0c0c\n"
    "s_mov_b64 exec, 0x3f\n"
    "v_mov_b32 v47, 0x01000c0c\n"
    "s_mov_b64 exec, 0xff\n"
    "v_mov_b32 v15, 0x01000c0c\n"
    "s_mov_b64 exec, 0x1f\n"
    "v_mov_b32 v47, 0x01000100\n"
    "s_mov_b64 exec, 0x7f\n"
    "v_mov_b32 v15, 0x01000100\n"
    "s_mov_b64 exec, 1\n"
    "v_mov_b32 v15, 0x03020100\n"
    "s_mov_b64 exec, -1\n"
    "s_mov_b32 s58, 0x3f800000\n"
    "v_lshlrev_b32 v25, 3, v21\n"
    "v_mov_b32 v22, 0x1ff0\n"
    "v_lshlrev_b32 v22, v20, v22\n"
    "v_lshrrev_b32 v22, v25, v22\n"
    "v_bfe_i32 v23, v22, 0, 1\n"
    "v_bfe_i32 v24, v22, 1, 1\n"
    "v_and_b32 v23, 0x3f80, v23\n"
    "v_and_or_b32 v32, v24, s58, v23\n"
    "v_bfe_i32 v23, v22, 2, 1\n"
    "v_bfe_i32 v24, v22, 3, 1\n"
    "v_and_b32 v23, 0x3f80, v23\n"
    "v_and_or_b32 v33, v24, s58, v23\n"
    "v_bfe_i32 v23, v22, 4, 1\n"
    "v_bfe_i32 v24, v22, 5, 1\n"
    "v_and_b32 v23, 0x3f80, v23\n"
    "v_and_or_b32 v34, v24, s58, v23\n"
    "v_bfe_i32 v23, v22, 6, 1\n"
    "v_bfe_i32 v24, v22, 7, 1\n"
    "v_and_b32 v23, 0x3f80, v23\n"
    "v_and_or_b32 v35, v24, s58, v23\n"
    "v_mov_b32 v22, 0xfe0\n"
    "v_lshlrev_b32 v22, v20, v22\n"
    "v_lshrrev_b32 v22, v25, v22\n"
    "v_bfe_i32 v23, v22, 0, 1\n"
    "v_bfe_i32 v24, v22, 1, 1\n"
    "v_and_b32 v23, 0x3f80, v23\n"
    "v_and_or_b32 v48, v24, s58, v23\n"
    "v_bfe_i32 v23, v22, 2, 1\n"
    "v_bfe_i32 v24, v22, 3, 1\n"
    "v_and_b32 v23, 0x3f80, v23\n"
    "v_and_or_b32 v49, v24, s58, v23\n"
    "v_bfe_i32 v23, v22, 4, 1\n"
    "v_bfe_i32 v24, v22, 5, 1\n"
    "v_and_b32 v23, 0x3f80, v23\n"
    "v_and_or_b32 v50, v24, s58, v23\n"
    "v_bfe_i32 v23, v22, 6, 1\n"
    "v_bfe_i32 v24, v22, 7, 1\n"
    "v_and_b32 v23, 0x3f80, v23\n"
    "v_and_or_b32 v51, v24, s58, v23\n"
    "v_lshlrev_b32 v22, 4, v20\n"
    "v_lshl_add_u32 v10, v21, 13, v22\n"
    "v_and_b32 v22, 0x70, v22\n"
    "v_lshl_add_u32 v22, v20, 9, v22\n"
    "v_lshl_add_u32 v22, v21, 2, v22\n"
    "s_lshl_b32 s58, s47, 5\n"
    "v_add_u32 v22, 0x22c00, v22\n"
    "v_xor_b32 v11, s58, v22\n"
    "v_xor_b32 v12, 16, v11\n"
    "s_lshl2_add_u32 s58, s42, s47\n"
    "s_lshl_b32 s58, s58, 16\n"
    "s_lshl_b32 s59, s41, 8\n"
    "s_add_u32 s58, s58, s59\n"
    "s_add_u32 s66, s58, 0x300000\n"
    "s_add_u32 s67, s66, 0x8000\n"
    "s_lshl_b32 s58, s55, 2\n"
    "v_add_u32 v16, s58, v245\n"
    "v_add_u32 v17, s58, v243\n"
    "v_lshl_add_u32 v19, v241, 3, s62\n"
    "v_lshl_add_u32 v19, v21, 20, v19\n"
    "v_mov_b32 v20, 0\n"
    "v_mov_b32 v21, 0\n"
    ".LfL67:\n"
    "ds_read_b32 v250, v243 offset:0\n"
    "s_add_u32 s49, s49, 1\n"
    "s_cmp_lt_u32 s49, 0x100000\n"
    "s_cselect_b32 s50, 1, 0\n"
    "s_waitcnt lgkmcnt(0)\n"
    "v_cmp_gt_u32 vcc, s50, v250\n"
    "s_cbranch_vccnz .LfL67\n"
    ".LfL68:\n"
    "ds_read_b32 v250, v243 offset:172\n"
    "s_add_u32 s49, s49, 1\n"
    "s_cmp_lt_u32 s49, 0x100000\n"
    "s_cselect_b32 s50, 0, -1\n"
    "s_waitcnt lgkmcnt(0)\n"
    "v_cmp_lt_u32 vcc, s50, v250\n"
    "s_cbranch_vccnz .LfL68\n"
    ".Lfzh0:\n"
    "s_setprio 0\n"
    "ds_write_addtid_b32 v136 offset:0\n"
    "ds_write_addtid_b32 v137 offset:272\n"
    "ds_write_addtid_b32 v138 offset:544\n"
    "ds_write_addtid_b32 v139 offset:816\n"
    "ds_write_addtid_b32 v140 offset:1088\n"
    "ds_write_addtid_b32 v141 offset:1360\n"
    "ds_write_addtid_b32 v142 offset:1632\n"
    "ds_write_addtid_b32 v143 offset:1904\n"
    "ds_write_addtid_b32 v144 offset:2176\n"
    "ds_write_addtid_b32 v145 offset:2448\n"
    "ds_write_addtid_b32 v146 offset:2720\n"
    "ds_write_addtid_b32 v147 offset:2992\n"
    "ds_write_addtid_b32 v148 offset:3264\n"
    "ds_write_addtid_b32 v149 offset:3536\n"
    "ds_write_addtid_b32 v150 offset:3808\n"
    "ds_write_addtid_b32 v151 offset:4080\n"
    "ds_write_addtid_b32 v152 offset:4352\n"
    "ds_write_addtid_b32 v153 offset:4624\n"
    "ds_write_addtid_b32 v154 offset:4896\n"
    "ds_write_addtid_b32 v155 offset:5168\n"
    "ds_write_addtid_b32 v156 offset:5440\n"
    "ds_read_b128 v[72:75], v240 offset:0\n"
    "ds_read_b128 v[76:79], v240 offset:32\n"
    "ds_read_b128 v[80:83], v240 offset:64\n"
    "ds_read_b128 v[84:87], v240 offset:96\n"
    "ds_read_b128 v[88:91], v240 offset:128\n"
    "ds_read_b128 v[92:95], v240 offset:160\n"
    "ds_read_b128 v[96:99], v240 offset:192\n"
    "ds_read_b128 v[100:103], v240 offset:224\n"
    "ds_write_addtid_b32 v249 offset:0\n"
    "ds_write_addtid_b32 v157 offset:272\n"
    "ds_write_addtid_b32 v158 offset:544\n"
    "ds_write_addtid_b32 v159 offset:816\n"
    "ds_write_addtid_b32 v160 offset:1088\n"
    "ds_write_addtid_b32 v161 offset:1360\n"
    "ds_write_addtid_b32 v162 offset:1632\n"
    "ds_write_addtid_b32 v163 offset:1904\n"
    "ds_write_addtid_b32 v164 offset:2176\n"
    "ds_write_addtid_b32 v165 offset:2448\n"
    "ds_write_addtid_b32 v166 offset:2720\n"
    "ds_write_addtid_b32 v167 offset:2992\n"
    "ds_write_addtid_b32 v168 offset:3264\n"
    "ds_write_addtid_b32 v169 offset:3536\n"
    "ds_write_addtid_b32 v170 offset:3808\n"
    "ds_write_addtid_b32 v171 offset:4080\n"
    "ds_write_addtid_b32 v172 offset:4352\n"
    "ds_write_addtid_b32 v173 offset:4624\n"
    "ds_write_addtid_b32 v174 offset:4896\n"
    "ds_write_addtid_b32 v175 offset:5168\n"
    "ds_write_addtid_b32 v176 offset:5440\n"
    "ds_write_addtid_b32 v177 offset:5712\n"
    "ds_write_addtid_b32 v178 offset:5984\n"
    "ds_write_addtid_b32 v179 offset:6256\n"
    "ds_write_addtid_b32 v180 offset:6528\n"
    "ds_write_addtid_b32 v181 offset:6800\n"
    "ds_write_addtid_b32 v182 offset:7072\n"
    "ds_write_addtid_b32 v183 offset:7344\n"
    "ds_write_addtid_b32 v184 offset:7616\n"
    "ds_write_addtid_b32 v185 offset:7888\n"
    "ds_write_addtid_b32 v186 offset:8160\n"
    "ds_write_addtid_b32 v187 offset:8432\n"
    "ds_read_b128 v[104:107], v240 offset:0\n"
    "ds_read_b128 v[108:111], v240 offset:32\n"
    "ds_read_b128 v[112:115], v240 offset:64\n"
    "ds_read_b128 v[116:119], v240 offset:96\n"
    "ds_read_b128 v[120:123], v240 offset:128\n"
    "ds_read_b128 v[124:127], v240 offset:160\n"
    "ds_read_b128 v[128:131], v240 offset:192\n"
    "ds_read_b128 v[132:135], v240 offset:224\n"
    "ds_write_addtid_b32 v188 offset:0\n"
    "ds_write_addtid_b32 v189 offset:272\n"
    "ds_write_addtid_b32 v190 offset:544\n"
    "s_waitcnt lgkmcnt(10)\n"
    "v_mfma_f32_32x32x16_bf16 v[56:71], v[72:75], v[104:107], 0\n"
    "ds_write_addtid_b32 v191 offset:816\n"
    "ds_write_addtid_b32 v192 offset:1088\n"
    "ds_write_addtid_b32 v193 offset:1360\n"
    "ds_write_addtid_b32 v194 offset:1632\n"
    "ds_write_addtid_b32 v195 offset:1904\n"
    "s_waitcnt lgkmcnt(14)\n"
    "v_mfma_f32_32x32x16_bf16 v[56:71], v[76:79], v[108:111], v[56:71]\n"
    "ds_write_addtid_b32 v196 offset:2176\n"
    "ds_write_addtid_b32 v197 offset:2448\n"
    "ds_write_addtid_b32 v198 offset:2720\n"
    "ds_write_addtid_b32 v199 offset:2992\n"
    "ds_write_addtid_b32 v200 offset:3264\n"
    "s_waitcnt lgkmcnt(15)\n"
    "v_mfma_f32_32x32x16_bf16 v[56:71], v[80:83], v[112:115], v[56:71]\n"
    "ds_write_addtid_b32 v201 offset:3536\n"
    "ds_write_addtid_b32 v202 offset:3808\n"
    "ds_write_addtid_b32 v203 offset:4080\n"
    "ds_write_addtid_b32 v204 offset:4352\n"
    "ds_write_addtid_b32 v205 offset:4624\n"
    "v_mfma_f32_32x32x16_bf16 v[56:71], v[84:87], v[116:119], v[56:71]\n"
    "ds_write_addtid_b32 v206 offset:4896\n"
    "ds_write_addtid_b32 v207 offset:5168\n"
    "ds_write_addtid_b32 v208 offset:5440\n"
    "ds_read_b128 v[136:139], v240 offset:0\n"
    "ds_read_b128 v[140:143], v240 offset:32\n"
    "ds_read_b128 v[144:147], v240 offset:64\n"
    "ds_read_b128 v[148:151], v240 offset:96\n"
    "ds_read_b128 v[152:155], v240 offset:128\n"
    "ds_read_b128 v[156:159], v240 offset:160\n"
    "ds_read_b128 v[160:163], v240 offset:192\n"
    "ds_read_b128 v[164:167], v240 offset:224\n"
    "ds_write_addtid_b32 v249 offset:0\n"
    "ds_write_addtid_b32 v209 offset:272\n"
    "v_mfma_f32_32x32x16_bf16 v[56:71], v[88:91], v[120:123], v[56:71]\n"
    "ds_write_addtid_b32 v210 offset:544\n"
    "ds_write_addtid_b32 v211 offset:816\n"
    "ds_write_addtid_b32 v212 offset:1088\n"
    "ds_write_addtid_b32 v213 offset:1360\n"
    "ds_write_addtid_b32 v214 offset:1632\n"
    "v_mfma_f32_32x32x16_bf16 v[56:71], v[92:95], v[124:127], v[56:71]\n"
    "ds_write_addtid_b32 v215 offset:1904\n"
    "ds_write_addtid_b32 v216 offset:2176\n"
    "ds_write_addtid_b32 v217 offset:2448\n"
    "ds_write_addtid_b32 v218 offset:2720\n"
    "ds_write_addtid_b32 v219 offset:2992\n"
    "s_waitcnt lgkmcnt(15)\n"
    "v_mfma_f32_32x32x16_bf16 v[56:71], v[96:99], v[128:131], v[56:71]\n"
    "ds_write_addtid_b32 v220 offset:3264\n"
    "ds_write_addtid_b32 v221 offset:3536\n"
    "ds_write_addtid_b32 v222 offset:3808\n"
    "ds_write_addtid_b32 v223 offset:4080\n"
    "ds_write_addtid_b32 v224 offset:4352\n"
    "v_mfma_f32_32x32x16_bf16 v[56:71], v[100:103], v[132:135], v[56:71]\n"
    "ds_write_addtid_b32 v225 offset:4624\n"
    "ds_write_addtid_b32 v226 offset:4896\n"
    "ds_write_addtid_b32 v227 offset:5168\n"
    "ds_write_addtid_b32 v228 offset:5440\n"
    "ds_write_addtid_b32 v229 offset:5712\n"
    "ds_write_addtid_b32 v230 offset:5984\n"
    "ds_write_addtid_b32 v231 offset:6256\n"
    "ds_write_addtid_b32 v232 offset:6528\n"
    "ds_write_addtid_b32 v233 offset:6800\n"
    "ds_write_addtid_b32 v234 offset:7072\n"
    "ds_write_addtid_b32 v235 offset:7344\n"
    "ds_write_addtid_b32 v236 offset:7616\n"
    "ds_write_addtid_b32 v237 offset:7888\n"
    "ds_write_addtid_b32 v238 offset:8160\n"
    "ds_write_addtid_b32 v239 offset:8432\n"
    "ds_read_b128 v[104:107], v240 offset:0\n"
    "ds_read_b128 v[108:111], v240 offset:32\n"
    "ds_read_b128 v[112:115], v240 offset:64\n"
    "ds_read_b128 v[116:119], v240 offset:96\n"
    "ds_read_b128 v[120:123], v240 offset:128\n"
    "ds_read_b128 v[124:127], v240 offset:160\n"
    "ds_read_b128 v[128:131], v240 offset:192\n"
    "ds_read_b128 v[132:135], v240 offset:224\n"
    "s_setprio 2\n"
    ".LfL69:\n"
    "ds_read_b32 v250, v243 offset:176\n"
    "s_add_u32 s49, s49, 1\n"
    "s_cmp_lt_u32 s49, 0x100000\n"
    "s_cselect_b32 s50, 0, -1\n"
    "s_waitcnt lgkmcnt(0)\n"
    "v_cmp_lt_u32 vcc, s50, v250\n"
    "s_cbranch_vccnz .LfL69\n"
    "ds_read2_b32 v[184:185], v11 offset1:32\n"
    "ds_read2_b32 v[186:187], v11 offset0:64 offset1:96\n"
    "ds_read2_b32 v[188:189], v12 offset1:32\n"
    "ds_read2_b32 v[190:191], v12 offset0:64 offset1:96\n"
    "s_waitcnt lgkmcnt(2)\n"
    "buffer_store_dwordx4 v[184:187], v10, s[24:27], s66 offen sc1\n"
    "s_waitcnt lgkmcnt(0)\n"
    "buffer_store_dwordx4 v[188:191], v10, s[24:27], s67 offen sc1\n"
    "s_setprio 0\n"
    "v_mfma_f32_32x32x16_bf16 v[56:71], v[136:139], v[104:107], v[56:71]\n"
    "v_mfma_f32_32x32x16_bf16 v[56:71], v[140:143], v[108:111], v[56:71]\n"
    "v_mfma_f32_32x32x16_bf16 v[56:71], v[144:147], v[112:115], v[56:71]\n"
    "v_mfma_f32_32x32x16_bf16 v[56:71], v[148:151], v[116:119], v[56:71]\n"
    "v_mfma_f32_32x32x16_bf16 v[56:71], v[152:155], v[120:123], v[56:71]\n"
    "v_mfma_f32_32x32x16_bf16 v[56:71], v[156:159], v[124:127], v[56:71]\n"
    "v_mfma_f32_32x32x16_bf16 v[56:71], v[160:163], v[128:131], v[56:71]\n"
    "v_mfma_f32_32x32x16_bf16 v[56:71], v[164:167], v[132:135], v[56:71]\n"
    "s_setprio 2\n"
    "s_nop 10\n"
    "v_add_f32 v56, v56, v252\n"
    "ds_write2_b32 v52, v58, v59 offset0:2 offset1:3\n"
    "ds_write2_b32 v52, v56, v57 offset1:1\n"
    "ds_write2_b32 v53, v60, v61 offset1:1\n"
    "ds_write2_b32 v54, v62, v63 offset1:1\n"
    "ds_write_b32 v55, v64\n"
    "ds_write2_b32 v253, v65, v66 offset0:33 offset1:66\n"
    "ds_write_b32 v253, v67 offset:396\n"
    "ds_add_u32 v16, v244 offset:8\n"
    ".LfL70:\n"
    "ds_read_b32 v250, v17 offset:8\n"
    "s_add_u32 s49, s49, 1\n"
    "s_cmp_lt_u32 s49, 0x100000\n"
    "s_cselect_b32 s50, 2, 0\n"
    "s_waitcnt lgkmcnt(0)\n"
    "v_cmp_gt_u32 vcc, s50, v250\n"
    "s_cbranch_vccnz .LfL70\n"
    "s_lshl_b32 s58, s43, 7\n"
    "s_add_u32 s58, s58, 0x10000\n"
    "s_waitcnt vmcnt(0)\n"
    "ds_add_rtn_u32 v250, v245, v244 offset:24\n"
    "s_waitcnt lgkmcnt(0)\n"
    "v_readfirstlane_b32 s59, v250\n"
    "s_nop 0\n"
    "s_cmp_eq_u32 s59, 3\n"
    "s_cbranch_scc0 .LfL71\n"
    "v_mov_b32 v250, s44\n"
    "v_mov_b32 v251, s45\n"
    "s_mov_b64 exec, 1\n"
    "buffer_store_dwordx2 v[250:251], off, s[24:27], s58 sc1\n"
    "s_mov_b64 exec, -1\n"
    ".LfL71:\n"
    "v_mov_b32 v16, s72\n"
    "v_add_u32 v17, 8, v16\n"
    "s_mov_b32 m0, s62\n"
    "ds_read2st64_b32 v[72:73], v36 offset1:11\n"
    "ds_read2st64_b32 v[74:75], v37 offset1:11\n"
    "ds_read2st64_b32 v[76:77], v38 offset1:11\n"
    "ds_read2st64_b32 v[78:79], v39 offset1:11\n"
    "ds_read2st64_b32 v[80:81], v40 offset1:11\n"
    "ds_read2st64_b32 v[82:83], v41 offset1:11\n"
    "ds_read2st64_b32 v[84:85], v42 offset1:11\n"
    "ds_read2st64_b32 v[86:87], v43 offset1:11\n"
    "ds_read2st64_b32 v[88:89], v44 offset1:11\n"
    "ds_read2st64_b32 v[90:91], v45 offset1:11\n"
    "ds_read2st64_b32 v[92:93], v46 offset1:11\n"
    "ds_read2st64_b32 v[94:95], v16 offset1:11\n"
    "ds_read2st64_b32 v[96:97], v17 offset1:11\n"
    "ds_read2st64_b32 v[98:99], v30 offset1:11\n"
    "ds_read2st64_b32 v[100:101], v31 offset1:11\n"
    "s_waitcnt lgkmcnt(14)\n"
    "v_add_f32 v72, v72, v73\n"
    "s_waitcnt lgkmcnt(13)\n"
    "v_add_f32 v74, v74, v75\n"
    "s_waitcnt lgkmcnt(12)\n"
    "v_add_f32 v76, v76, v77\n"
    "s_waitcnt lgkmcnt(11)\n"
    "v_add_f32 v78, v78, v79\n"
    "s_waitcnt lgkmcnt(10)\n"
    "v_add_f32 v80, v80, v81\n"
    "v_cvt_pk_bf16_f32 v104, v78, v76\n"
    "v_cvt_pk_bf16_f32 v105, v74, v72\n"
    "v_cvt_pk_bf16_f32 v106, v80, v78\n"
    "v_cvt_pk_bf16_f32 v107, v76, v74\n"
    "ds_write_b128 v26, v[104:107]\n"
    "s_waitcnt lgkmcnt(10)\n"
    "v_add_f32 v82, v82, v83\n"
    "s_waitcnt lgkmcnt(9)\n"
    "v_add_f32 v84, v84, v85\n"
    "v_cvt_pk_bf16_f32 v108, v82, v80\n"
    "v_cvt_pk_bf16_f32 v109, v78, v76\n"
    "v_cvt_pk_bf16_f32 v110, v84, v82\n"
    "v_cvt_pk_bf16_f32 v111, v80, v78\n"
    "ds_write_b128 v27, v[108:111]\n"
    "s_waitcnt lgkmcnt(9)\n"
    "v_add_f32 v86, v86, v87\n"
    "s_waitcnt lgkmcnt(8)\n"
    "v_add_f32 v88, v88, v89\n"
    "v_cvt_pk_bf16_f32 v112, v86, v84\n"
    "v_cvt_pk_bf16_f32 v113, v82, v80\n"
    "v_cvt_pk_bf16_f32 v114, v88, v86\n"
    "v_cvt_pk_bf16_f32 v115, v84, v82\n"
    "ds_write_b128 v28, v[112:115]\n"
    "s_waitcnt lgkmcnt(8)\n"
    "v_add_f32 v90, v90, v91\n"
    "s_waitcnt lgkmcnt(7)\n"
    "v_add_f32 v92, v92, v93\n"
    "v_cvt_pk_bf16_f32 v116, v90, v88\n"
    "v_cvt_pk_bf16_f32 v117, v86, v84\n"
    "v_cvt_pk_bf16_f32 v118, v92, v90\n"
    "v_cvt_pk_bf16_f32 v119, v88, v86\n"
    "ds_write_b128 v29, v[116:119]\n"
    "s_waitcnt lgkmcnt(7)\n"
    "v_add_f32 v94, v94, v95\n"
    "s_waitcnt lgkmcnt(6)\n"
    "v_add_f32 v96, v96, v97\n"
    "s_waitcnt lgkmcnt(5)\n"
    "v_add_f32 v98, v98, v99\n"
    "s_waitcnt lgkmcnt(4)\n"
    "v_add_f32 v100, v100, v101\n"
    "v_cvt_pk_bf16_f32 v122, v96, v94\n"
    "v_cvt_pk_bf16_f32 v123, v98, v100\n"
    "v_perm_b32 v120, v122, v122, v47\n"
    "v_perm_b32 v121, v122, v122, v15\n"
    "ds_write_addtid_b32 v123 offset:9344\n"
    "ds_write_b64 v18, v[120:121]\n"
    "ds_write2st64_b64 v19, v[20:21], v[20:21] offset1:18\n"
    ".LfL72:\n"
    "ds_read_b32 v250, v243 offset:24\n"
    "s_add_u32 s49, s49, 1\n"
    "s_cmp_lt_u32 s49, 0x100000\n"
    "s_cselect_b32 s50, 4, 0\n"
    "s_waitcnt lgkmcnt(0)\n"
    "v_cmp_gt_u32 vcc, s50, v250\n"
    "s_cbranch_vccnz .LfL72\n"
    ".LfL73:\n"
    "s_sleep 3\n"
    "ds_read_b32 v250, v243 offset:48\n"
    "s_add_u32 s49, s49, 1\n"
    "s_cmp_lt_u32 s49, 0x100000\n"
    "s_cselect_b32 s50, 4, 0\n"
    "s_waitcnt lgkmcnt(0)\n"
    "v_cmp_gt_u32 vcc, s50, v250\n"
    "s_cbranch_vccnz .LfL73\n"
    "s_lshl2_add_u32 s58, s41, s47\n"
    "s_mul_i32 s58, s58, 28672\n"
    "s_mov_b32 m0, 0x27e00\n"
    "s_mov_b64 exec, 1\n"
    "buffer_load_dword off, s[20:23], s58 lds\n"
    "s_mov_b64 exec, s[94:95]\n"
    "buffer_load_dwordx2 v[12:13], v0, s[24:27], 0 offen sc1\n"
    "s_mov_b64 exec, -1\n"
    ".LfH:\n"
    "s_lshr_b32 s84, s42, 1\n"
    "s_and_b32 s85, s42, 1\n"
    "s_lshl1_add_u32 s85, s41, s85\n"
    "s_lshl2_add_u32 s65, s84, s47\n"
    "s_lshl4_add_u32 s65, s65, s83\n"
    "s_lshl_b32 s65, s65, 11\n"
    "s_add_u32 s65, s65, 0x80000\n"
    "s_sub_u32 s63, s64, 0x0\n"
    "s_mov_b32 s59, 0x05040100\n"
    "s_mov_b32 s60, 0x07060302\n"
    "s_waitcnt vmcnt(0)\n"
    "v_cmp_ne_u64 vcc, s[44:45], v[12:13]\n"
    "s_nop 0\n"
    "s_and_b64 vcc, vcc, s[94:95]\n"
    "s_cbranch_scc1 .LfS74\n"
    ".LfB75:\n"
    "s_cmp_eq_u32 s42, 7\n"
    "s_cbranch_scc1 .LfH7\n"
    "buffer_load_dwordx4 v[16:19], v242, s[24:27], s63 offen sc1\n"
    "buffer_load_dwordx4 v[20:23], v242, s[24:27], s63 offen offset:1024 sc1\n"
    "v_mov_b32 v14, 0\n"
    "v_mov_b32 v15, 0\n"
    "v_lshlrev_b32 v13, 3, v241\n"
    "v_add_u32 v13, 0x22c00, v13\n"
    "ds_write2st64_b64 v13, v[14:15], v[14:15] offset1:9\n"
    "s_mul_i32 s58, s56, 576\n"
    "v_add_u32 v13, s58, v248\n"
    "s_lshl2_add_u32 s58, s41, s47\n"
    "s_lshl3_add_u32 s58, s58, s42\n"
    "s_lshl_b32 s58, s58, 11\n"
    "s_add_u32 s58, s58, 0x100000\n"
    "v_mov_b32 v36, -1\n"
    "v_mov_b32 v37, -1\n"
    "v_mov_b32 v38, -1\n"
    "v_mov_b32 v39, -1\n"
    "buffer_store_dwordx4 v[36:39], v242, s[24:27], s58 offen offset:0 sc1\n"
    "buffer_store_dwordx4 v[36:39], v242, s[24:27], s58 offen offset:1024 sc1\n"
    "s_waitcnt vmcnt(2)\n"
    "v_perm_b32 v24, v17, v16, s59\n"
    "v_perm_b32 v25, v19, v18, s59\n"
    "v_perm_b32 v26, v21, v20, s59\n"
    "v_perm_b32 v27, v23, v22, s59\n"
    "ds_write2st64_b64 v1, v[24:25], v[26:27] offset0:0 offset1:1\n"
    "v_perm_b32 v28, v17, v16, s60\n"
    "v_perm_b32 v29, v19, v18, s60\n"
    "v_perm_b32 v30, v21, v20, s60\n"
    "v_perm_b32 v31, v23, v22, s60\n"
    "ds_write2st64_b64 v1, v[28:29], v[30:31] offset0:9 offset1:10\n"
    "ds_add_u32 v245, v244 offset:52\n"
    ".LfL76:\n"
    "ds_read_b32 v250, v243 offset:52\n"
    "s_add_u32 s49, s49, 1\n"
    "s_cmp_lt_u32 s49, 0x100000\n"
    "s_cselect_b32 s50, 4, 0\n"
    "s_waitcnt lgkmcnt(0)\n"
    "v_cmp_gt_u32 vcc, s50, v250\n"
    "s_cbranch_vccnz .LfL76\n"
    "ds_read_b128 v[192:195], v13 offset:384\n"
    "ds_read_b128 v[196:199], v13 offset:368\n"
    "ds_read_b128 v[200:203], v13 offset:1408\n"
    "ds_read_b128 v[204:207], v13 offset:1392\n"
    "ds_read2_b32 v[208:209], v2 offset0:3 offset1:2\n"
    "ds_read2_b32 v[210:211], v2 offset0:1 offset1:0\n"
    "ds_read2_b32 v[212:213], v2 offset0:19 offset1:18\n"
    "ds_read2_b32 v[214:215], v2 offset0:17 offset1:16\n"
    "ds_read2_b32 v[216:217], v2 offset0:35 offset1:34\n"
    "ds_read2_b32 v[218:219], v2 offset0:33 offset1:32\n"
    "ds_read2_b32 v[220:221], v2 offset0:51 offset1:50\n"
    "ds_read2_b32 v[222:223], v2 offset0:49 offset1:48\n"
    "s_waitcnt lgkmcnt(11)\n"
    "v_mfma_f32_16x16x32_bf16 v[224:227], v[192:195], v[48:51], 0\n"
    "s_waitcnt lgkmcnt(10)\n"
    "v_mfma_f32_16x16x32_bf16 v[228:231], v[196:199], v[32:35], 0\n"
    "s_waitcnt lgkmcnt(9)\n"
    "v_mfma_f32_16x16x32_bf16 v[232:235], v[200:203], v[48:51], 0\n"
    "s_waitcnt lgkmcnt(8)\n"
    "v_mfma_f32_16x16x32_bf16 v[236:239], v[204:207], v[32:35], 0\n"
    "ds_read_b64 v[48:49], v246 offset:0\n"
    "ds_read_b64 v[50:51], v247 offset:0\n"
    "ds_read_b128 v[108:111], v248 offset:3968\n"
    "ds_read_b128 v[104:107], v248 offset:3456\n"
    "ds_read_b128 v[100:103], v248 offset:2944\n"
    "ds_read_b128 v[96:99], v248 offset:2432\n"
    "ds_read_b128 v[92:95], v248 offset:1920\n"
    "ds_read_b128 v[88:91], v248 offset:1408\n"
    "ds_read_b128 v[84:87], v248 offset:896\n"
    "ds_read_b128 v[80:83], v248 offset:384\n"
    "v_cvt_pk_bf16_f32 v68, v224, v228\n"
    "v_cvt_pk_bf16_f32 v69, v225, v229\n"
    "v_cvt_pk_bf16_f32 v70, v226, v230\n"
    "v_cvt_pk_bf16_f32 v71, v227, v231\n"
    "v_cvt_pk_bf16_f32 v72, v232, v236\n"
    "v_cvt_pk_bf16_f32 v73, v233, v237\n"
    "v_cvt_pk_bf16_f32 v74, v234, v238\n"
    "v_cvt_pk_bf16_f32 v75, v235, v239\n"
    "s_setprio 0\n"
    "s_waitcnt lgkmcnt(15)\n"
    "v_mfma_f32_16x16x32_bf16 v[32:35], v[68:71], v[208:211], 0\n"
    "s_waitcnt lgkmcnt(14)\n"
    "v_mfma_f32_16x16x32_bf16 v[36:39], v[68:71], v[212:215], 0\n"
    "s_waitcnt lgkmcnt(12)\n"
    "v_mfma_f32_16x16x32_bf16 v[40:43], v[68:71], v[216:219], 0\n"
    "s_waitcnt lgkmcnt(10)\n"
    "v_mfma_f32_16x16x32_bf16 v[44:47], v[68:71], v[220:223], 0\n"
    "v_mfma_f32_16x16x32_bf16 v[40:43], v[72:75], v[208:211], v[40:43]\n"
    "v_mfma_f32_16x16x32_bf16 v[44:47], v[72:75], v[212:215], v[44:47]\n"
    "s_waitcnt lgkmcnt(8)\n"
    "s_waitcnt lgkmcnt(7)\n"
    "v_mfma_f32_16x16x32_bf16 v[44:47], v[48:51], v[108:111], v[44:47]\n"
    "ds_read_b64 v[52:53], v246 offset:2048\n"
    "s_waitcnt lgkmcnt(7)\n"
    "v_mfma_f32_16x16x32_bf16 v[40:43], v[48:51], v[104:107], v[40:43]\n"
    "ds_read_b64 v[54:55], v247 offset:2048\n"
    "s_waitcnt lgkmcnt(7)\n"
    "v_mfma_f32_16x16x32_bf16 v[36:39], v[48:51], v[100:103], v[36:39]\n"
    "ds_read_b64 v[56:57], v246 offset:4096\n"
    "s_waitcnt lgkmcnt(7)\n"
    "v_mfma_f32_16x16x32_bf16 v[32:35], v[48:51], v[96:99], v[32:35]\n"
    "ds_read_b64 v[58:59], v247 offset:4096\n"
    "s_waitcnt lgkmcnt(7)\n"
    "v_mfma_f32_16x16x32_bf16 v[28:31], v[48:51], v[92:95], 0\n"
    "ds_read_b64 v[60:61], v246 offset:6144\n"
    "s_waitcnt lgkmcnt(7)\n"
    "v_mfma_f32_16x16x32_bf16 v[24:27], v[48:51], v[88:91], 0\n"
    "ds_read_b64 v[62:63], v247 offset:6144\n"
    "s_xor_b32 s66, s65, 0\n"
    "buffer_load_dwordx4 v[112:115], v3, s[24:27], s66 offen offset:0 sc1\n"
    "buffer_load_dwordx4 v[116:119], v3, s[24:27], s66 offen offset:1024 sc1\n"
    "s_waitcnt lgkmcnt(7)\n"
    "v_mfma_f32_16x16x32_bf16 v[20:23], v[48:51], v[84:87], 0\n"
    "ds_read_b64 v[64:65], v246 offset:8192\n"
    "s_waitcnt lgkmcnt(7)\n"
    "v_mfma_f32_16x16x32_bf16 v[16:19], v[48:51], v[80:83], 0\n"
    "ds_read_b64 v[48:49], v246 offset:512\n"
    "s_waitcnt lgkmcnt(6)\n"
    "v_mfma_f32_16x16x32_bf16 v[44:47], v[52:55], v[104:107], v[44:47]\n"
    "ds_read_b64 v[50:51], v247 offset:512\n"
    "v_mfma_f32_16x16x32_bf16 v[40:43], v[52:55], v[100:103], v[40:43]\n"
    "ds_read_b64 v[66:67], v247 offset:8192\n"
    "v_mfma_f32_16x16x32_bf16 v[36:39], v[52:55], v[96:99], v[36:39]\n"
    "ds_read_b128 v[108:111], v248 offset:3840\n"
    "v_mfma_f32_16x16x32_bf16 v[32:35], v[52:55], v[92:95], v[32:35]\n"
    "ds_read_b128 v[104:107], v248 offset:3328\n"
    "s_xor_b32 s66, s65, 8192\n"
    "buffer_load_dwordx4 v[144:147], v3, s[24:27], s66 offen offset:0 sc1\n"
    "buffer_load_dwordx4 v[148:151], v3, s[24:27], s66 offen offset:1024 sc1\n"
    "v_mfma_f32_16x16x32_bf16 v[28:31], v[52:55], v[88:91], v[28:31]\n"
    "v_mfma_f32_16x16x32_bf16 v[24:27], v[52:55], v[84:87], v[24:27]\n"
    "v_mfma_f32_16x16x32_bf16 v[20:23], v[52:55], v[80:83], v[20:23]\n"
    "ds_read_b64 v[52:53], v246 offset:2560\n"
    "s_waitcnt lgkmcnt(9)\n"
    "v_mfma_f32_16x16x32_bf16 v[44:47], v[56:59], v[100:103], v[44:47]\n"
    "ds_read_b64 v[54:55], v247 offset:2560\n"
    "v_mfma_f32_16x16x32_bf16 v[40:43], v[56:59], v[96:99], v[40:43]\n"
    "ds_read_b128 v[100:103], v248 offset:2816\n"
    "v_mfma_f32_16x16x32_bf16 v[36:39], v[56:59], v[92:95], v[36:39]\n"
    "s_xor_b32 s66, s65, 16384\n"
    "buffer_load_dwordx4 v[176:179], v3, s[24:27], s66 offen offset:0 sc1\n"
    "buffer_load_dwordx4 v[180:183], v3, s[24:27], s66 offen offset:1024 sc1\n"
    "v_mfma_f32_16x16x32_bf16 v[32:35], v[56:59], v[88:91], v[32:35]\n"
    "v_mfma_f32_16x16x32_bf16 v[28:31], v[56:59], v[84:87], v[28:31]\n"
    "v_mfma_f32_16x16x32_bf16 v[24:27], v[56:59], v[80:83], v[24:27]\n"
    "ds_read_b64 v[56:57], v246 offset:4608\n"
    "s_waitcnt lgkmcnt(10)\n"
    "v_mfma_f32_16x16x32_bf16 v[44:47], v[60:63], v[96:99], v[44:47]\n"
    "ds_read_b64 v[58:59], v247 offset:4608\n"
    "v_mfma_f32_16x16x32_bf16 v[40:43], v[60:63], v[92:95], v[40:43]\n"
    "ds_read_b128 v[96:99], v248 offset:2304\n"
    "v_mfma_f32_16x16x32_bf16 v[36:39], v[60:63], v[88:91], v[36:39]\n"
    "s_xor_b32 s66, s65, 24576\n"
    "buffer_load_dwordx4 v[208:211], v3, s[24:27], s66 offen offset:0 sc1\n"
    "buffer_load_dwordx4 v[212:215], v3, s[24:27], s66 offen offset:1024 sc1\n"
    "v_mfma_f32_16x16x32_bf16 v[32:35], v[60:63], v[84:87], v[32:35]\n"
    "v_mfma_f32_16x16x32_bf16 v[28:31], v[60:63], v[80:83], v[28:31]\n"
    "ds_read_b64 v[60:61], v246 offset:6656\n"
    "s_waitcnt lgkmcnt(9)\n"
    "v_mfma_f32_16x16x32_bf16 v[44:47], v[64:67], v[92:95], v[44:47]\n"
    "ds_read_b64 v[62:63], v247 offset:6656\n"
    "v_mfma_f32_16x16x32_bf16 v[40:43], v[64:67], v[88:91], v[40:43]\n"
    "ds_read_b128 v[92:95], v248 offset:1792\n"
    "v_mfma_f32_16x16x32_bf16 v[36:39], v[64:67], v[84:87], v[36:39]\n"
    "ds_read_b128 v[88:91], v248 offset:1280\n"
    "v_mfma_f32_16x16x32_bf16 v[32:35], v[64:67], v[80:83], v[32:35]\n"
    "ds_read_b128 v[84:87], v248 offset:768\n"
    "s_xor_b32 s66, s65, 2048\n"
    "buffer_load_dwordx4 v[120:123], v3, s[24:27], s66 offen offset:0 sc1\n"
    "buffer_load_dwordx4 v[124:127], v3, s[24:27], s66 offen offset:1024 sc1\n"
    "s_waitcnt lgkmcnt(12)\n"
    "v_mfma_f32_16x16x32_bf16 v[44:47], v[48:51], v[108:111], v[44:47]\n"
    "ds_read_b128 v[80:83], v248 offset:256\n"
    "s_waitcnt lgkmcnt(12)\n"
    "v_mfma_f32_16x16x32_bf16 v[40:43], v[48:51], v[104:107], v[40:43]\n"
    "ds_read_b128 v[108:111], v248 offset:3712\n"
    "s_waitcnt lgkmcnt(10)\n"
    "v_mfma_f32_16x16x32_bf16 v[36:39], v[48:51], v[100:103], v[36:39]\n"
    "s_waitcnt lgkmcnt(7)\n"
    "v_mfma_f32_16x16x32_bf16 v[32:35], v[48:51], v[96:99], v[32:35]\n"
    "s_waitcnt lgkmcnt(4)\n"
    "v_mfma_f32_16x16x32_bf16 v[28:31], v[48:51], v[92:95], v[28:31]\n"
    "s_waitcnt lgkmcnt(3)\n"
    "v_mfma_f32_16x16x32_bf16 v[24:27], v[48:51], v[88:91], v[24:27]\n"
    "s_xor_b32 s66, s65, 10240\n"
    "buffer_load_dwordx4 v[152:155], v3, s[24:27], s66 offen offset:0 sc1\n"
    "buffer_load_dwordx4 v[156:159], v3, s[24:27], s66 offen offset:1024 sc1\n"
    "s_waitcnt lgkmcnt(2)\n"
    "v_mfma_f32_16x16x32_bf16 v[20:23], v[48:51], v[84:87], v[20:23]\n"
    "s_waitcnt lgkmcnt(1)\n"
    "v_mfma_f32_16x16x32_bf16 v[16:19], v[48:51], v[80:83], v[16:19]\n"
    "ds_read_b64 v[48:49], v246 offset:1024\n"
    "v_mfma_f32_16x16x32_bf16 v[44:47], v[52:55], v[104:107], v[44:47]\n"
    "ds_read_b64 v[50:51], v247 offset:1024\n"
    "v_mfma_f32_16x16x32_bf16 v[40:43], v[52:55], v[100:103], v[40:43]\n"
    "ds_read_b128 v[104:107], v248 offset:3200\n"
    "v_mfma_f32_16x16x32_bf16 v[36:39], v[52:55], v[96:99], v[36:39]\n"
    "v_mfma_f32_16x16x32_bf16 v[32:35], v[52:55], v[92:95], v[32:35]\n"
    "s_xor_b32 s66, s65, 18432\n"
    "buffer_load_dwordx4 v[184:187], v3, s[24:27], s66 offen offset:0 sc1\n"
    "buffer_load_dwordx4 v[188:191], v3, s[24:27], s66 offen offset:1024 sc1\n"
    "v_mfma_f32_16x16x32_bf16 v[28:31], v[52:55], v[88:91], v[28:31]\n"
    "v_mfma_f32_16x16x32_bf16 v[24:27], v[52:55], v[84:87], v[24:27]\n"
    "v_mfma_f32_16x16x32_bf16 v[20:23], v[52:55], v[80:83], v[20:23]\n"
    "ds_read_b64 v[52:53], v246 offset:3072\n"
    "v_mfma_f32_16x16x32_bf16 v[44:47], v[56:59], v[100:103], v[44:47]\n"
    "ds_read_b64 v[54:55], v247 offset:3072\n"
    "v_mfma_f32_16x16x32_bf16 v[40:43], v[56:59], v[96:99], v[40:43]\n"
    "ds_read_b128 v[100:103], v248 offset:2688\n"
    "v_mfma_f32_16x16x32_bf16 v[36:39], v[56:59], v[92:95], v[36:39]\n"
    "s_xor_b32 s66, s65, 26624\n"
    "buffer_load_dwordx4 v[216:219], v3, s[24:27], s66 offen offset:0 sc1\n"
    "buffer_load_dwordx4 v[220:223], v3, s[24:27], s66 offen offset:1024 sc1\n"
    "v_mfma_f32_16x16x32_bf16 v[32:35], v[56:59], v[88:91], v[32:35]\n"
    "v_mfma_f32_16x16x32_bf16 v[28:31], v[56:59], v[84:87], v[28:31]\n"
    "v_mfma_f32_16x16x32_bf16 v[24:27], v[56:59], v[80:83], v[24:27]\n"
    "ds_read_b64 v[56:57], v246 offset:5120\n"
    "v_mfma_f32_16x16x32_bf16 v[44:47], v[60:63], v[96:99], v[44:47]\n"
    "ds_read_b64 v[58:59], v247 offset:5120\n"
    "v_mfma_f32_16x16x32_bf16 v[40:43], v[60:63], v[92:95], v[40:43]\n"
    "ds_read_b128 v[96:99], v248 offset:2176\n"
    "v_mfma_f32_16x16x32_bf16 v[36:39], v[60:63], v[88:91], v[36:39]\n"
    "ds_read_b128 v[92:95], v248 offset:1664\n"
    "s_xor_b32 s66, s65, 4096\n"
    "buffer_load_dwordx4 v[128:131], v3, s[24:27], s66 offen offset:0 sc1\n"
    "buffer_load_dwordx4 v[132:135], v3, s[24:27], s66 offen offset:1024 sc1\n"
    "v_mfma_f32_16x16x32_bf16 v[32:35], v[60:63], v[84:87], v[32:35]\n"
    "ds_read_b128 v[88:91], v248 offset:1152\n"
    "v_mfma_f32_16x16x32_bf16 v[28:31], v[60:63], v[80:83], v[28:31]\n"
    "ds_read_b128 v[84:87], v248 offset:640\n"
    "s_waitcnt lgkmcnt(10)\n"
    "v_mfma_f32_16x16x32_bf16 v[44:47], v[48:51], v[108:111], v[44:47]\n"
    "ds_read_b128 v[80:83], v248 offset:128\n"
    "s_waitcnt lgkmcnt(10)\n"
    "v_mfma_f32_16x16x32_bf16 v[40:43], v[48:51], v[104:107], v[40:43]\n"
    "ds_read_b64 v[60:61], v246 offset:7168\n"
    "s_waitcnt lgkmcnt(8)\n"
    "v_mfma_f32_16x16x32_bf16 v[36:39], v[48:51], v[100:103], v[36:39]\n"
    "ds_read_b64 v[62:63], v247 offset:7168\n"
    "s_waitcnt lgkmcnt(6)\n"
    "v_mfma_f32_16x16x32_bf16 v[32:35], v[48:51], v[96:99], v[32:35]\n"
    "ds_read_b128 v[108:111], v248 offset:3584\n"
    "s_xor_b32 s66, s65, 12288\n"
    "buffer_load_dwordx4 v[160:163], v3, s[24:27], s66 offen offset:0 sc1\n"
    "buffer_load_dwordx4 v[164:167], v3, s[24:27], s66 offen offset:1024 sc1\n"
    "s_waitcnt lgkmcnt(6)\n"
    "v_mfma_f32_16x16x32_bf16 v[28:31], v[48:51], v[92:95], v[28:31]\n"
    "s_waitcnt lgkmcnt(5)\n"
    "v_mfma_f32_16x16x32_bf16 v[24:27], v[48:51], v[88:91], v[24:27]\n"
    "s_waitcnt lgkmcnt(4)\n"
    "v_mfma_f32_16x16x32_bf16 v[20:23], v[48:51], v[84:87], v[20:23]\n"
    "s_waitcnt lgkmcnt(3)\n"
    "v_mfma_f32_16x16x32_bf16 v[16:19], v[48:51], v[80:83], v[16:19]\n"
    "ds_read_b64 v[48:49], v246 offset:1536\n"
    "v_mfma_f32_16x16x32_bf16 v[44:47], v[52:55], v[104:107], v[44:47]\n"
    "ds_read_b64 v[50:51], v247 offset:1536\n"
    "v_mfma_f32_16x16x32_bf16 v[40:43], v[52:55], v[100:103], v[40:43]\n"
    "ds_read_b128 v[104:107], v248 offset:3072\n"
    "s_xor_b32 s66, s65, 20480\n"
    "buffer_load_dwordx4 v[192:195], v3, s[24:27], s66 offen offset:0 sc1\n"
    "buffer_load_dwordx4 v[196:199], v3, s[24:27], s66 offen offset:1024 sc1\n"
    "v_mfma_f32_16x16x32_bf16 v[36:39], v[52:55], v[96:99], v[36:39]\n"
    "v_mfma_f32_16x16x32_bf16 v[32:35], v[52:55], v[92:95], v[32:35]\n"
    "v_mfma_f32_16x16x32_bf16 v[28:31], v[52:55], v[88:91], v[28:31]\n"
    "v_mfma_f32_16x16x32_bf16 v[24:27], v[52:55], v[84:87], v[24:27]\n"
    "v_mfma_f32_16x16x32_bf16 v[20:23], v[52:55], v[80:83], v[20:23]\n"
    "ds_read_b64 v[52:53], v246 offset:3584\n"
    "v_mfma_f32_16x16x32_bf16 v[44:47], v[56:59], v[100:103], v[44:47]\n"
    "ds_read_b64 v[54:55], v247 offset:3584\n"
    "s_xor_b32 s66, s65, 28672\n"
    "buffer_load_dwordx4 v[224:227], v3, s[24:27], s66 offen offset:0 sc1\n"
    "buffer_load_dwordx4 v[228:231], v3, s[24:27], s66 offen offset:1024 sc1\n"
    "v_mfma_f32_16x16x32_bf16 v[40:43], v[56:59], v[96:99], v[40:43]\n"
    "ds_read_b128 v[100:103], v248 offset:2560\n"
    "v_mfma_f32_16x16x32_bf16 v[36:39], v[56:59], v[92:95], v[36:39]\n"
    "v_mfma_f32_16x16x32_bf16 v[32:35], v[56:59], v[88:91], v[32:35]\n"
    "v_mfma_f32_16x16x32_bf16 v[28:31], v[56:59], v[84:87], v[28:31]\n"
    "v_mfma_f32_16x16x32_bf16 v[24:27], v[56:59], v[80:83], v[24:27]\n"
    "ds_read_b64 v[56:57], v246 offset:5632\n"
    "s_waitcnt lgkmcnt(8)\n"
    "v_mfma_f32_16x16x32_bf16 v[44:47], v[60:63], v[96:99], v[44:47]\n"
    "ds_read_b64 v[58:59], v247 offset:5632\n"
    "s_xor_b32 s66, s65, 6144\n"
    "buffer_load_dwordx4 v[136:139], v3, s[24:27], s66 offen offset:0 sc1\n"
    "buffer_load_dwordx4 v[140:143], v3, s[24:27], s66 offen offset:1024 sc1\n"
    "v_mfma_f32_16x16x32_bf16 v[40:43], v[60:63], v[92:95], v[40:43]\n"
    "ds_read_b128 v[96:99], v248 offset:2048\n"
    "v_mfma_f32_16x16x32_bf16 v[36:39], v[60:63], v[88:91], v[36:39]\n"
    "ds_read_b128 v[92:95], v248 offset:1536\n"
    "v_mfma_f32_16x16x32_bf16 v[32:35], v[60:63], v[84:87], v[32:35]\n"
    "ds_read_b128 v[88:91], v248 offset:1024\n"
    "v_mfma_f32_16x16x32_bf16 v[28:31], v[60:63], v[80:83], v[28:31]\n"
    "ds_read_b128 v[84:87], v248 offset:512\n"
    "s_waitcnt lgkmcnt(10)\n"
    "v_mfma_f32_16x16x32_bf16 v[44:47], v[48:51], v[108:111], v[44:47]\n"
    "ds_read_b128 v[80:83], v248 offset:0\n"
    "s_waitcnt lgkmcnt(10)\n"
    "v_mfma_f32_16x16x32_bf16 v[40:43], v[48:51], v[104:107], v[40:43]\n"
    "ds_read_b64 v[60:61], v246 offset:7680\n"
    "s_xor_b32 s66, s65, 14336\n"
    "buffer_load_dwordx4 v[168:171], v3, s[24:27], s66 offen offset:0 sc1\n"
    "buffer_load_dwordx4 v[172:175], v3, s[24:27], s66 offen offset:1024 sc1\n"
    "s_waitcnt lgkmcnt(8)\n"
    "v_mfma_f32_16x16x32_bf16 v[36:39], v[48:51], v[100:103], v[36:39]\n"
    "ds_read_b64 v[62:63], v247 offset:7680\n"
    "s_waitcnt lgkmcnt(6)\n"
    "v_mfma_f32_16x16x32_bf16 v[32:35], v[48:51], v[96:99], v[32:35]\n"
    "s_waitcnt lgkmcnt(5)\n"
    "v_mfma_f32_16x16x32_bf16 v[28:31], v[48:51], v[92:95], v[28:31]\n"
    "s_waitcnt lgkmcnt(4)\n"
    "v_mfma_f32_16x16x32_bf16 v[24:27], v[48:51], v[88:91], v[24:27]\n"
    "s_waitcnt lgkmcnt(3)\n"
    "v_mfma_f32_16x16x32_bf16 v[20:23], v[48:51], v[84:87], v[20:23]\n"
    "s_waitcnt lgkmcnt(2)\n"
    "v_mfma_f32_16x16x32_bf16 v[16:19], v[48:51], v[80:83], v[16:19]\n"
    "s_xor_b32 s66, s65, 22528\n"
    "buffer_load_dwordx4 v[200:203], v3, s[24:27], s66 offen offset:0 sc1\n"
    "buffer_load_dwordx4 v[204:207], v3, s[24:27], s66 offen offset:1024 sc1\n"
    "v_mfma_f32_16x16x32_bf16 v[44:47], v[52:55], v[104:107], v[44:47]\n"
    "v_mfma_f32_16x16x32_bf16 v[40:43], v[52:55], v[100:103], v[40:43]\n"
    "v_mfma_f32_16x16x32_bf16 v[36:39], v[52:55], v[96:99], v[36:39]\n"
    "v_mfma_f32_16x16x32_bf16 v[32:35], v[52:55], v[92:95], v[32:35]\n"
    "v_mfma_f32_16x16x32_bf16 v[28:31], v[52:55], v[88:91], v[28:31]\n"
    "v_mfma_f32_16x16x32_bf16 v[24:27], v[52:55], v[84:87], v[24:27]\n"
    "s_xor_b32 s66, s65, 30720\n"
    "buffer_load_dwordx4 v[232:235], v3, s[24:27], s66 offen offset:0 sc1\n"
    "buffer_load_dwordx4 v[236:239], v3, s[24:27], s66 offen offset:1024 sc1\n"
    "v_mfma_f32_16x16x32_bf16 v[20:23], v[52:55], v[80:83], v[20:23]\n"
    "v_mfma_f32_16x16x32_bf16 v[44:47], v[56:59], v[100:103], v[44:47]\n"
    "v_mfma_f32_16x16x32_bf16 v[40:43], v[56:59], v[96:99], v[40:43]\n"
    "v_mfma_f32_16x16x32_bf16 v[36:39], v[56:59], v[92:95], v[36:39]\n"
    "s_setprio 2\n"
    "v_mfma_f32_16x16x32_bf16 v[32:35], v[56:59], v[88:91], v[32:35]\n"
    "v_mfma_f32_16x16x32_bf16 v[28:31], v[56:59], v[84:87], v[28:31]\n"
    "v_mfma_f32_16x16x32_bf16 v[24:27], v[56:59], v[80:83], v[24:27]\n"
    "s_waitcnt lgkmcnt(0)\n"
    "v_mfma_f32_16x16x32_bf16 v[44:47], v[60:63], v[96:99], v[44:47]\n"
    "v_mfma_f32_16x16x32_bf16 v[40:43], v[60:63], v[92:95], v[40:43]\n"
    "v_mfma_f32_16x16x32_bf16 v[36:39], v[60:63], v[88:91], v[36:39]\n"
    "v_mfma_f32_16x16x32_bf16 v[32:35], v[60:63], v[84:87], v[32:35]\n"
    "v_mfma_f32_16x16x32_bf16 v[28:31], v[60:63], v[80:83], v[28:31]\n"
    "s_setprio 2\n"
    "ds_write_b128 v4, v[16:19] offset:0\n"
    "ds_write_b128 v4, v[20:23] offset:1024\n"
    "ds_write_b128 v4, v[24:27] offset:2048\n"
    "s_nop 3\n"
    "ds_write_b128 v4, v[28:31] offset:3072\n"
    "ds_write_b128 v4, v[32:35] offset:4096\n"
    "ds_write_b128 v4, v[36:39] offset:5120\n"
    "ds_write_b128 v4, v[40:43] offset:6144\n"
    "ds_write_b128 v4, v[44:47] offset:7168\n"
    "ds_add_u32 v245, v244 offset:20\n"
    ".LfL77:\n"
    "ds_read_b32 v250, v243 offset:20\n"
    "s_add_u32 s49, s49, 1\n"
    "s_cmp_lt_u32 s49, 0x100000\n"
    "s_cselect_b32 s50, 4, 0\n"
    "s_waitcnt lgkmcnt(0)\n"
    "v_cmp_gt_u32 vcc, s50, v250\n"
    "s_cbranch_vccnz .LfL77\n"
    "ds_read_b128 v[16:19], v5 offset:0\n"
    "ds_read_b128 v[20:23], v5 offset:8192\n"
    "ds_read_b128 v[24:27], v5 offset:16384\n"
    "ds_read_b128 v[28:31], v5 offset:24576\n"
    "ds_read_b128 v[32:35], v5 offset:1024\n"
    "ds_read_b128 v[36:39], v5 offset:9216\n"
    "ds_read_b128 v[40:43], v5 offset:17408\n"
    "ds_read_b128 v[44:47], v5 offset:25600\n"
    "s_waitcnt lgkmcnt(6)\n"
    "v_add_f32 v16, v16, v20\n"
    "v_add_f32 v17, v17, v21\n"
    "v_add_f32 v18, v18, v22\n"
    "v_add_f32 v19, v19, v23\n"
    "s_waitcnt lgkmcnt(4)\n"
    "v_add_f32 v24, v24, v28\n"
    "v_add_f32 v25, v25, v29\n"
    "v_add_f32 v26, v26, v30\n"
    "v_add_f32 v27, v27, v31\n"
    "v_cvt_pk_bf16_f32 v20, v16, v24\n"
    "v_cvt_pk_bf16_f32 v21, v17, v25\n"
    "v_cvt_pk_bf16_f32 v22, v18, v26\n"
    "v_cvt_pk_bf16_f32 v23, v19, v27\n"
    "buffer_store_dwordx4 v[20:23], v242, s[24:27], s64 offen offset:0 sc1\n"
    "s_waitcnt lgkmcnt(2)\n"
    "v_add_f32 v32, v32, v36\n"
    "v_add_f32 v33, v33, v37\n"
    "v_add_f32 v34, v34, v38\n"
    "v_add_f32 v35, v35, v39\n"
    "s_waitcnt lgkmcnt(0)\n"
    "v_add_f32 v40, v40, v44\n"
    "v_add_f32 v41, v41, v45\n"
    "v_add_f32 v42, v42, v46\n"
    "v_add_f32 v43, v43, v47\n"
    "v_cvt_pk_bf16_f32 v36, v32, v40\n"
    "v_cvt_pk_bf16_f32 v37, v33, v41\n"
    "v_cvt_pk_bf16_f32 v38, v34, v42\n"
    "v_cvt_pk_bf16_f32 v39, v35, v43\n"
    "buffer_store_dwordx4 v[36:39], v242, s[24:27], s64 offen offset:1024 sc1\n"
    ".LfHZ:\n"
    "s_lshl_b32 s58, s43, 7\n"
    "s_add_u32 s58, s58, 0x30000\n"
    "s_waitcnt vmcnt(0)\n"
    "ds_add_rtn_u32 v250, v245, v244 offset:60\n"
    "s_waitcnt lgkmcnt(0)\n"
    "v_readfirstlane_b32 s59, v250\n"
    "s_nop 0\n"
    "s_cmp_eq_u32 s59, 3\n"
    "s_cbranch_scc0 .LfL78\n"
    "v_mov_b32 v250, s44\n"
    "v_mov_b32 v251, s45\n"
    "s_mov_b64 exec, 1\n"
    "buffer_store_dwordx2 v[250:251], off, s[24:27], s58 sc1\n"
    "s_mov_b64 exec, -1\n"
    ".LfL78:\n"
    ".LfHT:\n"
    "s_setprio 2\n"
    "s_lshr_b32 s58, s47, 1\n"
    "s_lshl1_add_u32 s58, s41, s58\n"
    "s_lshl4_add_u32 s58, s58, s42\n"
    "s_and_b32 s59, s47, 1\n"
    "s_lshl1_add_u32 s58, s58, s59\n"
    "s_lshl_b32 s58, s58, 10\n"
    "s_add_u32 s72, s58, 0x100000\n"
    "s_add_u32 s73, s72, 16384\n"
    "s_xor_b32 s64, s83, s47\n"
    "s_lshl3_add_u32 s64, s85, s64\n"
    "s_lshl_b32 s64, s64, 12\n"
    "s_add_u32 s64, s64, 0x100000\n"
    "s_add_u32 s65, s64, 16384\n"
    "s_mul_i32 s60, s85, 57344\n"
    "s_mul_i32 s61, s84, 256\n"
    "s_add_u32 s60, s60, s61\n"
    "s_lshl_b32 s61, s47, 6\n"
    "s_add_u32 s60, s60, s61\n"
    "s_add_u32 s61, s60, 3584\n"
    "s_add_u32 s62, s60, 28672\n"
    "s_add_u32 s63, s60, 32256\n"
    "s_mov_b32 s66, 0x48000000\n"
    "s_lshl_b32 s67, s47, 12\n"
    "s_add_u32 s67, s67, 0x1a800\n"
    "s_add_u32 s68, s67, 16384\n"
    "s_cmp_eq_u32 s47, 3\n"
    "s_cselect_b32 s74, 0, s54\n"
    "v_lshl_or_b32 v11, v241, 24, s74\n"
    "s_lshl_b32 s76, s43, 7\n"
    "s_add_u32 s76, s76, 0x20000\n"
    "s_add_u32 s77, s76, 0x8000\n"
    "s_mov_b64 s[78:79], -1\n"
    "v_mov_b32 v252, s44\n"
    "v_mov_b32 v253, s45\n"
    "v_add_u32 v10, 0x8000, v9\n"
    "s_mov_b64 exec, 12\n"
    "v_add_u32 v9, 0xff00, v9\n"
    "s_mov_b64 exec, -1\n"
    ".LfL79:\n"
    "s_sleep 2\n"
    "ds_read_b32 v250, v243 offset:60\n"
    "s_add_u32 s49, s49, 1\n"
    "s_cmp_lt_u32 s49, 0x100000\n"
    "s_cselect_b32 s50, 4, 0\n"
    "s_waitcnt lgkmcnt(0)\n"
    "v_cmp_gt_u32 vcc, s50, v250\n"
    "s_cbranch_vccnz .LfL79\n"
    ".LfL80:\n"
    "s_sleep 1\n"
    "ds_read_b32 v250, v243 offset:32\n"
    "s_add_u32 s49, s49, 1\n"
    "s_cmp_lt_u32 s49, 0x100000\n"
    "s_cselect_b32 s50, 4, 0\n"
    "s_waitcnt lgkmcnt(0)\n"
    "v_cmp_gt_u32 vcc, s50, v250\n"
    "s_cbranch_vccnz .LfL80\n"
    "ds_read_b128 v[16:19], v6 offset:0\n"
    "ds_read_b128 v[20:23], v6 offset:1024\n"
    "ds_read_b128 v[40:43], v6 offset:16384\n"
    "ds_read_b128 v[44:47], v6 offset:17408\n"
    "ds_read_b128 v[48:51], v6 offset:32768\n"
    "ds_read_b128 v[52:55], v6 offset:33792\n"
    "ds_read_b128 v[56:59], v6 offset:49152\n"
    "ds_read_b128 v[60:63], v6 offset:50176\n"
    "s_waitcnt lgkmcnt(4)\n"
    "v_add_f32 v16, v16, v40\n"
    "v_add_f32 v17, v17, v41\n"
    "v_add_f32 v18, v18, v42\n"
    "v_add_f32 v19, v19, v43\n"
    "v_add_f32 v20, v20, v44\n"
    "v_add_f32 v21, v21, v45\n"
    "v_add_f32 v22, v22, v46\n"
    "v_add_f32 v23, v23, v47\n"
    "s_waitcnt lgkmcnt(2)\n"
    "v_add_f32 v16, v16, v48\n"
    "v_add_f32 v17, v17, v49\n"
    "v_add_f32 v18, v18, v50\n"
    "v_add_f32 v19, v19, v51\n"
    "v_add_f32 v20, v20, v52\n"
    "v_add_f32 v21, v21, v53\n"
    "v_add_f32 v22, v22, v54\n"
    "v_add_f32 v23, v23, v55\n"
    "s_waitcnt lgkmcnt(0)\n"
    "v_add_f32 v16, v16, v56\n"
    "v_add_f32 v17, v17, v57\n"
    "v_add_f32 v18, v18, v58\n"
    "v_add_f32 v19, v19, v59\n"
    "v_add_f32 v20, v20, v60\n"
    "v_add_f32 v21, v21, v61\n"
    "v_add_f32 v22, v22, v62\n"
    "v_add_f32 v23, v23, v63\n"
    "v_rcp_f32 v34, v23\n"
    "v_add_u32 v33, 0xe8800000, v23\n"
    "v_add_f32 v32, |v16|, |v17|\n"
    "v_cmp_lt_u32 s[52:53], s66, v33\n"
    "v_add_f32 v33, |v18|, |v19|\n"
    "v_add_f32 v35, |v20|, |v21|\n"
    "v_add_f32 v32, v32, |v22|\n"
    "v_add_f32 v33, v33, v35\n"
    "v_add_f32 v32, v32, v33\n"
    "v_cmp_ngt_f32 vcc, 0x7b800000, v32\n"
    "s_nop 0\n"
    "s_or_b64 vcc, vcc, s[52:53]\n"
    "s_cbranch_scc1 .LfFB0\n"
    ".LfFBback0:\n"
    "v_mul_f32 v16, v16, v34\n"
    "v_mul_f32 v17, v17, v34\n"
    "v_mul_f32 v18, v18, v34\n"
    "v_mul_f32 v19, v19, v34\n"
    "v_mul_f32 v20, v20, v34\n"
    "v_mul_f32 v21, v21, v34\n"
    "v_mul_f32 v22, v22, v34\n"
    "v_cvt_pk_bf16_f32 v16, v16, v17\n"
    "v_cvt_pk_bf16_f32 v17, v18, v19\n"
    "v_cvt_pk_bf16_f32 v18, v20, v21\n"
    "v_cvt_pk_bf16_f32 v19, v22, 0\n"
    "buffer_store_dwordx4 v[16:19], v242, s[24:27], s72 offen sc1\n"
    "s_memrealtime s[86:87]\n"
    "v_mov_b32 v36, s44\n"
    "v_mov_b32 v37, s45\n"
    "s_waitcnt lgkmcnt(0)\n"
    "v_mov_b32 v38, s86\n"
    "buffer_store_dwordx3 v[36:38], v11, s[24:27], s76 offen sc1\n"
    "s_or_b32 s58, s2, s47\n"
    "s_cbranch_scc1 .Lfnoep\n"
    "v_mov_b32 v250, s44\n"
    "v_mov_b32 v251, s45\n"
    "s_mov_b64 exec, 1\n"
    "buffer_store_dwordx2 v[250:251], off, s[24:27], s58 sc1\n"
    "s_mov_b64 exec, -1\n"
    ".Lfnoep:\n"
    "s_sleep 8\n"
    "s_mov_b64 exec, 15\n"
    "buffer_load_dwordx3 v[12:14], v9, s[24:27], 0 offen sc1\n"
    "s_mov_b64 exec, -1\n"
    ".LfL81:\n"
    "ds_read_b32 v250, v243 offset:36\n"
    "s_add_u32 s49, s49, 1\n"
    "s_cmp_lt_u32 s49, 0x100000\n"
    "s_cselect_b32 s50, 4, 0\n"
    "s_waitcnt lgkmcnt(0)\n"
    "v_cmp_gt_u32 vcc, s50, v250\n"
    "s_cbranch_vccnz .LfL81\n"
    "ds_read_b128 v[24:27], v6 offset:8192\n"
    "ds_read_b128 v[28:31], v6 offset:9216\n"
    "ds_read_b128 v[40:43], v6 offset:24576\n"
    "ds_read_b128 v[44:47], v6 offset:25600\n"
    "ds_read_b128 v[48:51], v6 offset:40960\n"
    "ds_read_b128 v[52:55], v6 offset:41984\n"
    "ds_read_b128 v[56:59], v6 offset:57344\n"
    "ds_read_b128 v[60:63], v6 offset:58368\n"
    "s_waitcnt lgkmcnt(4)\n"
    "v_pk_add_f32 v[24:25], v[24:25], v[40:41]\n"
    "v_pk_add_f32 v[26:27], v[26:27], v[42:43]\n"
    "v_pk_add_f32 v[28:29], v[28:29], v[44:45]\n"
    "v_pk_add_f32 v[30:31], v[30:31], v[46:47]\n"
    "s_waitcnt lgkmcnt(2)\n"
    "v_pk_add_f32 v[24:25], v[24:25], v[48:49]\n"
    "v_pk_add_f32 v[26:27], v[26:27], v[50:51]\n"
    "v_pk_add_f32 v[28:29], v[28:29], v[52:53]\n"
    "v_pk_add_f32 v[30:31], v[30:31], v[54:55]\n"
    "s_waitcnt lgkmcnt(0)\n"
    "v_pk_add_f32 v[24:25], v[24:25], v[56:57]\n"
    "v_pk_add_f32 v[26:27], v[26:27], v[58:59]\n"
    "v_pk_add_f32 v[28:29], v[28:29], v[60:61]\n"
    "v_pk_add_f32 v[30:31], v[30:31], v[62:63]\n"
    "v_rcp_f32 v34, v31\n"
    "v_add_u32 v33, 0xe8800000, v31\n"
    "v_add_f32 v32, |v24|, |v25|\n"
    "v_cmp_lt_u32 s[52:53], s66, v33\n"
    "v_add_f32 v33, |v26|, |v27|\n"
    "v_add_f32 v35, |v28|, |v29|\n"
    "v_add_f32 v32, v32, |v30|\n"
    "v_add_f32 v33, v33, v35\n"
    "v_add_f32 v32, v32, v33\n"
    "v_cmp_ngt_f32 vcc, 0x7b800000, v32\n"
    "s_nop 0\n"
    "s_or_b64 vcc, vcc, s[52:53]\n"
    "s_cbranch_scc1 .LfFB1\n"
    ".LfFBback1:\n"
    "v_pk_mul_f32 v[24:25], v[24:25], v[34:35] op_sel_hi:[1,0]\n"
    "v_pk_mul_f32 v[26:27], v[26:27], v[34:35] op_sel_hi:[1,0]\n"
    "v_pk_mul_f32 v[28:29], v[28:29], v[34:35] op_sel_hi:[1,0]\n"
    "v_pk_mul_f32 v[30:31], v[30:31], v[34:35] op_sel_hi:[1,0]\n"
    "v_cvt_pk_bf16_f32 v24, v24, v25\n"
    "v_cvt_pk_bf16_f32 v25, v26, v27\n"
    "v_cvt_pk_bf16_f32 v26, v28, v29\n"
    "v_cvt_pk_bf16_f32 v27, v30, 0\n"
    "buffer_store_dwordx4 v[24:27], v242, s[24:27], s73 offen sc1\n"
    "s_memrealtime s[88:89]\n"
    "s_waitcnt vmcnt(1)\n"
    "v_cmp_ne_u64 vcc, s[44:45], v[12:13]\n"
    "s_nop 0\n"
    "s_and_b64 vcc, vcc, 15\n"
    "s_cbranch_scc1 .LfH0S\n"
    ".LfH0B:\n"
    "s_mov_b32 m0, s67\n"
    "s_nop 0\n"
    "buffer_load_dwordx4 v242, s[24:27], s64 offen offset:0 sc1 lds\n"
    "buffer_load_dwordx4 v242, s[24:27], s64 offen offset:1024 sc1 lds\n"
    "buffer_load_dwordx4 v242, s[24:27], s64 offen offset:2048 sc1 lds\n"
    "buffer_load_dwordx4 v242, s[24:27], s64 offen offset:3072 sc1 lds\n"
    "v_readlane_b32 s58, v14, 0\n"
    "v_readlane_b32 s59, v14, 1\n"
    "s_waitcnt lgkmcnt(0)\n"
    "s_sub_i32 s58, s58, s86\n"
    "s_sub_i32 s59, s59, s86\n"
    "s_max_i32 s58, s58, s59\n"
    "s_min_i32 s58, s58, 300\n"
    "s_add_i32 s58, s58, 32\n"
    ".LfTW:\n"
    "s_memrealtime s[86:87]\n"
    "s_waitcnt lgkmcnt(0)\n"
    "s_sub_u32 s86, s86, s88\n"
    "s_cmp_lt_i32 s86, s58\n"
    "s_cbranch_scc1 .LfTW\n"
    "s_mov_b32 m0, s68\n"
    "s_nop 0\n"
    "buffer_load_dwordx4 v242, s[24:27], s65 offen offset:0 sc1 lds\n"
    "buffer_load_dwordx4 v242, s[24:27], s65 offen offset:1024 sc1 lds\n"
    "buffer_load_dwordx4 v242, s[24:27], s65 offen offset:2048 sc1 lds\n"
    "buffer_load_dwordx4 v242, s[24:27], s65 offen offset:3072 sc1 lds\n"
    "ds_add_u32 v245, v244 offset:64\n"
    "s_waitcnt vmcnt(4)\n"
    "ds_add_u32 v245, v244 offset:40\n"
    ".LfL82:\n"
    "ds_read_b32 v250, v243 offset:40\n"
    "s_add_u32 s49, s49, 1\n"
    "s_cmp_lt_u32 s49, 0x100000\n"
    "s_cselect_b32 s50, 4, 0\n"
    "s_waitcnt lgkmcnt(0)\n"
    "v_cmp_gt_u32 vcc, s50, v250\n"
    "s_cbranch_vccnz .LfL82\n"
    ".LfOP:\n"
    "ds_read_b128 v[40:43], v7 offset:0\n"
    "ds_read_b128 v[44:47], v7 offset:2048\n"
    "ds_read_b128 v[48:51], v7 offset:4096\n"
    "ds_read_b128 v[52:55], v7 offset:6144\n"
    "ds_read_b128 v[56:59], v7 offset:8192\n"
    "ds_read_b128 v[60:63], v7 offset:10240\n"
    "s_waitcnt lgkmcnt(5)\n"
    "v_mfma_f32_16x16x32_bf16 v[80:83], v[40:43], v[112:115], 0\n"
    "v_mfma_f32_16x16x32_bf16 v[84:87], v[40:43], v[176:179], 0\n"
    "ds_read_b128 v[64:67], v7 offset:12288\n"
    "s_waitcnt lgkmcnt(5)\n"
    "v_mfma_f32_16x16x32_bf16 v[80:83], v[44:47], v[116:119], v[80:83]\n"
    "v_mfma_f32_16x16x32_bf16 v[84:87], v[44:47], v[180:183], v[84:87]\n"
    "ds_read_b128 v[68:71], v7 offset:14336\n"
    "s_waitcnt lgkmcnt(5)\n"
    "v_mfma_f32_16x16x32_bf16 v[80:83], v[48:51], v[120:123], v[80:83]\n"
    "v_mfma_f32_16x16x32_bf16 v[84:87], v[48:51], v[184:187], v[84:87]\n"
    "ds_read_b128 v[40:43], v7 offset:256\n"
    "s_waitcnt lgkmcnt(5)\n"
    "v_mfma_f32_16x16x32_bf16 v[80:83], v[52:55], v[124:127], v[80:83]\n"
    "v_mfma_f32_16x16x32_bf16 v[84:87], v[52:55], v[188:191], v[84:87]\n"
    "ds_read_b128 v[44:47], v7 offset:2304\n"
    "s_waitcnt lgkmcnt(5)\n"
    "v_mfma_f32_16x16x32_bf16 v[80:83], v[56:59], v[128:131], v[80:83]\n"
    "v_mfma_f32_16x16x32_bf16 v[84:87], v[56:59], v[192:195], v[84:87]\n"
    "ds_read_b128 v[48:51], v7 offset:4352\n"
    "s_waitcnt lgkmcnt(5)\n"
    "v_mfma_f32_16x16x32_bf16 v[80:83], v[60:63], v[132:135], v[80:83]\n"
    "v_mfma_f32_16x16x32_bf16 v[84:87], v[60:63], v[196:199], v[84:87]\n"
    "ds_read_b128 v[52:55], v7 offset:6400\n"
    "s_waitcnt lgkmcnt(5)\n"
    "v_mfma_f32_16x16x32_bf16 v[80:83], v[64:67], v[136:139], v[80:83]\n"
    "v_mfma_f32_16x16x32_bf16 v[84:87], v[64:67], v[200:203], v[84:87]\n"
    "ds_read_b128 v[56:59], v7 offset:8448\n"
    "s_waitcnt lgkmcnt(5)\n"
    "v_mfma_f32_16x16x32_bf16 v[80:83], v[68:71], v[140:143], v[80:83]\n"
    "v_mfma_f32_16x16x32_bf16 v[84:87], v[68:71], v[204:207], v[84:87]\n"
    "ds_read_b128 v[60:63], v7 offset:10496\n"
    "s_waitcnt lgkmcnt(5)\n"
    "v_mfma_f32_16x16x32_bf16 v[88:91], v[40:43], v[112:115], 0\n"
    "v_mfma_f32_16x16x32_bf16 v[92:95], v[40:43], v[176:179], 0\n"
    "ds_read_b128 v[64:67], v7 offset:12544\n"
    "s_waitcnt lgkmcnt(5)\n"
    "v_mfma_f32_16x16x32_bf16 v[88:91], v[44:47], v[116:119], v[88:91]\n"
    "v_mfma_f32_16x16x32_bf16 v[92:95], v[44:47], v[180:183], v[92:95]\n"
    "ds_read_b128 v[68:71], v7 offset:14592\n"
    "s_waitcnt lgkmcnt(5)\n"
    "v_mfma_f32_16x16x32_bf16 v[88:91], v[48:51], v[120:123], v[88:91]\n"
    "v_mfma_f32_16x16x32_bf16 v[92:95], v[48:51], v[184:187], v[92:95]\n"
    "s_waitcnt lgkmcnt(4)\n"
    "v_mfma_f32_16x16x32_bf16 v[88:91], v[52:55], v[124:127], v[88:91]\n"
    "v_mfma_f32_16x16x32_bf16 v[92:95], v[52:55], v[188:191], v[92:95]\n"
    "s_waitcnt lgkmcnt(3)\n"
    "v_mfma_f32_16x16x32_bf16 v[88:91], v[56:59], v[128:131], v[88:91]\n"
    "v_mfma_f32_16x16x32_bf16 v[92:95], v[56:59], v[192:195], v[92:95]\n"
    "s_waitcnt lgkmcnt(2)\n"
    "v_mfma_f32_16x16x32_bf16 v[88:91], v[60:63], v[132:135], v[88:91]\n"
    "v_mfma_f32_16x16x32_bf16 v[92:95], v[60:63], v[196:199], v[92:95]\n"
    "s_waitcnt lgkmcnt(1)\n"
    "v_mfma_f32_16x16x32_bf16 v[88:91], v[64:67], v[136:139], v[88:91]\n"
    "v_mfma_f32_16x16x32_bf16 v[92:95], v[64:67], v[200:203], v[92:95]\n"
    "s_waitcnt lgkmcnt(0)\n"
    "v_mfma_f32_16x16x32_bf16 v[88:91], v[68:71], v[140:143], v[88:91]\n"
    "v_mfma_f32_16x16x32_bf16 v[92:95], v[68:71], v[204:207], v[92:95]\n"
    "s_waitcnt vmcnt(0)\n"
    "ds_add_u32 v245, v244 offset:68\n"
    ".LfL83:\n"
    "ds_read_b32 v250, v243 offset:68\n"
    "s_add_u32 s49, s49, 1\n"
    "s_cmp_lt_u32 s49, 0x100000\n"
    "s_cselect_b32 s50, 4, 0\n"
    "s_waitcnt lgkmcnt(0)\n"
    "v_cmp_gt_u32 vcc, s50, v250\n"
    "s_cbranch_vccnz .LfL83\n"
    "ds_read_b128 v[40:43], v7 offset:16384\n"
    "ds_read_b128 v[44:47], v7 offset:18432\n"
    "ds_read_b128 v[48:51], v7 offset:20480\n"
    "ds_read_b128 v[52:55], v7 offset:22528\n"
    "ds_read_b128 v[56:59], v7 offset:24576\n"
    "ds_read_b128 v[60:63], v7 offset:26624\n"
    "s_waitcnt lgkmcnt(5)\n"
    "v_mfma_f32_16x16x32_bf16 v[80:83], v[40:43], v[144:147], v[80:83]\n"
    "v_mfma_f32_16x16x32_bf16 v[84:87], v[40:43], v[208:211], v[84:87]\n"
    "ds_read_b128 v[64:67], v7 offset:28672\n"
    "s_waitcnt lgkmcnt(5)\n"
    "v_mfma_f32_16x16x32_bf16 v[80:83], v[44:47], v[148:151], v[80:83]\n"
    "v_mfma_f32_16x16x32_bf16 v[84:87], v[44:47], v[212:215], v[84:87]\n"
    "ds_read_b128 v[68:71], v7 offset:30720\n"
    "s_waitcnt lgkmcnt(5)\n"
    "v_mfma_f32_16x16x32_bf16 v[80:83], v[48:51], v[152:155], v[80:83]\n"
    "v_mfma_f32_16x16x32_bf16 v[84:87], v[48:51], v[216:219], v[84:87]\n"
    "ds_read_b128 v[40:43], v7 offset:16640\n"
    "s_waitcnt lgkmcnt(5)\n"
    "v_mfma_f32_16x16x32_bf16 v[80:83], v[52:55], v[156:159], v[80:83]\n"
    "v_mfma_f32_16x16x32_bf16 v[84:87], v[52:55], v[220:223], v[84:87]\n"
    "ds_read_b128 v[44:47], v7 offset:18688\n"
    "s_waitcnt lgkmcnt(5)\n"
    "v_mfma_f32_16x16x32_bf16 v[80:83], v[56:59], v[160:163], v[80:83]\n"
    "v_mfma_f32_16x16x32_bf16 v[84:87], v[56:59], v[224:227], v[84:87]\n"
    "ds_read_b128 v[48:51], v7 offset:20736\n"
    "s_waitcnt lgkmcnt(5)\n"
    "v_mfma_f32_16x16x32_bf16 v[80:83], v[60:63], v[164:167], v[80:83]\n"
    "v_mfma_f32_16x16x32_bf16 v[84:87], v[60:63], v[228:231], v[84:87]\n"
    "ds_read_b128 v[52:55], v7 offset:22784\n"
    "s_waitcnt lgkmcnt(5)\n"
    "v_mfma_f32_16x16x32_bf16 v[80:83], v[64:67], v[168:171], v[80:83]\n"
    "v_mfma_f32_16x16x32_bf16 v[84:87], v[64:67], v[232:235], v[84:87]\n"
    "ds_read_b128 v[56:59], v7 offset:24832\n"
    "s_waitcnt lgkmcnt(5)\n"
    "v_mfma_f32_16x16x32_bf16 v[80:83], v[68:71], v[172:175], v[80:83]\n"
    "v_mfma_f32_16x16x32_bf16 v[84:87], v[68:71], v[236:239], v[84:87]\n"
    "ds_read_b128 v[60:63], v7 offset:26880\n"
    "s_waitcnt lgkmcnt(5)\n"
    "v_mfma_f32_16x16x32_bf16 v[88:91], v[40:43], v[144:147], v[88:91]\n"
    "v_mfma_f32_16x16x32_bf16 v[92:95], v[40:43], v[208:211], v[92:95]\n"
    "ds_read_b128 v[64:67], v7 offset:28928\n"
    "s_waitcnt lgkmcnt(5)\n"
    "v_mfma_f32_16x16x32_bf16 v[88:91], v[44:47], v[148:151], v[88:91]\n"
    "v_mfma_f32_16x16x32_bf16 v[92:95], v[44:47], v[212:215], v[92:95]\n"
    "ds_read_b128 v[68:71], v7 offset:30976\n"
    "s_waitcnt lgkmcnt(5)\n"
    "v_mfma_f32_16x16x32_bf16 v[88:91], v[48:51], v[152:155], v[88:91]\n"
    "v_mfma_f32_16x16x32_bf16 v[92:95], v[48:51], v[216:219], v[92:95]\n"
    "s_waitcnt lgkmcnt(4)\n"
    "v_mfma_f32_16x16x32_bf16 v[88:91], v[52:55], v[156:159], v[88:91]\n"
    "v_mfma_f32_16x16x32_bf16 v[92:95], v[52:55], v[220:223], v[92:95]\n"
    "v_cvt_pk_bf16_f32 v32, v80, v84\n"
    "buffer_store_dword v32, v8, s[20:23], s60 offen offset:0 sc1\n"
    "s_waitcnt lgkmcnt(3)\n"
    "v_mfma_f32_16x16x32_bf16 v[88:91], v[56:59], v[160:163], v[88:91]\n"
    "v_mfma_f32_16x16x32_bf16 v[92:95], v[56:59], v[224:227], v[92:95]\n"
    "v_cvt_pk_bf16_f32 v33, v81, v85\n"
    "buffer_store_dword v33, v8, s[20:23], s60 offen offset:1792 sc1\n"
    "s_waitcnt lgkmcnt(2)\n"
    "v_mfma_f32_16x16x32_bf16 v[88:91], v[60:63], v[164:167], v[88:91]\n"
    "v_mfma_f32_16x16x32_bf16 v[92:95], v[60:63], v[228:231], v[92:95]\n"
    "v_cvt_pk_bf16_f32 v34, v82, v86\n"
    "buffer_store_dword v34, v8, s[20:23], s61 offen offset:0 sc1\n"
    "s_waitcnt lgkmcnt(1)\n"
    "v_mfma_f32_16x16x32_bf16 v[88:91], v[64:67], v[168:171], v[88:91]\n"
    "v_mfma_f32_16x16x32_bf16 v[92:95], v[64:67], v[232:235], v[92:95]\n"
    "v_cvt_pk_bf16_f32 v35, v83, v87\n"
    "buffer_store_dword v35, v8, s[20:23], s61 offen offset:1792 sc1\n"
    "s_waitcnt lgkmcnt(0)\n"
    "v_mfma_f32_16x16x32_bf16 v[88:91], v[68:71], v[172:175], v[88:91]\n"
    "v_mfma_f32_16x16x32_bf16 v[92:95], v[68:71], v[236:239], v[92:95]\n"
    "s_nop 7\n"
    "v_cvt_pk_bf16_f32 v36, v88, v92\n"
    "buffer_store_dword v36, v8, s[20:23], s62 offen offset:0 sc1\n"
    "v_cvt_pk_bf16_f32 v37, v89, v93\n"
    "buffer_store_dword v37, v8, s[20:23], s62 offen offset:1792 sc1\n"
    "v_cvt_pk_bf16_f32 v38, v90, v94\n"
    "buffer_store_dword v38, v8, s[20:23], s63 offen offset:0 sc1\n"
    "v_cvt_pk_bf16_f32 v39, v91, v95\n"
    "buffer_store_dword v39, v8, s[20:23], s63 offen offset:1792 sc1\n"
    "v_cmp_u_f32 vcc, v80, v81\n"
    "v_cmp_u_f32 s[58:59], v82, v83\n"
    "v_cmp_u_f32 s[52:53], v88, v89\n"
    "s_or_b64 s[58:59], s[58:59], vcc\n"
    "v_cmp_u_f32 vcc, v90, v91\n"
    "s_or_b64 s[58:59], s[58:59], s[52:53]\n"
    "s_or_b64 vcc, vcc, s[58:59]\n"
    "s_and_b64 vcc, vcc, s[78:79]\n"
    "s_cbranch_scc1 .LfOR\n"
    ".LfEND:\n"
    "s_endpgm\n"
    ".LfS74:\n"
    "s_mov_b64 exec, vcc\n"
    "s_sleep 4\n"
    "buffer_load_dwordx2 v[12:13], v0, s[24:27], 0 offen sc1\n"
    "s_add_u32 s49, s49, 1\n"
    "s_cmp_lt_u32 s49, 0x100000\n"
    "s_cselect_b64 s[52:53], -1, 0\n"
    "s_waitcnt vmcnt(0)\n"
    "v_cmp_ne_u64 vcc, s[44:45], v[12:13]\n"
    "s_nop 0\n"
    "s_and_b64 vcc, vcc, s[52:53]\n"
    "s_cbranch_scc1 .LfS74\n"
    "s_mov_b64 exec, -1\n"
    "s_branch .LfB75\n"
    ".LfH7:\n"
    "s_lshl2_add_u32 s58, s41, s47\n"
    "s_lshl3_add_u32 s58, s58, s42\n"
    "s_lshl_b32 s58, s58, 11\n"
    "s_add_u32 s58, s58, 0x100000\n"
    "v_mov_b32 v16, -1\n"
    "v_mov_b32 v17, -1\n"
    "v_mov_b32 v18, -1\n"
    "v_mov_b32 v19, -1\n"
    "buffer_store_dwordx4 v[16:19], v242, s[24:27], s58 offen offset:0 sc1\n"
    "buffer_store_dwordx4 v[16:19], v242, s[24:27], s58 offen offset:1024 sc1\n"
    "ds_add_u32 v245, v244 offset:52\n"
    ".LfL84:\n"
    "s_sleep 2\n"
    "ds_read_b32 v250, v243 offset:52\n"
    "s_add_u32 s49, s49, 1\n"
    "s_cmp_lt_u32 s49, 0x100000\n"
    "s_cselect_b32 s50, 4, 0\n"
    "s_waitcnt lgkmcnt(0)\n"
    "v_cmp_gt_u32 vcc, s50, v250\n"
    "s_cbranch_vccnz .LfL84\n"
    "s_setprio 0\n"
    "s_xor_b32 s66, s65, 0\n"
    "s_sleep 3\n"
    "buffer_load_dwordx4 v[112:115], v3, s[24:27], s66 offen offset:0 sc1\n"
    "buffer_load_dwordx4 v[116:119], v3, s[24:27], s66 offen offset:1024 sc1\n"
    "s_xor_b32 s66, s65, 8192\n"
    "s_sleep 3\n"
    "buffer_load_dwordx4 v[144:147], v3, s[24:27], s66 offen offset:0 sc1\n"
    "buffer_load_dwordx4 v[148:151], v3, s[24:27], s66 offen offset:1024 sc1\n"
    "s_xor_b32 s66, s65, 16384\n"
    "s_sleep 3\n"
    "buffer_load_dwordx4 v[176:179], v3, s[24:27], s66 offen offset:0 sc1\n"
    "buffer_load_dwordx4 v[180:183], v3, s[24:27], s66 offen offset:1024 sc1\n"
    "s_xor_b32 s66, s65, 24576\n"
    "s_sleep 3\n"
    "buffer_load_dwordx4 v[208:211], v3, s[24:27], s66 offen offset:0 sc1\n"
    "buffer_load_dwordx4 v[212:215], v3, s[24:27], s66 offen offset:1024 sc1\n"
    "s_xor_b32 s66, s65, 2048\n"
    "s_sleep 3\n"
    "buffer_load_dwordx4 v[120:123], v3, s[24:27], s66 offen offset:0 sc1\n"
    "buffer_load_dwordx4 v[124:127], v3, s[24:27], s66 offen offset:1024 sc1\n"
    "s_xor_b32 s66, s65, 10240\n"
    "s_sleep 3\n"
    "buffer_load_dwordx4 v[152:155], v3, s[24:27], s66 offen offset:0 sc1\n"
    "buffer_load_dwordx4 v[156:159], v3, s[24:27], s66 offen offset:1024 sc1\n"
    "s_xor_b32 s66, s65, 18432\n"
    "s_sleep 3\n"
    "buffer_load_dwordx4 v[184:187], v3, s[24:27], s66 offen offset:0 sc1\n"
    "buffer_load_dwordx4 v[188:191], v3, s[24:27], s66 offen offset:1024 sc1\n"
    "s_xor_b32 s66, s65, 26624\n"
    "s_sleep 3\n"
    "buffer_load_dwordx4 v[216:219], v3, s[24:27], s66 offen offset:0 sc1\n"
    "buffer_load_dwordx4 v[220:223], v3, s[24:27], s66 offen offset:1024 sc1\n"
    "s_xor_b32 s66, s65, 4096\n"
    "s_sleep 3\n"
    "buffer_load_dwordx4 v[128:131], v3, s[24:27], s66 offen offset:0 sc1\n"
    "buffer_load_dwordx4 v[132:135], v3, s[24:27], s66 offen offset:1024 sc1\n"
    "s_xor_b32 s66, s65, 12288\n"
    "s_sleep 3\n"
    "buffer_load_dwordx4 v[160:163], v3, s[24:27], s66 offen offset:0 sc1\n"
    "buffer_load_dwordx4 v[164:167], v3, s[24:27], s66 offen offset:1024 sc1\n"
    "s_xor_b32 s66, s65, 20480\n"
    "s_sleep 3\n"
    "buffer_load_dwordx4 v[192:195], v3, s[24:27], s66 offen offset:0 sc1\n"
    "buffer_load_dwordx4 v[196:199], v3, s[24:27], s66 offen offset:1024 sc1\n"
    "s_xor_b32 s66, s65, 28672\n"
    "s_sleep 3\n"
    "buffer_load_dwordx4 v[224:227], v3, s[24:27], s66 offen offset:0 sc1\n"
    "buffer_load_dwordx4 v[228:231], v3, s[24:27], s66 offen offset:1024 sc1\n"
    "s_xor_b32 s66, s65, 6144\n"
    "s_sleep 3\n"
    "buffer_load_dwordx4 v[136:139], v3, s[24:27], s66 offen offset:0 sc1\n"
    "buffer_load_dwordx4 v[140:143], v3, s[24:27], s66 offen offset:1024 sc1\n"
    "s_xor_b32 s66, s65, 14336\n"
    "s_sleep 3\n"
    "buffer_load_dwordx4 v[168:171], v3, s[24:27], s66 offen offset:0 sc1\n"
    "buffer_load_dwordx4 v[172:175], v3, s[24:27], s66 offen offset:1024 sc1\n"
    "s_xor_b32 s66, s65, 22528\n"
    "s_sleep 3\n"
    "buffer_load_dwordx4 v[200:203], v3, s[24:27], s66 offen offset:0 sc1\n"
    "buffer_load_dwordx4 v[204:207], v3, s[24:27], s66 offen offset:1024 sc1\n"
    "s_xor_b32 s66, s65, 30720\n"
    "s_sleep 3\n"
    "buffer_load_dwordx4 v[232:235], v3, s[24:27], s66 offen offset:0 sc1\n"
    "buffer_load_dwordx4 v[236:239], v3, s[24:27], s66 offen offset:1024 sc1\n"
    "s_setprio 2\n"
    "s_branch .LfHZ\n"
    ".LfH0S:\n"
    "s_mov_b64 exec, vcc\n"
    "buffer_load_dwordx3 v[12:14], v9, s[24:27], 0 offen sc1\n"
    "s_add_u32 s49, s49, 1\n"
    "s_cmp_lt_u32 s49, 0x100000\n"
    "s_cselect_b64 s[52:53], -1, 0\n"
    "s_waitcnt vmcnt(0)\n"
    "v_cmp_ne_u64 vcc, s[44:45], v[12:13]\n"
    "s_nop 0\n"
    "s_and_b64 vcc, vcc, s[52:53]\n"
    "s_cbranch_scc1 .LfH0S\n"
    "s_mov_b64 exec, -1\n"
    "s_branch .LfH0B\n"
    ".LfOR:\n"
    "ds_add_u32 v245, v244 offset:40\n"
    "buffer_load_dwordx4 v[40:43], v242, s[24:27], s64 offen offset:0 sc1\n"
    "buffer_load_dwordx4 v[44:47], v242, s[24:27], s64 offen offset:1024 sc1\n"
    "buffer_load_dwordx4 v[48:51], v242, s[24:27], s64 offen offset:2048 sc1\n"
    "buffer_load_dwordx4 v[52:55], v242, s[24:27], s64 offen offset:3072 sc1\n"
    "buffer_load_dwordx4 v[56:59], v242, s[24:27], s65 offen offset:0 sc1\n"
    "buffer_load_dwordx4 v[60:63], v242, s[24:27], s65 offen offset:1024 sc1\n"
    "buffer_load_dwordx4 v[64:67], v242, s[24:27], s65 offen offset:2048 sc1\n"
    "buffer_load_dwordx4 v[68:71], v242, s[24:27], s65 offen offset:3072 sc1\n"
    "s_waitcnt vmcnt(0)\n"
    "v_max3_u32 v250, v40, v41, v42\n"
    "v_max3_u32 v250, v250, v43, v44\n"
    "v_max3_u32 v250, v250, v45, v46\n"
    "v_max3_u32 v250, v250, v47, v48\n"
    "v_max3_u32 v250, v250, v49, v50\n"
    "v_max3_u32 v250, v250, v51, v52\n"
    "v_max3_u32 v250, v250, v53, v54\n"
    "v_max3_u32 v250, v250, v55, v56\n"
    "v_max3_u32 v250, v250, v57, v58\n"
    "v_max3_u32 v250, v250, v59, v60\n"
    "v_max3_u32 v250, v250, v61, v62\n"
    "v_max3_u32 v250, v250, v63, v64\n"
    "v_max3_u32 v250, v250, v65, v66\n"
    "v_max3_u32 v250, v250, v67, v68\n"
    "v_max3_u32 v250, v250, v69, v70\n"
    "v_max_u32 v250, v250, v71\n"
    "v_cmp_eq_u32 vcc, -1, v250\n"
    "s_cbranch_vccnz .LfS85\n"
    ".LfB86:\n"
    ".LfL87:\n"
    "ds_read_b32 v250, v243 offset:40\n"
    "s_add_u32 s49, s49, 1\n"
    "s_cmp_lt_u32 s49, 0x100000\n"
    "s_cselect_b32 s50, 8, 0\n"
    "s_waitcnt lgkmcnt(0)\n"
    "v_cmp_gt_u32 vcc, s50, v250\n"
    "s_cbranch_vccnz .LfL87\n"
    "v_add_u32 v32, s67, v242\n"
    "ds_write_b128 v32, v[40:43] offset:0\n"
    "ds_write_b128 v32, v[44:47] offset:1024\n"
    "ds_write_b128 v32, v[48:51] offset:2048\n"
    "ds_write_b128 v32, v[52:55] offset:3072\n"
    "ds_write_b128 v32, v[56:59] offset:16384\n"
    "ds_write_b128 v32, v[60:63] offset:17408\n"
    "ds_write_b128 v32, v[64:67] offset:18432\n"
    "ds_write_b128 v32, v[68:71] offset:19456\n"
    "ds_add_u32 v245, v244 offset:40\n"
    ".LfL88:\n"
    "ds_read_b32 v250, v243 offset:40\n"
    "s_add_u32 s49, s49, 1\n"
    "s_cmp_lt_u32 s49, 0x100000\n"
    "s_cselect_b32 s50, 12, 0\n"
    "s_waitcnt lgkmcnt(0)\n"
    "v_cmp_gt_u32 vcc, s50, v250\n"
    "s_cbranch_vccnz .LfL88\n"
    "s_mov_b64 s[78:79], 0\n"
    "s_branch .LfOP\n"
    ".LfS85:\n"
    "s_sleep 4\n"
    "buffer_load_dwordx4 v[40:43], v242, s[24:27], s64 offen offset:0 sc1\n"
    "buffer_load_dwordx4 v[44:47], v242, s[24:27], s64 offen offset:1024 sc1\n"
    "buffer_load_dwordx4 v[48:51], v242, s[24:27], s64 offen offset:2048 sc1\n"
    "buffer_load_dwordx4 v[52:55], v242, s[24:27], s64 offen offset:3072 sc1\n"
    "buffer_load_dwordx4 v[56:59], v242, s[24:27], s65 offen offset:0 sc1\n"
    "buffer_load_dwordx4 v[60:63], v242, s[24:27], s65 offen offset:1024 sc1\n"
    "buffer_load_dwordx4 v[64:67], v242, s[24:27], s65 offen offset:2048 sc1\n"
    "buffer_load_dwordx4 v[68:71], v242, s[24:27], s65 offen offset:3072 sc1\n"
    "s_add_u32 s49, s49, 1\n"
    "s_cmp_lt_u32 s49, 0x100000\n"
    "s_cselect_b64 s[52:53], -1, 0\n"
    "s_waitcnt vmcnt(0)\n"
    "v_max3_u32 v250, v40, v41, v42\n"
    "v_max3_u32 v250, v250, v43, v44\n"
    "v_max3_u32 v250, v250, v45, v46\n"
    "v_max3_u32 v250, v250, v47, v48\n"
    "v_max3_u32 v250, v250, v49, v50\n"
    "v_max3_u32 v250, v250, v51, v52\n"
    "v_max3_u32 v250, v250, v53, v54\n"
    "v_max3_u32 v250, v250, v55, v56\n"
    "v_max3_u32 v250, v250, v57, v58\n"
    "v_max3_u32 v250, v250, v59, v60\n"
    "v_max3_u32 v250, v250, v61, v62\n"
    "v_max3_u32 v250, v250, v63, v64\n"
    "v_max3_u32 v250, v250, v65, v66\n"
    "v_max3_u32 v250, v250, v67, v68\n"
    "v_max3_u32 v250, v250, v69, v70\n"
    "v_max_u32 v250, v250, v71\n"
    "v_cmp_eq_u32 vcc, -1, v250\n"
    "s_nop 0\n"
    "s_and_b64 vcc, vcc, s[52:53]\n"
    "s_cbranch_scc1 .LfS85\n"
    "s_branch .LfB86\n"
    ".LfWO:\n"
    "v_lshrrev_b32 v28, 3, v241\n"
    "v_and_b32 v29, 7, v241\n"
    "v_mul_u32_u24 v28, 896, v28\n"
    "v_lshl_add_u32 v24, v29, 4, v28\n"
    "v_add_u32 v25, 6144, v24\n"
    "v_add_u32 v26, 12288, v24\n"
    "v_add_u32 v27, 18432, v24\n"
    "s_lshr_b32 s58, s41, 1\n"
    "s_and_b32 s59, s41, 1\n"
    "s_lshr_b32 s60, s47, 1\n"
    "s_lshl1_add_u32 s59, s59, s60\n"
    "s_and_b32 s60, s47, 1\n"
    "s_mul_i32 s61, s58, 25088\n"
    "s_mul_i32 s62, s59, 256\n"
    "s_add_u32 s61, s61, s62\n"
    "s_lshl_b32 s62, s60, 7\n"
    "s_add_u32 s61, s61, s62\n"
    "s_lshl_b32 s62, s47, 12\n"
    "s_lshl1_add_u32 s59, s59, s60\n"
    "s_lshl_b32 s59, s59, 16\n"
    "s_and_b32 s60, s58, 1\n"
    "s_lshr_b32 s58, s58, 1\n"
    "s_lshl3_add_u32 s58, s60, s58\n"
    "s_lshl_b32 s58, s58, 10\n"
    "s_add_u32 s58, s58, s59\n"
    "s_add_u32 s65, s58, 0x80000\n"
    "v_and_b32 v20, 15, v241\n"
    "v_lshrrev_b32 v21, 4, v241\n"
    "v_mov_b32 v244, 1\n"
    "v_lshl_add_u32 v245, v241, 20, v243\n"
    "s_mov_b32 s49, 0\n"
    "s_bfm_b64 s[80:81], 32, 0\n"
    "s_bfm_b64 s[92:93], 8, 0\n"
    "s_mov_b32 s22, 0x380000\n"
    "s_mov_b32 s23, 0x20000\n"
    "s_lshr_b32 s58, s42, 1\n"
    "s_lshl2_add_u32 s58, s58, s47\n"
    "v_mov_b32 v22, -1\n"
    "s_cmp_gt_u32 s58, 13\n"
    "s_cselect_b32 s59, s54, 0\n"
    "s_lshl_b32 s58, s47, 11\n"
    "s_add_u32 s58, s58, 0xa800\n"
    "v_and_b32 v22, s59, v22\n"
    "v_add_u32 v6, s58, v242\n"
    "v_or_b32 v3, v22, v242\n"
    "v_lshlrev_b32 v23, 4, v20\n"
    "v_lshl_add_u32 v23, v21, 9, v23\n"
    "v_add_u32 v7, 0x1a800, v23\n"
    "v_mul_u32_u24 v23, 7168, v21\n"
    "v_lshl_add_u32 v23, v20, 2, v23\n"
    "v_or_b32 v8, v22, v23\n"
    "s_and_b32 s83, s41, 3\n"
    "s_xor_b32 s58, s83, s47\n"
    "s_lshl2_add_u32 s58, s41, s58\n"
    "s_lshl_b32 s58, s58, 8\n"
    "s_add_u32 s58, s58, 0x20000\n"
    "v_lshl_add_u32 v9, v241, 7, s58\n"
    "s_lshl_b32 s58, s47, 6\n"
    "s_add_u32 s59, s58, s42\n"
    "v_lshl_add_u32 v22, v241, 3, s59\n"
    "s_lshr_b32 s59, s42, 2\n"
    "s_lshl3_add_u32 s58, s59, s58\n"
    "s_sub_u32 s58, s58, 121\n"
    "v_lshl_add_u32 v23, v241, 4, s58\n"
    "s_mov_b32 s58, 0x10000\n"
    "v_cndmask_b32 v22, v23, v22, s[92:93]\n"
    "s_bfm_b64 s[94:95], 12, 0\n"
    "v_lshl_add_u32 v0, v22, 7, s58\n"
    "s_mov_b32 s18, 0x62000\n"
    "s_mov_b32 s19, 0x20000\n"
    "s_waitcnt lgkmcnt(0)\n"
    "s_add_u32 s44, s44, 1\n"
    "s_addc_u32 s45, s45, 0\n"
    "ds_add_u32 v245, v244 offset:28\n"
    ".LfL89:\n"
    "ds_read_b32 v250, v243 offset:0\n"
    "s_add_u32 s49, s49, 1\n"
    "s_cmp_lt_u32 s49, 0x100000\n"
    "s_cselect_b32 s50, 1, 0\n"
    "s_waitcnt lgkmcnt(0)\n"
    "v_cmp_gt_u32 vcc, s50, v250\n"
    "s_cbranch_vccnz .LfL89\n"
    ".LfL90:\n"
    "ds_read_b32 v250, v243 offset:172\n"
    "s_add_u32 s49, s49, 1\n"
    "s_cmp_lt_u32 s49, 0x100000\n"
    "s_cselect_b32 s50, 0, -1\n"
    "s_waitcnt lgkmcnt(0)\n"
    "v_cmp_lt_u32 vcc, s50, v250\n"
    "s_cbranch_vccnz .LfL90\n"
    "s_setprio 2\n"
    "s_mov_b32 m0, s62\n"
    "s_nop 0\n"
    "buffer_load_dwordx4 v24, s[16:19], s61 offen offset:0 lds\n"
    "buffer_load_dwordx4 v25, s[16:19], s61 offen offset:1024 lds\n"
    "buffer_load_dwordx4 v26, s[16:19], s61 offen offset:2048 lds\n"
    "buffer_load_dwordx4 v27, s[16:19], s61 offen offset:3072 lds\n"
    "v_lshrrev_b32 v28, 4, v241\n"
    "v_and_b32 v29, 15, v241\n"
    "v_mul_u32_u24 v28, 896, v28\n"
    "v_lshl_add_u32 v28, v29, 2, v28\n"
    "v_add_u32 v28, s62, v28\n"
    "s_mov_b32 s63, 0x05040100\n"
    "s_mov_b32 s64, 0x07060302\n"
    "s_waitcnt vmcnt(0)\n"
    "ds_read2_b32 v[48:49], v28 offset0:0 offset1:16\n"
    "ds_read2_b32 v[50:51], v28 offset0:32 offset1:48\n"
    "ds_read2_b32 v[52:53], v28 offset0:64 offset1:80\n"
    "ds_read2_b32 v[54:55], v28 offset0:96 offset1:112\n"
    "ds_read2_b32 v[56:57], v28 offset0:128 offset1:144\n"
    "ds_read2_b32 v[58:59], v28 offset0:160 offset1:176\n"
    "ds_read2_b32 v[60:61], v28 offset0:192 offset1:208\n"
    "s_waitcnt lgkmcnt(0)\n"
    "v_perm_b32 v32, v50, v48, s63\n"
    "v_perm_b32 v33, v54, v52, s63\n"
    "v_perm_b32 v34, v58, v56, s63\n"
    "v_and_b32 v35, 0xffff, v60\n"
    "buffer_store_dwordx4 v[32:35], v242, s[24:27], s65 offen sc1\n"
    "s_add_u32 s65, s65, 16384\n"
    "v_perm_b32 v36, v50, v48, s64\n"
    "v_perm_b32 v37, v54, v52, s64\n"
    "v_perm_b32 v38, v58, v56, s64\n"
    "v_lshrrev_b32 v39, 16, v60\n"
    "buffer_store_dwordx4 v[36:39], v242, s[24:27], s65 offen sc1\n"
    "s_add_u32 s65, s65, 16384\n"
    "v_perm_b32 v40, v51, v49, s63\n"
    "v_perm_b32 v41, v55, v53, s63\n"
    "v_perm_b32 v42, v59, v57, s63\n"
    "v_and_b32 v43, 0xffff, v61\n"
    "buffer_store_dwordx4 v[40:43], v242, s[24:27], s65 offen sc1\n"
    "s_add_u32 s65, s65, 16384\n"
    "v_perm_b32 v44, v51, v49, s64\n"
    "v_perm_b32 v45, v55, v53, s64\n"
    "v_perm_b32 v46, v59, v57, s64\n"
    "v_lshrrev_b32 v47, 16, v61\n"
    "buffer_store_dwordx4 v[44:47], v242, s[24:27], s65 offen sc1\n"
    "s_add_u32 s65, s65, 16384\n"
    "s_lshl_b32 s58, s43, 7\n"
    "s_add_u32 s58, s58, 0x10000\n"
    "s_waitcnt vmcnt(0)\n"
    "ds_add_rtn_u32 v250, v245, v244 offset:24\n"
    "s_waitcnt lgkmcnt(0)\n"
    "v_readfirstlane_b32 s59, v250\n"
    "s_nop 0\n"
    "s_cmp_eq_u32 s59, 3\n"
    "s_cbranch_scc0 .LfL91\n"
    "v_mov_b32 v250, s44\n"
    "v_mov_b32 v251, s45\n"
    "s_mov_b64 exec, 1\n"
    "buffer_store_dwordx2 v[250:251], off, s[24:27], s58 sc1\n"
    "s_mov_b64 exec, -1\n"
    ".LfL91:\n"
    "s_sleep 16\n"
    ".LfL92:\n"
    "s_sleep 3\n"
    "ds_read_b32 v250, v243 offset:48\n"
    "s_add_u32 s49, s49, 1\n"
    "s_cmp_lt_u32 s49, 0x100000\n"
    "s_cselect_b32 s50, 4, 0\n"
    "s_waitcnt lgkmcnt(0)\n"
    "v_cmp_gt_u32 vcc, s50, v250\n"
    "s_cbranch_vccnz .LfL92\n"
    "s_lshl2_add_u32 s58, s41, s47\n"
    "s_mul_i32 s58, s58, 28672\n"
    "s_mov_b32 m0, 0x27e00\n"
    "s_mov_b64 exec, 1\n"
    "buffer_load_dword off, s[20:23], s58 lds\n"
    "s_mov_b64 exec, s[94:95]\n"
    "buffer_load_dwordx2 v[12:13], v0, s[24:27], 0 offen sc1\n"
    "s_mov_b64 exec, -1\n"
    "s_branch .LfH\n"
    ".Lfz_0:\n"
    ".Lfend:\n"
    ".LfFB0:\n"
    "s_lshr_b32 s58, s47, 1\n"
    "v_mov_b32 v240, 0xdddddddd\n"
    "s_lshl_b32 s58, s58, 12\n"
    "v_add_u32 v48, s58, v242\n"
    "ds_read_b128 v[60:63], v48 offset:34816\n"
    "ds_read_b128 v[64:67], v48 offset:35840\n"
    "v_mov_b32 v48, 0xff800000\n"
    "v_mov_b32 v80, 0\n"
    "v_mov_b32 v81, 0\n"
    "v_mov_b32 v82, 0\n"
    "v_mov_b32 v83, 0\n"
    "v_mov_b32 v84, 0\n"
    "v_mov_b32 v85, 0\n"
    "v_mov_b32 v86, 0\n"
    "v_mov_b32 v87, 0\n"
    "v_mov_b32 v88, 0\n"
    "v_mov_b32 v89, 0\n"
    "v_mov_b32 v90, 0\n"
    "v_mov_b32 v91, 0\n"
    "v_mov_b32 v92, 0\n"
    "v_mov_b32 v93, 0\n"
    "v_mov_b32 v94, 0\n"
    "v_mov_b32 v95, 0\n"
    "s_mov_b32 s58, 0x40000\n"
    ".LfFBa0:\n"
    "buffer_load_dwordx4 v[40:43], v242, s[24:27], s58 offen sc1\n"
    "buffer_load_dwordx4 v[44:47], v242, s[24:27], s58 offen offset:1024 sc1\n"
    "buffer_load_dwordx4 v[52:55], v242, s[24:27], s58 offen offset:2048 sc1\n"
    "buffer_load_dwordx4 v[56:59], v242, s[24:27], s58 offen offset:3072 sc1\n"
    "s_add_u32 s58, s58, 4096\n"
    "s_waitcnt vmcnt(0) lgkmcnt(0)\n"
    "v_mfma_f32_32x32x16_bf16 v[96:111], v[40:43], v[60:63], 0\n"
    "v_mfma_f32_32x32x16_bf16 v[96:111], v[44:47], v[64:67], v[96:111]\n"
    "s_nop 11\n"
    "v_max3_f32 v48, v48, v96, v97\n"
    "v_max3_f32 v48, v48, v98, v99\n"
    "v_max3_f32 v48, v48, v100, v101\n"
    "v_max3_f32 v48, v48, v102, v103\n"
    "v_max3_f32 v48, v48, v104, v105\n"
    "v_max3_f32 v48, v48, v106, v107\n"
    "v_max3_f32 v48, v48, v108, v109\n"
    "v_max3_f32 v48, v48, v110, v111\n"
    "s_cmp_lt_u32 s58, 0x80000\n"
    "s_cbranch_scc1 .LfFBa0\n"
    "v_mov_b32 v49, v48\n"
    "s_nop 1\n"
    "v_permlane32_swap_b32 v48, v49\n"
    "v_max_f32 v48, v48, v49\n"
    "s_mov_b32 s58, 0x40000\n"
    ".LfFBb0:\n"
    "buffer_load_dwordx4 v[40:43], v242, s[24:27], s58 offen sc1\n"
    "buffer_load_dwordx4 v[44:47], v242, s[24:27], s58 offen offset:1024 sc1\n"
    "buffer_load_dwordx4 v[52:55], v242, s[24:27], s58 offen offset:2048 sc1\n"
    "buffer_load_dwordx4 v[56:59], v242, s[24:27], s58 offen offset:3072 sc1\n"
    "s_add_u32 s58, s58, 4096\n"
    "s_waitcnt vmcnt(0) lgkmcnt(0)\n"
    "v_mfma_f32_32x32x16_bf16 v[96:111], v[40:43], v[60:63], 0\n"
    "v_mfma_f32_32x32x16_bf16 v[96:111], v[44:47], v[64:67], v[96:111]\n"
    "s_nop 11\n"
    "v_sub_f32 v96, v96, v48\n"
    "v_sub_f32 v97, v97, v48\n"
    "v_sub_f32 v98, v98, v48\n"
    "v_sub_f32 v99, v99, v48\n"
    "v_sub_f32 v100, v100, v48\n"
    "v_sub_f32 v101, v101, v48\n"
    "v_sub_f32 v102, v102, v48\n"
    "v_sub_f32 v103, v103, v48\n"
    "v_sub_f32 v104, v104, v48\n"
    "v_sub_f32 v105, v105, v48\n"
    "v_sub_f32 v106, v106, v48\n"
    "v_sub_f32 v107, v107, v48\n"
    "v_sub_f32 v108, v108, v48\n"
    "v_sub_f32 v109, v109, v48\n"
    "v_sub_f32 v110, v110, v48\n"
    "v_sub_f32 v111, v111, v48\n"
    "v_exp_f32 v96, v96\n"
    "v_exp_f32 v97, v97\n"
    "v_exp_f32 v98, v98\n"
    "v_exp_f32 v99, v99\n"
    "v_exp_f32 v100, v100\n"
    "v_exp_f32 v101, v101\n"
    "v_exp_f32 v102, v102\n"
    "v_exp_f32 v103, v103\n"
    "v_exp_f32 v104, v104\n"
    "v_exp_f32 v105, v105\n"
    "v_exp_f32 v106, v106\n"
    "v_exp_f32 v107, v107\n"
    "v_exp_f32 v108, v108\n"
    "v_exp_f32 v109, v109\n"
    "v_exp_f32 v110, v110\n"
    "v_exp_f32 v111, v111\n"
    "v_smfmac_f32_32x32x32_bf16 v[80:95], v[52:55], v[96:103], v240\n"
    "s_nop 0\n"
    "v_smfmac_f32_32x32x32_bf16 v[80:95], v[56:59], v[104:111], v240\n"
    "s_cmp_lt_u32 s58, 0x80000\n"
    "s_cbranch_scc1 .LfFBb0\n"
    "s_bitcmp1_b32 s40, 0\n"
    "s_cselect_b64 vcc, -1, 0\n"
    "s_nop 7\n"
    "v_cndmask_b32 v16, v80, v88, vcc\n"
    "v_cndmask_b32 v17, v81, v89, vcc\n"
    "v_cndmask_b32 v18, v82, v90, vcc\n"
    "v_cndmask_b32 v19, v83, v91, vcc\n"
    "v_cndmask_b32 v20, v84, v92, vcc\n"
    "v_cndmask_b32 v21, v85, v93, vcc\n"
    "v_cndmask_b32 v22, v86, v94, vcc\n"
    "v_cndmask_b32 v23, v87, v95, vcc\n"
    "v_mov_b32 v48, 0x7fc00000\n"
    "v_cmp_u_f32 vcc, v16, v16\n"
    "s_nop 1\n"
    "v_cndmask_b32 v16, v16, v48, vcc\n"
    "v_cmp_u_f32 vcc, v17, v17\n"
    "s_nop 1\n"
    "v_cndmask_b32 v17, v17, v48, vcc\n"
    "v_cmp_u_f32 vcc, v18, v18\n"
    "s_nop 1\n"
    "v_cndmask_b32 v18, v18, v48, vcc\n"
    "v_cmp_u_f32 vcc, v19, v19\n"
    "s_nop 1\n"
    "v_cndmask_b32 v19, v19, v48, vcc\n"
    "v_cmp_u_f32 vcc, v20, v20\n"
    "s_nop 1\n"
    "v_cndmask_b32 v20, v20, v48, vcc\n"
    "v_cmp_u_f32 vcc, v21, v21\n"
    "s_nop 1\n"
    "v_cndmask_b32 v21, v21, v48, vcc\n"
    "v_cmp_u_f32 vcc, v22, v22\n"
    "s_nop 1\n"
    "v_cndmask_b32 v22, v22, v48, vcc\n"
    "v_cmp_u_f32 vcc, v23, v23\n"
    "s_nop 1\n"
    "v_cndmask_b32 v23, v23, v48, vcc\n"
    "v_rcp_f32 v34, v23\n"
    "s_branch .LfFBback0\n"
    ".LfFB1:\n"
    "s_lshr_b32 s58, s47, 1\n"
    "v_mov_b32 v240, 0xdddddddd\n"
    "s_lshl_b32 s58, s58, 12\n"
    "v_add_u32 v48, s58, v242\n"
    "ds_read_b128 v[60:63], v48 offset:36864\n"
    "ds_read_b128 v[64:67], v48 offset:37888\n"
    "v_mov_b32 v48, 0xff800000\n"
    "v_mov_b32 v80, 0\n"
    "v_mov_b32 v81, 0\n"
    "v_mov_b32 v82, 0\n"
    "v_mov_b32 v83, 0\n"
    "v_mov_b32 v84, 0\n"
    "v_mov_b32 v85, 0\n"
    "v_mov_b32 v86, 0\n"
    "v_mov_b32 v87, 0\n"
    "v_mov_b32 v88, 0\n"
    "v_mov_b32 v89, 0\n"
    "v_mov_b32 v90, 0\n"
    "v_mov_b32 v91, 0\n"
    "v_mov_b32 v92, 0\n"
    "v_mov_b32 v93, 0\n"
    "v_mov_b32 v94, 0\n"
    "v_mov_b32 v95, 0\n"
    "s_mov_b32 s58, 0x40000\n"
    ".LfFBa1:\n"
    "buffer_load_dwordx4 v[40:43], v242, s[24:27], s58 offen sc1\n"
    "buffer_load_dwordx4 v[44:47], v242, s[24:27], s58 offen offset:1024 sc1\n"
    "buffer_load_dwordx4 v[52:55], v242, s[24:27], s58 offen offset:2048 sc1\n"
    "buffer_load_dwordx4 v[56:59], v242, s[24:27], s58 offen offset:3072 sc1\n"
    "s_add_u32 s58, s58, 4096\n"
    "s_waitcnt vmcnt(0) lgkmcnt(0)\n"
    "v_mfma_f32_32x32x16_bf16 v[96:111], v[40:43], v[60:63], 0\n"
    "v_mfma_f32_32x32x16_bf16 v[96:111], v[44:47], v[64:67], v[96:111]\n"
    "s_nop 11\n"
    "v_max3_f32 v48, v48, v96, v97\n"
    "v_max3_f32 v48, v48, v98, v99\n"
    "v_max3_f32 v48, v48, v100, v101\n"
    "v_max3_f32 v48, v48, v102, v103\n"
    "v_max3_f32 v48, v48, v104, v105\n"
    "v_max3_f32 v48, v48, v106, v107\n"
    "v_max3_f32 v48, v48, v108, v109\n"
    "v_max3_f32 v48, v48, v110, v111\n"
    "s_cmp_lt_u32 s58, 0x80000\n"
    "s_cbranch_scc1 .LfFBa1\n"
    "v_mov_b32 v49, v48\n"
    "s_nop 1\n"
    "v_permlane32_swap_b32 v48, v49\n"
    "v_max_f32 v48, v48, v49\n"
    "s_mov_b32 s58, 0x40000\n"
    ".LfFBb1:\n"
    "buffer_load_dwordx4 v[40:43], v242, s[24:27], s58 offen sc1\n"
    "buffer_load_dwordx4 v[44:47], v242, s[24:27], s58 offen offset:1024 sc1\n"
    "buffer_load_dwordx4 v[52:55], v242, s[24:27], s58 offen offset:2048 sc1\n"
    "buffer_load_dwordx4 v[56:59], v242, s[24:27], s58 offen offset:3072 sc1\n"
    "s_add_u32 s58, s58, 4096\n"
    "s_waitcnt vmcnt(0) lgkmcnt(0)\n"
    "v_mfma_f32_32x32x16_bf16 v[96:111], v[40:43], v[60:63], 0\n"
    "v_mfma_f32_32x32x16_bf16 v[96:111], v[44:47], v[64:67], v[96:111]\n"
    "s_nop 11\n"
    "v_sub_f32 v96, v96, v48\n"
    "v_sub_f32 v97, v97, v48\n"
    "v_sub_f32 v98, v98, v48\n"
    "v_sub_f32 v99, v99, v48\n"
    "v_sub_f32 v100, v100, v48\n"
    "v_sub_f32 v101, v101, v48\n"
    "v_sub_f32 v102, v102, v48\n"
    "v_sub_f32 v103, v103, v48\n"
    "v_sub_f32 v104, v104, v48\n"
    "v_sub_f32 v105, v105, v48\n"
    "v_sub_f32 v106, v106, v48\n"
    "v_sub_f32 v107, v107, v48\n"
    "v_sub_f32 v108, v108, v48\n"
    "v_sub_f32 v109, v109, v48\n"
    "v_sub_f32 v110, v110, v48\n"
    "v_sub_f32 v111, v111, v48\n"
    "v_exp_f32 v96, v96\n"
    "v_exp_f32 v97, v97\n"
    "v_exp_f32 v98, v98\n"
    "v_exp_f32 v99, v99\n"
    "v_exp_f32 v100, v100\n"
    "v_exp_f32 v101, v101\n"
    "v_exp_f32 v102, v102\n"
    "v_exp_f32 v103, v103\n"
    "v_exp_f32 v104, v104\n"
    "v_exp_f32 v105, v105\n"
    "v_exp_f32 v106, v106\n"
    "v_exp_f32 v107, v107\n"
    "v_exp_f32 v108, v108\n"
    "v_exp_f32 v109, v109\n"
    "v_exp_f32 v110, v110\n"
    "v_exp_f32 v111, v111\n"
    "v_smfmac_f32_32x32x32_bf16 v[80:95], v[52:55], v[96:103], v240\n"
    "s_nop 0\n"
    "v_smfmac_f32_32x32x32_bf16 v[80:95], v[56:59], v[104:111], v240\n"
    "s_cmp_lt_u32 s58, 0x80000\n"
    "s_cbranch_scc1 .LfFBb1\n"
    "s_bitcmp1_b32 s40, 0\n"
    "s_cselect_b64 vcc, -1, 0\n"
    "s_nop 7\n"
    "v_cndmask_b32 v24, v80, v88, vcc\n"
    "v_cndmask_b32 v25, v81, v89, vcc\n"
    "v_cndmask_b32 v26, v82, v90, vcc\n"
    "v_cndmask_b32 v27, v83, v91, vcc\n"
    "v_cndmask_b32 v28, v84, v92, vcc\n"
    "v_cndmask_b32 v29, v85, v93, vcc\n"
    "v_cndmask_b32 v30, v86, v94, vcc\n"
    "v_cndmask_b32 v31, v87, v95, vcc\n"
    "v_mov_b32 v48, 0x7fc00000\n"
    "v_cmp_u_f32 vcc, v24, v24\n"
    "s_nop 1\n"
    "v_cndmask_b32 v24, v24, v48, vcc\n"
    "v_cmp_u_f32 vcc, v25, v25\n"
    "s_nop 1\n"
    "v_cndmask_b32 v25, v25, v48, vcc\n"
    "v_cmp_u_f32 vcc, v26, v26\n"
    "s_nop 1\n"
    "v_cndmask_b32 v26, v26, v48, vcc\n"
    "v_cmp_u_f32 vcc, v27, v27\n"
    "s_nop 1\n"
    "v_cndmask_b32 v27, v27, v48, vcc\n"
    "v_cmp_u_f32 vcc, v28, v28\n"
    "s_nop 1\n"
    "v_cndmask_b32 v28, v28, v48, vcc\n"
    "v_cmp_u_f32 vcc, v29, v29\n"
    "s_nop 1\n"
    "v_cndmask_b32 v29, v29, v48, vcc\n"
    "v_cmp_u_f32 vcc, v30, v30\n"
    "s_nop 1\n"
    "v_cndmask_b32 v30, v30, v48, vcc\n"
    "v_cmp_u_f32 vcc, v31, v31\n"
    "s_nop 1\n"
    "v_cndmask_b32 v31, v31, v48, vcc\n"
    "v_rcp_f32 v34, v31\n"
    "s_branch .LfFBback1\n"
    ".p2align 12\n"
    :
    : "{s[0:1]}"(__builtin_amdgcn_kernarg_segment_ptr()), "{s2}"(__builtin_amdgcn_workgroup_id_x()), "{v0}"(__builtin_amdgcn_workitem_id_x()),
      "i"((__attribute__((address_space(3))) char*)smem)
    :
      "v1","v2","v3","v4","v5","v6","v7","v8","v9","v10","v11","v12","v13","v14","v15","v16","v17","v18","v19","v20",
      "v21","v22","v23","v24","v25","v26","v27","v28","v29","v30","v31","v32","v33","v34","v35","v36","v37","v38","v39","v40",
      "v41","v42","v43","v44","v45","v46","v47","v48","v49","v50","v51","v52","v53","v54","v55","v56","v57","v58","v59","v60",
      "v61","v62","v63","v64","v65","v66","v67","v68","v69","v70","v71","v72","v73","v74","v75","v76","v77","v78","v79","v80",
      "v81","v82","v83","v84","v85","v86","v87","v88","v89","v90","v91","v92","v93","v94","v95","v96","v97","v98","v99","v100",
      "v101","v102","v103","v104","v105","v106","v107","v108","v109","v110","v111","v112","v113","v114","v115","v116","v117","v118","v119","v120",
      "v121","v122","v123","v124","v125","v126","v127","v128","v129","v130","v131","v132","v133","v134","v135","v136","v137","v138","v139","v140",
      "v141","v142","v143","v144","v145","v146","v147","v148","v149","v150","v151","v152","v153","v154","v155","v156","v157","v158","v159","v160",
      "v161","v162","v163","v164","v165","v166","v167","v168","v169","v170","v171","v172","v173","v174","v175","v176","v177","v178","v179","v180",
      "v181","v182","v183","v184","v185","v186","v187","v188","v189","v190","v191","v192","v193","v194","v195","v196","v197","v198","v199","v200",
      "v201","v202","v203","v204","v205","v206","v207","v208","v209","v210","v211","v212","v213","v214","v215","v216","v217","v218","v219","v220",
      "v221","v222","v223","v224","v225","v226","v227","v228","v229","v230","v231","v232","v233","v234","v235","v236","v237","v238","v239","v240",
      "v241","v242","v243","v244","v245","v246","v247","v248","v249","v250","v251","v252","v253","v254","v255","s3","s4","s5","s6","s7",
      "s8","s9","s10","s11","s12","s13","s14","s15","s16","s17","s18","s19","s20","s21","s22","s23","s24","s25","s26","s27",
      "s28","s29","s30","s31","s33","s34","s35","s36","s37","s38","s39","s40","s41","s42","s43","s44","s45","s46","s47","s48",
      "s49","s50","s51","s52","s53","s54","s55","s56","s57","s58","s59","s60","s61","s62","s63","s64","s65","s66","s67","s68",
      "s69","s70","s71","s72","s73","s74","s75","s76","s77","s78","s79","s80","s81","s82","s83","s84","s85","s86","s87","s88",
      "s89","s90","s91","s92","s93","s94","s95","s96","s97","s98","s99","vcc","scc","memory");
}

extern "C" void kernel_launch(void* const* d_in, const int* in_sizes, int n_in, void* d_out, int out_size, void* d_ws, size_t ws_size,
                              hipStream_t stream) {
  fused_mqa_s4d<<<256, 512, 0, stream>>>(d_in[0], d_in[1], d_in[2], d_in[3], d_in[4], d_in[5], d_in[6], d_in[7], d_in[8], d_in[9], d_in[10],
                                         d_in[11], d_out, d_ws);
}
```
